```python
import torch

def _softmax(x, axis=-1):
    return torch.softmax(x, dim=axis)
def _gelu(x):
    return torch.nn.functional.gelu(x, approximate="tanh")
def _silu(x):
    return torch.nn.functional.silu(x)
def _sigmoid(x):
    return torch.sigmoid(x)
def _layernorm(x, axis=-1, eps=1e-5):
    m = x.mean(dim=axis, keepdim=True)
    v = x.var(dim=axis, keepdim=True, unbiased=False)
    return (x - m) / torch.sqrt(v + eps)
def _scatter_add(tgt, idx, src, axis=0):
    return tgt.clone().index_add_(axis, idx, src)
def _segment_sum(src, seg, n):
    out = torch.zeros((n,) + src.shape[1:], dtype=src.dtype)
    return out.index_add_(0, seg, src)
def _pad(x, pw):
    flat = tuple(v for pair in reversed(pw) for v in pair)
    return torch.nn.functional.pad(x, flat)

kernel_name = 'parallel_sum__spectral_gfnet__glu_gated_reglu__prompt'

def setup_inputs():

    _w = (lambda a: (lambda t: t.to(torch.bfloat16) if t.is_floating_point() else t)(torch.as_tensor(a)))
    def _A_setup():
        B, S, D = 1, 2048, 512
        Df = D // 2 + 1
        return {
            "x": _w((lambda *s: torch.randn(*s, dtype=torch.bfloat16) * 0.05)(B, S, D)),
            "w_r": _w((lambda *s: torch.randn(*s, dtype=torch.bfloat16) * 0.4)(S, Df) * 0.02),
            "w_i": _w((lambda *s: torch.randn(*s, dtype=torch.bfloat16) * 0.4)(S, Df) * 0.02),
        }
    _A_in = _A_setup()

    def _B_setup():
        B, S, D, F = 1, 2048, 512, 1792
        return {
            "x": _w((lambda *s: torch.randn(*s, dtype=torch.bfloat16) * 0.05)(B, S, D)),
            "W_gate": _w((lambda *s: torch.randn(*s, dtype=torch.bfloat16) * 0.2)(D, F) * (2.0 / D) ** 0.5),
            "W_up": _w((lambda *s: torch.randn(*s, dtype=torch.bfloat16) * 0.2)(D, F) * (2.0 / D) ** 0.5),
            "W_down": _w((lambda *s: torch.randn(*s, dtype=torch.bfloat16) * 0.2)(F, D) * (1.0 / F) ** 0.5),
        }
    _B_in = _B_setup()

    return {
        "A_x": _A_in["x"],
        "A_w_r": _A_in["w_r"],
        "A_w_i": _A_in["w_i"],
        "B_x": _B_in["x"],
        "B_W_gate": _B_in["W_gate"],
        "B_W_up": _B_in["W_up"],
        "B_W_down": _B_in["W_down"],
    }

def reference(A_x, A_w_r, A_w_i, B_x, B_W_gate, B_W_up, B_W_down):
    def _A_ref(x, w_r, w_i):
        B, S, D = x.shape
        X = (lambda x, axes=(-2,-1): torch.fft.rfft2(x.float(), dim=axes))(x, axes=(-2, -1))
        W = w_r + 1j * w_i
        X = X * W
        y = (lambda x, s, axes=(-2,-1): torch.fft.irfft2(x, s=s, dim=axes).to(torch.bfloat16))(X, (S, D), axes=(-2, -1))
        return y

    def _B_ref(x, W_gate, W_up, W_down):
        gate = torch.einsum("bsd,df->bsf", x, W_gate)
        up = torch.einsum("bsd,df->bsf", x, W_up)
        gate = gate * (lambda x, dt: x.to(dt))(gate > 0, gate.dtype)
        h = gate * up
        return torch.einsum("bsf,fd->bsd", h, W_down)

    x = A_x
    xn = _layernorm(x, axis=-1)
    return x + _A_ref(xn, A_w_r, A_w_i) + _B_ref(xn, B_W_gate, B_W_up, B_W_down)
```

```cpp
#include <hip/hip_runtime.h>
#include <hip/hip_bf16.h>
#include <stdint.h>

#define LAS __attribute__((address_space(3)))
#define GAS __attribute__((address_space(1)))
#define DI __device__ __forceinline__
typedef unsigned u32;
typedef unsigned u32x2 __attribute__((ext_vector_type(2)));
typedef unsigned u32x4 __attribute__((ext_vector_type(4)));
typedef float f32x2 __attribute__((ext_vector_type(2)));
typedef float f32x4 __attribute__((ext_vector_type(4)));
typedef float f32x16 __attribute__((ext_vector_type(16)));
typedef short s16x4 __attribute__((ext_vector_type(4)));
typedef short s16x8 __attribute__((ext_vector_type(8)));
typedef _Float16 h16x2 __attribute__((ext_vector_type(2)));
typedef _Float16 h16x4 __attribute__((ext_vector_type(4)));
typedef _Float16 h16x8 __attribute__((ext_vector_type(8)));
typedef __bf16 b16x2 __attribute__((ext_vector_type(2)));
typedef __bf16 b16x8 __attribute__((ext_vector_type(8)));

constexpr int S_ = 2048, D_ = 512, F_ = 1792, DF_ = 257;
constexpr size_t WS_FLAGS = 0;
constexpr size_t WS_U = 65536;
constexpr size_t WS_V = WS_U + 2097152;
constexpr size_t WS_R = WS_V + 2097152;
constexpr size_t WS_ST = WS_R + 2097152;
constexpr size_t WS_CS = WS_ST + 16384;
constexpr size_t WS_H = WS_CS + 16384;
constexpr int LDS_BYTES = 163840;

DI u32 pk_f16(float a, float b) { h16x2 p = {(_Float16)a, (_Float16)b}; return __builtin_bit_cast(u32, p); }
DI u32 pk_bf16(float a, float b) { b16x2 p = {(__bf16)a, (__bf16)b}; return __builtin_bit_cast(u32, p); }
DI float bf_lo(u32 v) { return __builtin_bit_cast(float, v << 16); }
DI float bf_hi(u32 v) { return __builtin_bit_cast(float, v & 0xffff0000u); }
DI float rnd_bf(float a) { return bf_lo(pk_bf16(a, a)); }
DI float cosr(float rev) { return __builtin_amdgcn_cosf(rev); }
DI float sinr(float rev) { return __builtin_amdgcn_sinf(rev); }
DI f32x4 mfma16_f16(u32x2 a, u32x2 b, f32x4 c) { return __builtin_amdgcn_mfma_f32_16x16x16f16(__builtin_bit_cast(h16x4, a), __builtin_bit_cast(h16x4, b), c, 0, 0, 0); }
DI f32x4 mfma16_bf16(u32x2 a, u32x2 b, f32x4 c) { return __builtin_amdgcn_mfma_f32_16x16x16bf16_1k(__builtin_bit_cast(s16x4, a), __builtin_bit_cast(s16x4, b), c, 0, 0, 0); }
DI f32x4 mfma32_f16(u32x4 a, u32x4 b, f32x4 c) { return __builtin_amdgcn_mfma_f32_16x16x32_f16(__builtin_bit_cast(h16x8, a), __builtin_bit_cast(h16x8, b), c, 0, 0, 0); }
DI f32x4 mfma32_bf16(u32x4 a, u32x4 b, f32x4 c) { return __builtin_amdgcn_mfma_f32_16x16x32_bf16(__builtin_bit_cast(b16x8, a), __builtin_bit_cast(b16x8, b), c, 0, 0, 0); }
DI f32x16 mfma3232(u32x4 a, u32x4 b, f32x16 c) { return __builtin_amdgcn_mfma_f32_32x32x16_bf16(__builtin_bit_cast(b16x8, a), __builtin_bit_cast(b16x8, b), c, 0, 0, 0); }
DI u32x2 tr_read(LAS char* p) { return __builtin_bit_cast(u32x2, __builtin_amdgcn_ds_read_tr16_b64_v4i16((LAS s16x4*)p)); }

template <int CTRL> DI float dpp_add(float v) {
  int o = __builtin_amdgcn_update_dpp(0, __builtin_bit_cast(int, v), CTRL, 0xf, 0xf, true);
  return v + __builtin_bit_cast(float, o);
}
DI float wave_sum(float v) {
  v = dpp_add<0xB1>(v); v = dpp_add<0x4E>(v); v = dpp_add<0x141>(v); v = dpp_add<0x140>(v);
  u32 u = __builtin_bit_cast(u32, v);
  auto a = __builtin_amdgcn_permlane16_swap(u, u, false, false);
  v = __builtin_bit_cast(float, (u32)a[0]) + __builtin_bit_cast(float, (u32)a[1]);
  u = __builtin_bit_cast(u32, v);
  auto b = __builtin_amdgcn_permlane32_swap(u, u, false, false);
  return __builtin_bit_cast(float, (u32)b[0]) + __builtin_bit_cast(float, (u32)b[1]);
}

DI void phase_rowfwd(const char* __restrict__ x, char* ws, int i, LAS char* sm) {
  const int tid = threadIdx.x, lane = tid & 63, w = __builtin_amdgcn_readfirstlane(tid >> 6);
  const int p16 = lane & 15, t = lane >> 4;
  const int s = i + 256 * w;
  const u32x4 xv = *(const u32x4*)(x + (size_t)s * 1024 + lane * 16);
  u32x2 b1re, b1im;
  {
    float c[4], sn[4];
#pragma unroll
    for (int jj = 0; jj < 4; ++jj) { float rev = (float)(((4 * t + jj) * p16) & 15) * (1.0f / 16); c[jj] = 0.25f * cosr(rev); sn[jj] = -0.25f * sinr(rev); }
    b1re = u32x2{pk_f16(c[0], c[1]), pk_f16(c[2], c[3])};
    b1im = u32x2{pk_f16(sn[0], sn[1]), pk_f16(sn[2], sn[3])};
  }
  float twc[2][4], tws[2][4];
  u32x4 b2re[2], b2im[2];
#pragma unroll
  for (int hh = 0; hh < 2; ++hh)
#pragma unroll
    for (int r = 0; r < 4; ++r) {
      int n2 = 16 * hh + 4 * t + r;
      float rev = (float)(n2 * p16) * (1.0f / 512);
      twc[hh][r] = cosr(rev); tws[hh][r] = sinr(rev);
      float rev2 = (float)((n2 * p16) & 31) * (1.0f / 32);
      float c = 0.125f * cosr(rev2), sn = 0.125f * sinr(rev2);
      b2re[hh][r] = pk_f16(c, sn); b2im[hh][r] = pk_f16(-sn, c);
    }
  float v[8];
#pragma unroll
  for (int k = 0; k < 4; ++k) { v[2 * k] = bf_lo(xv[k]); v[2 * k + 1] = bf_hi(xv[k]); }
  float sum = ((v[0] + v[1]) + (v[2] + v[3])) + ((v[4] + v[5]) + (v[6] + v[7]));
  const float mean = wave_sum(sum) * (1.0f / 512);
  float sq = 0.f;
#pragma unroll
  for (int k = 0; k < 8; ++k) { float d = v[k] - mean; sq += d * d; }
  const float var = wave_sum(sq) * (1.0f / 512);
  const float mb = rnd_bf(mean);
  const float sd = rnd_bf(__builtin_sqrtf(rnd_bf(rnd_bf(var) + 1e-5f)));
  const float rs = 1.0f / sd;
  if (lane == 0) *(f32x2*)(ws + WS_ST + (size_t)s * 8) = f32x2{mb, rs};
  u32x4 xh;
#pragma unroll
  for (int k = 0; k < 4; ++k) {
    u32 dp = pk_bf16(v[2 * k] - mb, v[2 * k + 1] - mb);
    u32 np = pk_bf16(bf_lo(dp) * rs, bf_hi(dp) * rs);
    xh[k] = pk_f16(bf_lo(np), bf_hi(np));
  }
  LAS char* img = sm + w * 1024;
  *(LAS u32x4*)(img + ((lane ^ (((lane >> 4) & 1) << 1)) << 4)) = xh;
  __builtin_amdgcn_fence(__ATOMIC_RELEASE, "wavefront");
  __builtin_amdgcn_wave_barrier();
  f32x4 d1re[2], d1im[2];
  const f32x4 z4 = {0.f, 0.f, 0.f, 0.f};
#pragma unroll
  for (int hh = 0; hh < 2; ++hh) {
    int q = p16 >> 2, p = p16 & 3;
    u32x2 a = tr_read(img + (4 * t + q) * 64 + ((hh ^ (t & 1)) << 5) + p * 8);
    d1re[hh] = mfma16_f16(a, b1re, z4);
    d1im[hh] = mfma16_f16(a, b1im, z4);
  }
  float ny = ((d1re[0][0] - d1re[0][1]) + (d1re[0][2] - d1re[0][3])) + ((d1re[1][0] - d1re[1][1]) + (d1re[1][2] - d1re[1][3]));
  f32x4 d2re = z4, d2im = z4;
#pragma unroll
  for (int hh = 0; hh < 2; ++hh) {
    u32x4 a;
#pragma unroll
    for (int r = 0; r < 4; ++r) {
      float re = d1re[hh][r] * twc[hh][r] + d1im[hh][r] * tws[hh][r];
      float im = d1im[hh][r] * twc[hh][r] - d1re[hh][r] * tws[hh][r];
      a[r] = pk_f16(re, im);
    }
    d2re = mfma32_f16(a, b2re[hh], d2re);
    d2im = mfma32_f16(a, b2im[hh], d2im);
  }
  {
    int nb = __builtin_bit_cast(int, ny);
    float n1_ = __builtin_bit_cast(float, __builtin_amdgcn_readlane(nb, 16));
    float n2_ = __builtin_bit_cast(float, __builtin_amdgcn_readlane(nb, 32));
    float n3_ = __builtin_bit_cast(float, __builtin_amdgcn_readlane(nb, 48));
    float tot = 0.125f * ((ny + n1_) + (n2_ + n3_));
    if (lane == 0) d2im[0] = tot;
  }
  LAS char* ex = sm + 8192;
  {
    u32x4 o;
#pragma unroll
    for (int r = 0; r < 4; ++r) o[r] = pk_f16(d2re[r], d2im[r]);
    int e = 4 * p16 + t; e ^= (e >> 3) & 3;
    *(LAS u32x4*)(ex + w * 1024 + e * 16) = o;
  }
  __syncthreads();
  if (w < 4) {
    const int jp = p16 >> 1, po = p16 & 1;
    float c0, s0, c1, s1;
    { float rev = (float)((2 * t * jp) & 7) * 0.125f; c0 = 0.5f * cosr(rev); s0 = 0.5f * sinr(rev); }
    { float rev = (float)(((2 * t + 1) * jp) & 7) * 0.125f; c1 = 0.5f * cosr(rev); s1 = 0.5f * sinr(rev); }
    u32x2 a8 = po ? u32x2{pk_f16(-s0, c0), pk_f16(-s1, c1)} : u32x2{pk_f16(c0, s0), pk_f16(c1, s1)};
    int e = 16 * w + p16; e ^= (e >> 3) & 3;
    u32x4 r0 = *(LAS u32x4*)(ex + (2 * t) * 1024 + e * 16);
    u32x4 r1 = *(LAS u32x4*)(ex + (2 * t + 1) * 1024 + e * 16);
    u32x4 o0, o1;
#pragma unroll
    for (int u = 0; u < 4; ++u) {
      f32x4 d = mfma16_f16(a8, u32x2{r0[u], r1[u]}, z4);
      if (u == 0 && w == 0) {
        float e0 = po ? -s0 : c0, e1 = po ? -s1 : c1;
        u32x2 as = (jp & 3) == 0 ? a8 : (jp < 4 ? u32x2{pk_f16(e0, 0.f), pk_f16(e1, 0.f)} : u32x2{pk_f16(0.f, e0), pk_f16(0.f, e1)});
        f32x4 ds = mfma16_f16(as, u32x2{r0[0], r1[0]}, z4);
        if (p16 == 0) d = ds;
      }
      o0[u] = pk_f16(d[0], d[1]); o1[u] = pk_f16(d[2], d[3]);
    }
    char* ub = ws + WS_U + (size_t)i * 8192 + (64 * w + 4 * p16) * 4;
    *(u32x4*)(ub + (2 * t) * 1024) = o0;
    *(u32x4*)(ub + (2 * t + 1) * 1024) = o1;
  }
}

constexpr int CB_STRIDE = 1024 + 8;
DI void phase_col(const unsigned short* __restrict__ wr, const unsigned short* __restrict__ wi, char* ws, int c, LAS char* sm) {
  const int tid = threadIdx.x, lane = tid & 63, w = __builtin_amdgcn_readfirstlane(tid >> 6);
  const int p16 = lane & 15, t = lane >> 4;
  const int jp = c & 7, g = c >> 3;
  LAS char* cb = sm;
  LAS char* fb = sm + 8 * 1040;
  {
    int i = tid >> 1, hf = tid & 1;
    u32x4 uv = *(const u32x4*)(ws + WS_U + (size_t)i * 8192 + jp * 1024 + g * 32 + hf * 16);
    int pos = (i & 192) + 4 * (i & 15) + ((i >> 4) & 3);
#pragma unroll
    for (int k = 0; k < 4; ++k) *(LAS u32*)(cb + (4 * hf + k) * 1040 + pos * 4) = uv[k];
    int q = tid & 255, arr = tid >> 8;
    const unsigned short* wsrc = arr ? wi : wr;
    int e0 = (8 * q + jp) * DF_ + 8 * g;
    const u32* pa = (const u32*)(wsrc + (e0 & ~1));
    u32 d0 = pa[0], d1 = pa[1], d2 = pa[2], d3 = pa[3], d4 = pa[4];
    u32 sh = (e0 & 1) * 16;
    u32 f[4] = {__builtin_amdgcn_alignbit(d1, d0, sh), __builtin_amdgcn_alignbit(d2, d1, sh), __builtin_amdgcn_alignbit(d3, d2, sh), __builtin_amdgcn_alignbit(d4, d3, sh)};
    int posq = (q & 192) + 4 * (q & 15) + ((q >> 4) & 3);
#pragma unroll
    for (int k = 0; k < 4; ++k) {
      *(LAS unsigned short*)(fb + (2 * k) * 1040 + posq * 4 + arr * 2) = (unsigned short)(f[k] & 0xffff);
      *(LAS unsigned short*)(fb + (2 * k + 1) * 1040 + posq * 4 + arr * 2) = (unsigned short)(f[k] >> 16);
    }
  }
  u32x4 bc1re, bc1im, ac2re, ac2im, bc3re, bc3im, ac4re, ac4im;
  float t1c[4], t1s[4], t2c[4], t2s[4];
  const float FS = (2.0f / (2048.0f * 512.0f)) * (4.0f * 8.0f * 2.0f * 4.0f * 4.0f);
#pragma unroll
  for (int r = 0; r < 4; ++r) {
    const int y = 4 * t + r;
    { float rev = (float)((y * (8 * p16 + jp)) & 127) * (1.0f / 128); float cc = 0.25f * cosr(rev), sn = 0.25f * sinr(rev);
      bc1re[r] = pk_f16(cc, sn); bc1im[r] = pk_f16(-sn, cc); }
    { float rev = (float)((y * p16) & 15) * (1.0f / 16); float cc = cosr(rev), sn = sinr(rev);
      ac2re[r] = pk_f16(0.25f * cc, 0.25f * sn); ac2im[r] = pk_f16(-0.25f * sn, 0.25f * cc);
      bc3re[r] = pk_bf16(cc, -sn); bc3im[r] = pk_bf16(sn, cc); }
    { float rev = (float)((p16 * (8 * y + jp)) & 127) * (1.0f / 128); float cc = cosr(rev), sn = sinr(rev);
      ac4re[r] = pk_bf16(cc, -sn); ac4im[r] = pk_bf16(sn, cc); }
    { float rev = (float)(y * (8 * p16 + jp)) * (1.0f / 2048); t1c[r] = cosr(rev); t1s[r] = sinr(rev); }
    { float rev = (float)(p16 * (8 * y + jp)) * (1.0f / 2048); t2c[r] = FS * cosr(rev); t2s[r] = FS * sinr(rev); }
  }
  __syncthreads();
  const u32x4 uin = *(LAS u32x4*)(cb + w * 1040 + lane * 16);
  const u32x4 fin = *(LAS u32x4*)(fb + w * 1040 + lane * 16);
  const bool special = (g == 0 && w == 0);
  const int npass = (special && (jp & 3) == 0) ? 2 : 1;
  const f32x4 z4 = {0.f, 0.f, 0.f, 0.f};
  f32x4 ore = z4, oim = z4;
  for (int pass = 0; pass < npass; ++pass) {
    u32x4 a = uin;
    float fr[4], fi[4];
#pragma unroll
    for (int r = 0; r < 4; ++r) { fr[r] = bf_lo(fin[r]); fi[r] = bf_hi(fin[r]); }
    if (special) {
      const int col = (npass == 2) ? (pass ? 256 : 0) : (jp < 4 ? 0 : 256);
#pragma unroll
      for (int r = 0; r < 4; ++r) {
        int ks = 8 * (p16 + 64 * t + 16 * r) + jp, km = (2048 - ks) & 2047;
        float ar = bf_lo(wr[ks * DF_ + col]), ai = bf_lo(wi[ks * DF_ + col]);
        float br = bf_lo(wr[km * DF_ + col]), bi = bf_lo(wi[km * DF_ + col]);
        fr[r] = 0.5f * (ar + br); fi[r] = 0.5f * (ai - bi);
        if (npass == 2) a[r] = pass ? (uin[r] >> 16) : (uin[r] & 0xffffu);
      }
    }
    f32x4 dre = mfma32_f16(a, bc1re, z4), dim = mfma32_f16(a, bc1im, z4);
#pragma unroll
    for (int r = 0; r < 4; ++r) a[r] = pk_f16(dre[r] * t1c[r] + dim[r] * t1s[r], dim[r] * t1c[r] - dre[r] * t1s[r]);
    dre = mfma32_f16(ac2re, a, z4); dim = mfma32_f16(ac2im, a, z4);
#pragma unroll
    for (int r = 0; r < 4; ++r) a[r] = pk_bf16(dre[r] * fr[r] - dim[r] * fi[r], dre[r] * fi[r] + dim[r] * fr[r]);
    dre = mfma32_bf16(a, bc3re, z4); dim = mfma32_bf16(a, bc3im, z4);
#pragma unroll
    for (int r = 0; r < 4; ++r) a[r] = pk_bf16(dre[r] * t2c[r] - dim[r] * t2s[r], dre[r] * t2s[r] + dim[r] * t2c[r]);
    dre = mfma32_bf16(ac4re, a, z4); dim = mfma32_bf16(ac4im, a, z4);
    if (npass == 2) { if (pass == 0) ore = dre; else oim = dre; }
    else { ore = dre; oim = dim; }
  }
  {
    u32x4 o;
#pragma unroll
    for (int r = 0; r < 4; ++r) o[r] = pk_bf16(ore[r], oim[r]);
    *(LAS u32x4*)(cb + w * 1040 + lane * 16) = o;
  }
  __syncthreads();
  {
    int ps = tid >> 1, hf = tid & 1;
    int i = (ps >> 5) + 8 * (ps & 31);
    int pos = (i & 192) + 4 * (i & 15) + ((i >> 4) & 3);
    u32x4 o;
#pragma unroll
    for (int k = 0; k < 4; ++k) o[k] = *(LAS u32*)(cb + (4 * hf + k) * 1040 + pos * 4);
    *(u32x4*)(ws + WS_V + (size_t)c * 8192 + tid * 16) = o;
  }
}

DI void phase_rowinv(const char* __restrict__ x, char* ws, int i, LAS char* sm) {
  const int tid = threadIdx.x, lane = tid & 63, w = __builtin_amdgcn_readfirstlane(tid >> 6);
  const int p16 = lane & 15, t = lane >> 4;
  const f32x4 z4 = {0.f, 0.f, 0.f, 0.f};
  const int s = i + 256 * w;
  const u32x4 xv = *(const u32x4*)(x + (size_t)s * 1024 + (4 * p16 + t) * 16);
  if (w < 4) {
    const int g = 8 * w + (p16 >> 1);
    const size_t off = WS_V + (size_t)((i & 7) * 32 + (i >> 3)) * 32 + (p16 & 1) * 16;
    u32x4 r0 = *(const u32x4*)(ws + off + (size_t)(g * 8 + 2 * t) * 8192);
    u32x4 r1 = *(const u32x4*)(ws + off + (size_t)(g * 8 + 2 * t + 1) * 8192);
    const int j = p16 >> 1, po = p16 & 1;
    float c0, s0, c1, s1;
    { float rev = (float)((2 * t * j) & 7) * 0.125f; c0 = cosr(rev); s0 = sinr(rev); }
    { float rev = (float)(((2 * t + 1) * j) & 7) * 0.125f; c1 = cosr(rev); s1 = sinr(rev); }
    u32x2 a8 = po ? u32x2{pk_bf16(s0, c0), pk_bf16(s1, c1)} : u32x2{pk_bf16(c0, -s0), pk_bf16(c1, -s1)};
    u32x4 o0, o1;
#pragma unroll
    for (int u = 0; u < 4; ++u) {
      f32x4 d = mfma16_bf16(a8, u32x2{r0[u], r1[u]}, z4);
      if (u == 0 && w == 0) {
        u32 e0, e1;
        const bool on = (po == 0) ? (t < 2) : (t >= 2);
        if ((t & 1) == 0) e0 = po ? pk_bf16(0.f, 0.5f * c0) : pk_bf16(0.5f * c0, 0.f);
        else e0 = on ? pk_bf16(c0, -s0) : 0u;
        e1 = on ? pk_bf16(c1, -s1) : 0u;
        f32x4 ds = mfma16_bf16(u32x2{e0, e1}, u32x2{r0[0], r1[0]}, z4);
        if (p16 == 0) d = ds;
      }
      o0[u] = pk_bf16(d[0], d[1]); o1[u] = pk_bf16(d[2], d[3]);
    }
    *(LAS u32x4*)(sm + (2 * t) * 1024 + (16 * w + p16) * 16) = o0;
    *(LAS u32x4*)(sm + (2 * t + 1) * 1024 + (16 * w + p16) * 16) = o1;
  }
  u32x4 bi1re[2], bi1im[2], bi2;
  float twc[2][4], tws[2][4];
#pragma unroll
  for (int hh = 0; hh < 2; ++hh) {
    const int n2 = 8 * (p16 >> 2) + 4 * hh + (p16 & 3);
#pragma unroll
    for (int m = 0; m < 4; ++m) {
      float rev = (float)((n2 * (t + 4 * m)) & 31) * (1.0f / 32); float cc = cosr(rev), sn = sinr(rev);
      bi1re[hh][m] = pk_bf16(cc, -sn); bi1im[hh][m] = pk_bf16(sn, cc);
      float rev2 = (float)(n2 * (4 * t + m)) * (1.0f / 512);
      twc[hh][m] = cosr(rev2); tws[hh][m] = sinr(rev2);
    }
  }
#pragma unroll
  for (int r = 0; r < 4; ++r) { float rev = (float)((p16 * (4 * t + r)) & 15) * (1.0f / 16); bi2[r] = pk_bf16(cosr(rev), -sinr(rev)); }
  __syncthreads();
  u32x4 a;
#pragma unroll
  for (int m = 0; m < 4; ++m) a[m] = *(LAS u32*)(sm + w * 1024 + (lane + 64 * m) * 4);
  const float nyq = bf_hi(*(LAS u32*)(sm + w * 1024));
  float o[8];
#pragma unroll
  for (int hh = 0; hh < 2; ++hh) {
    f32x4 gre = mfma32_bf16(a, bi1re[hh], z4), gim = mfma32_bf16(a, bi1im[hh], z4);
    u32x4 b;
#pragma unroll
    for (int r = 0; r < 4; ++r) b[r] = pk_bf16(gre[r] * twc[hh][r] - gim[r] * tws[hh][r], gre[r] * tws[hh][r] + gim[r] * twc[hh][r]);
    f32x4 d = mfma32_bf16(b, bi2, f32x4{nyq, -nyq, nyq, -nyq});
#pragma unroll
    for (int r = 0; r < 4; ++r) o[4 * hh + r] = d[r];
  }
  u32x4 ov;
#pragma unroll
  for (int k = 0; k < 4; ++k) ov[k] = pk_bf16(bf_lo(xv[k]) + o[2 * k], bf_hi(xv[k]) + o[2 * k + 1]);
  *(u32x4*)(ws + WS_R + (size_t)s * 1024 + (4 * p16 + t) * 16) = ov;
}

DI void phase_colsum(const char* __restrict__ wg, const char* __restrict__ wu, char* ws, int e, LAS char* sm) {
  const int tid = threadIdx.x;
  const char* wsrc = e < 14 ? wg : wu;
  const int ct = e < 14 ? e : e - 14;
  const int ch = tid & 15, r0 = tid >> 4;
  float acc[8] = {0, 0, 0, 0, 0, 0, 0, 0};
#pragma unroll
  for (int k = 0; k < 16; ++k) {
    u32x4 v = *(const u32x4*)(wsrc + (size_t)(r0 + 32 * k) * 3584 + ct * 256 + ch * 16);
#pragma unroll
    for (int q = 0; q < 4; ++q) { acc[2 * q] += bf_lo(v[q]); acc[2 * q + 1] += bf_hi(v[q]); }
  }
  LAS float* red = (LAS float*)sm;
#pragma unroll
  for (int q = 0; q < 8; ++q) red[r0 * 128 + ch * 8 + q] = acc[q];
  __syncthreads();
  if (tid < 128) {
    float sacc = 0.f;
#pragma unroll
    for (int k = 0; k < 32; ++k) sacc += red[k * 128 + tid];
    *(float*)(ws + WS_CS + (size_t)(e * 128 + tid) * 4) = sacc;
  }
}

constexpr int G1_STAGE = 49152;
DI void g1_issue(const char* xsrc, const char* wsrc, LAS char* buf, int w, int st) {
#pragma unroll
  for (int k = 0; k < 2; ++k)
    __builtin_amdgcn_global_load_lds((const GAS void*)(xsrc + (size_t)k * 65536 + st * 128), (LAS void*)(buf + (k * 8 + w) * 1024), 16, 0, 0);
#pragma unroll
  for (int k = 0; k < 4; ++k)
    __builtin_amdgcn_global_load_lds((const GAS void*)(wsrc + (size_t)(st * 64 + k * 16) * 3584), (LAS void*)(buf + 16384 + (k * 8 + w) * 1024), 16, 0, 0);
}
DI void phase_gemm1(const char* __restrict__ x, const char* __restrict__ wg, const char* __restrict__ wu, char* ws, int mi, int fj, LAS char* sm) {
  const int tid = threadIdx.x, lane = tid & 63, w = __builtin_amdgcn_readfirstlane(tid >> 6);
  const char* xsrc;
  { int row = w * 8 + (lane >> 3), pos = lane & 7; xsrc = x + (size_t)(mi * 128 + row) * 1024 + ((pos ^ ((row >> 1) & 7)) << 4); }
  const char* wsrc;
  { int k = w * 2 + (lane >> 5), arr = (lane >> 4) & 1, pos = lane & 15;
    wsrc = (arr ? wu : wg) + (size_t)k * 3584 + fj * 256 + ((pos ^ (2 * ((k & 3) | (arr << 2)))) << 4); }
  g1_issue(xsrc, wsrc, sm, w, 0);
  g1_issue(xsrc, wsrc, sm + G1_STAGE, w, 1);
  const int wr = w >> 1, wt = w & 1;
  const int h = lane >> 5;
  int xoff[2], woff[2];
#pragma unroll
  for (int n = 0; n < 2; ++n) { int tok = wt * 64 + n * 32 + (lane & 31); xoff[n] = tok * 128 + ((h ^ ((tok >> 1) & 7)) << 4); }
  {
    int arr = (lane >> 4) & 1, q = (lane & 15) >> 2, p = lane & 3;
#pragma unroll
    for (int m = 0; m < 2; ++m) {
      int ft = wr * 2 + m;
      woff[m] = 16384 + (8 * h + q) * 512 + arr * 256 + ((ft ^ (q | (arr << 2))) << 5) + (p & 1) * 16 + (p >> 1) * 8;
    }
  }
  f32x16 acc[2][2];
#pragma unroll
  for (int m = 0; m < 2; ++m)
#pragma unroll
    for (int n = 0; n < 2; ++n)
#pragma unroll
      for (int k = 0; k < 16; ++k) acc[m][n][k] = 0.f;
#pragma unroll
  for (int st = 0; st < 8; ++st) {
    if (st < 7) asm volatile("s_waitcnt vmcnt(6)" ::: "memory"); else asm volatile("s_waitcnt vmcnt(0)" ::: "memory");
    __builtin_amdgcn_s_barrier();
    if (st + 2 < 8) g1_issue(xsrc, wsrc, sm + ((st + 2) % 3) * G1_STAGE, w, st + 2);
    LAS char* buf = sm + (st % 3) * G1_STAGE;
#pragma unroll
    for (int ks = 0; ks < 4; ++ks) {
      u32x4 bx[2], aw[2];
#pragma unroll
      for (int n = 0; n < 2; ++n) bx[n] = *(LAS u32x4*)(buf + (xoff[n] ^ (ks << 5)));
#pragma unroll
      for (int m = 0; m < 2; ++m) {
        u32x2 lo = tr_read(buf + woff[m] + ks * 8192), hi = tr_read(buf + woff[m] + ks * 8192 + 2048);
        aw[m] = u32x4{lo[0], lo[1], hi[0], hi[1]};
      }
#pragma unroll
      for (int m = 0; m < 2; ++m)
#pragma unroll
        for (int n = 0; n < 2; ++n) acc[m][n] = mfma3232(aw[m], bx[n], acc[m][n]);
    }
    asm volatile("s_waitcnt lgkmcnt(0)" ::: "memory");
  }
  const float* cs = (const float*)(ws + WS_CS);
#pragma unroll
  for (int n = 0; n < 2; ++n) {
    const int tok = mi * 128 + wt * 64 + n * 32 + (lane & 31);
    const f32x2 stt = *(const f32x2*)(ws + WS_ST + (size_t)tok * 8);
    const float nm = -stt[0], rs2 = stt[1] * stt[1];
#pragma unroll
    for (int m = 0; m < 2; ++m) {
      const int f0 = fj * 128 + (wr * 2 + m) * 16 + 8 * h;
      f32x4 cg0 = *(const f32x4*)(cs + f0), cg1 = *(const f32x4*)(cs + f0 + 4);
      f32x4 cu0 = *(const f32x4*)(cs + F_ + f0), cu1 = *(const f32x4*)(cs + F_ + f0 + 4);
      float hv[8];
#pragma unroll
      for (int k = 0; k < 8; ++k) {
        float cg = k < 4 ? cg0[k & 3] : cg1[k & 3], cu = k < 4 ? cu0[k & 3] : cu1[k & 3];
        float gg = __builtin_fmaf(nm, cg, acc[m][n][k]), uu = __builtin_fmaf(nm, cu, acc[m][n][8 + k]);
        hv[k] = __builtin_fmaxf(gg, 0.f) * (uu * rs2);
      }
      u32x4 o = {pk_bf16(hv[0], hv[1]), pk_bf16(hv[2], hv[3]), pk_bf16(hv[4], hv[5]), pk_bf16(hv[6], hv[7])};
      const int fo = f0 >> 3;
      *(u32x4*)(ws + WS_H + ((size_t)(tok >> 4) * 56 + (fo >> 2)) * 1024 + (fo & 3) * 256 + (tok & 15) * 16) = o;
    }
  }
}

DI void phase_gemm2(const char* __restrict__ wd, char* ws, char* out, int rt, int ct, LAS char* sm) {
  const int tid = threadIdx.x, lane = tid & 63, w = __builtin_amdgcn_readfirstlane(tid >> 6);
  const int p16 = lane & 15, t = lane >> 4;
  u32x4 wf[7][4];
  {
    const char* src = wd + (size_t)(w * 224 + 8 * t) * 1024 + ct * 128 + p16 * 8;
#pragma unroll
    for (int ks = 0; ks < 7; ++ks) {
      u32x2 rw[8];
#pragma unroll
      for (int j = 0; j < 8; ++j) rw[j] = *(const u32x2*)(src + (size_t)(ks * 32 + j) * 1024);
#pragma unroll
      for (int kp = 0; kp < 4; ++kp) {
        wf[ks][0][kp] = __builtin_amdgcn_perm(rw[2 * kp + 1][0], rw[2 * kp][0], 0x05040100u);
        wf[ks][1][kp] = __builtin_amdgcn_perm(rw[2 * kp + 1][0], rw[2 * kp][0], 0x07060302u);
        wf[ks][2][kp] = __builtin_amdgcn_perm(rw[2 * kp + 1][1], rw[2 * kp][1], 0x05040100u);
        wf[ks][3][kp] = __builtin_amdgcn_perm(rw[2 * kp + 1][1], rw[2 * kp][1], 0x07060302u);
      }
    }
  }
  f32x4 acc[4][4];
#pragma unroll
  for (int a = 0; a < 4; ++a)
#pragma unroll
    for (int b = 0; b < 4; ++b) acc[a][b] = f32x4{0.f, 0.f, 0.f, 0.f};
  const char* hs = ws + WS_H + ((size_t)(rt * 4) * 56 + w * 7) * 1024 + lane * 16;
#pragma unroll
  for (int ks = 0; ks < 7; ++ks) {
    u32x4 hf[4];
#pragma unroll
    for (int tt = 0; tt < 4; ++tt) hf[tt] = *(const u32x4*)(hs + (size_t)(tt * 56 + ks) * 1024);
#pragma unroll
    for (int tt = 0; tt < 4; ++tt)
#pragma unroll
      for (int u = 0; u < 4; ++u) acc[tt][u] = mfma32_bf16(wf[ks][u], hf[tt], acc[tt][u]);
  }
#pragma unroll
  for (int tt = 0; tt < 4; ++tt)
#pragma unroll
    for (int u = 0; u < 4; ++u) *(LAS f32x4*)(sm + w * 16384 + (tt * 4 + u) * 1024 + lane * 16) = acc[tt][u];
  __syncthreads();
  {
    const int tt = w >> 1, hf = w & 1;
    f32x2 sacc[4];
#pragma unroll
    for (int u = 0; u < 4; ++u) {
      sacc[u] = f32x2{0.f, 0.f};
#pragma unroll
      for (int pw = 0; pw < 8; ++pw) sacc[u] += *(LAS f32x2*)(sm + pw * 16384 + (tt * 4 + u) * 1024 + lane * 16 + hf * 8);
    }
    const size_t off = (size_t)(rt * 64 + tt * 16 + p16) * 1024 + ct * 128 + t * 32 + hf * 16;
    const u32x4 rv = *(const u32x4*)(ws + WS_R + off);
    u32x4 o;
    o[0] = pk_bf16(bf_lo(rv[0]) + sacc[0][0], bf_hi(rv[0]) + sacc[1][0]);
    o[1] = pk_bf16(bf_lo(rv[1]) + sacc[2][0], bf_hi(rv[1]) + sacc[3][0]);
    o[2] = pk_bf16(bf_lo(rv[2]) + sacc[0][1], bf_hi(rv[2]) + sacc[1][1]);
    o[3] = pk_bf16(bf_lo(rv[3]) + sacc[2][1], bf_hi(rv[3]) + sacc[3][1]);
    *(u32x4*)(out + off) = o;
  }
}

__global__ __launch_bounds__(512, 2) void k_p0(const char* x, const char* wg, const char* wu, char* ws) {
  extern __shared__ __attribute__((aligned(16))) char smem[];
  LAS char* sm = (LAS char*)smem;
  phase_rowfwd(x, ws, blockIdx.x, sm);
  if (blockIdx.x < 28) { __syncthreads(); phase_colsum(wg, wu, ws, blockIdx.x, sm); }
}
__global__ __launch_bounds__(512, 2) void k_g1(const char* x, const unsigned short* wr, const unsigned short* wi, const char* wg, const char* wu, char* ws) {
  extern __shared__ __attribute__((aligned(16))) char smem[];
  LAS char* sm = (LAS char*)smem;
  const int b = blockIdx.x;
  if (b >= 32) { int q = b - 32; int xc = q & 7, r = q >> 3; phase_gemm1(x, wg, wu, ws, (xc >> 1) * 4 + (r & 3), (xc & 1) * 7 + (r >> 2), sm); __syncthreads(); }
  phase_col(wr, wi, ws, b, sm);
}
__global__ __launch_bounds__(512, 2) void k_t4(const char* x, char* ws) {
  extern __shared__ __attribute__((aligned(16))) char smem[];
  phase_rowinv(x, ws, blockIdx.x, (LAS char*)smem);
}
__global__ __launch_bounds__(512, 2) void k_g2(const char* wd, char* ws, char* out) {
  extern __shared__ __attribute__((aligned(16))) char smem[];
  const int b = blockIdx.x;
  phase_gemm2(wd, ws, out, (b & 7) * 4 + ((b >> 3) & 3), b >> 5, (LAS char*)smem);
}

extern "C" void kernel_launch(void* const* d_in, const int* in_sizes, int n_in, void* d_out, int out_size, void* d_ws, size_t ws_size,
                              hipStream_t stream) {
  const char* x = (const char*)d_in[0];
  const unsigned short* wr = (const unsigned short*)d_in[1];
  const unsigned short* wi = (const unsigned short*)d_in[2];
  const char* wg = (const char*)d_in[4];
  const char* wu = (const char*)d_in[5];
  const char* wd = (const char*)d_in[6];
  char* ws = (char*)d_ws;
  hipFuncSetAttribute((const void*)k_g1, hipFuncAttributeMaxDynamicSharedMemorySize, LDS_BYTES);
  hipFuncSetAttribute((const void*)k_g2, hipFuncAttributeMaxDynamicSharedMemorySize, LDS_BYTES);
  k_p0<<<256, 512, 16384, stream>>>(x, wg, wu, ws);
  k_g1<<<256, 512, 3 * G1_STAGE, stream>>>(x, wr, wi, wg, wu, ws);
  k_t4<<<256, 512, 8192, stream>>>(x, ws);
  k_g2<<<256, 512, 131072, stream>>>(wd, ws, (char*)d_out);
}
```

```cpp
#include <hip/hip_runtime.h>
#include <stdint.h>

#define LAS __attribute__((address_space(3)))
#define GAS __attribute__((address_space(1)))
#define DI __device__ __forceinline__
typedef unsigned u32;
typedef unsigned u32x2 __attribute__((ext_vector_type(2)));
typedef unsigned u32x4 __attribute__((ext_vector_type(4)));
typedef float f32x2 __attribute__((ext_vector_type(2)));
typedef float f32x4 __attribute__((ext_vector_type(4)));
typedef float f32x16 __attribute__((ext_vector_type(16)));
typedef short s16x4 __attribute__((ext_vector_type(4)));
typedef _Float16 h16x2 __attribute__((ext_vector_type(2)));
typedef _Float16 h16x4 __attribute__((ext_vector_type(4)));
typedef _Float16 h16x8 __attribute__((ext_vector_type(8)));
typedef __bf16 b16x2 __attribute__((ext_vector_type(2)));
typedef __bf16 b16x8 __attribute__((ext_vector_type(8)));
typedef __amdgpu_buffer_rsrc_t rsrc_t;

constexpr int F_ = 1792, DF_ = 257;
constexpr u32 WS_EPOCH = 0;
constexpr u32 WS_F2 = 4096;
constexpr u32 WS_ST = 8192;
constexpr u32 WS_CS = WS_ST + 16384;
constexpr u32 WS_U = 65536;
constexpr u32 WS_V = WS_U + 4194304;
constexpr u32 WS_R = WS_V + 4194304;
constexpr u32 WS_H = WS_R + 4194304;
constexpr u32 WS_END = WS_H + 7340032;
constexpr int LDS_BYTES = 163840;
constexpr int G1_STAGE = 49152;
constexpr int FS = 3 * G1_STAGE;
constexpr int SPIN_CAP = 1 << 17;
constexpr size_t WS_DBG = 64 << 20;
#define STAMP(k) do {} while (0)
#define NST 0

DI u32 pk_f16(float a, float b) { h16x2 p = {(_Float16)a, (_Float16)b}; return __builtin_bit_cast(u32, p); }
DI u32 pk_bf16(float a, float b) { b16x2 p = {(__bf16)a, (__bf16)b}; return __builtin_bit_cast(u32, p); }
DI float bf_lo(u32 v) { return __builtin_bit_cast(float, v << 16); }
DI float bf_hi(u32 v) { return __builtin_bit_cast(float, v & 0xffff0000u); }
DI float rnd_bf(float a) { return bf_lo(pk_bf16(a, a)); }
DI float cosr(float rev) { return __builtin_amdgcn_cosf(rev); }
DI float sinr(float rev) { return __builtin_amdgcn_sinf(rev); }
DI f32x4 mfma16_f16(u32x2 a, u32x2 b, f32x4 c) { return __builtin_amdgcn_mfma_f32_16x16x16f16(__builtin_bit_cast(h16x4, a), __builtin_bit_cast(h16x4, b), c, 0, 0, 0); }
DI f32x4 mfma16_bf16(u32x2 a, u32x2 b, f32x4 c) { return __builtin_amdgcn_mfma_f32_16x16x16bf16_1k(__builtin_bit_cast(s16x4, a), __builtin_bit_cast(s16x4, b), c, 0, 0, 0); }
DI f32x4 mfma32_f16(u32x4 a, u32x4 b, f32x4 c) { return __builtin_amdgcn_mfma_f32_16x16x32_f16(__builtin_bit_cast(h16x8, a), __builtin_bit_cast(h16x8, b), c, 0, 0, 0); }
DI f32x4 mfma32_bf16(u32x4 a, u32x4 b, f32x4 c) { return __builtin_amdgcn_mfma_f32_16x16x32_bf16(__builtin_bit_cast(b16x8, a), __builtin_bit_cast(b16x8, b), c, 0, 0, 0); }
DI f32x16 mfma3232(u32x4 a, u32x4 b, f32x16 c) { return __builtin_amdgcn_mfma_f32_32x32x16_bf16(__builtin_bit_cast(b16x8, a), __builtin_bit_cast(b16x8, b), c, 0, 0, 0); }
DI u32x2 tr_read(LAS char* p) { return __builtin_bit_cast(u32x2, __builtin_amdgcn_ds_read_tr16_b64_v4i16((LAS s16x4*)p)); }
#define SB() __builtin_amdgcn_sched_barrier(0)
#define BAR() asm volatile("s_waitcnt lgkmcnt(0)\n\ts_barrier" ::: "memory")
#define WAITV(n) asm volatile("s_waitcnt vmcnt(%0)" :: "i"(n) : "memory")
DI u32x4 mk_srd(const void* p, u32 bytes) { uint64_t a = (uint64_t)p; return u32x4{(u32)a, (u32)(a >> 32), bytes, 0x00020000u}; }
DI void glds16(u32 ldsaddr, u32 voff, u32x4 srd, u32 soff) {
  asm volatile("s_mov_b32 m0, %0\n\ts_nop 0\n\tbuffer_load_dwordx4 %1, %2, %3 offen lds" :: "s"(ldsaddr), "v"(voff), "s"(srd), "s"(soff) : "memory");
}
DI u32x4 ld_sc1(rsrc_t r, u32 off) { return __builtin_amdgcn_raw_buffer_load_b128(r, off, 0, 16); }
DI u32x2 ld_sc1_2(rsrc_t r, u32 off) { return __builtin_amdgcn_raw_buffer_load_b64(r, off, 0, 16); }
DI void st_sc1(rsrc_t r, u32 off, u32x4 v) { __builtin_amdgcn_raw_buffer_store_b128(v, r, off, 0, 16); }
DI void st_sc1_2(rsrc_t r, u32 off, u32x2 v) { __builtin_amdgcn_raw_buffer_store_b64(v, r, off, 0, 16); }
DI bool all_ok(bool ok) { return __builtin_amdgcn_ballot_w64(!ok) == 0; }

template <int CTRL> DI float dpp_add(float v) {
  int o = __builtin_amdgcn_update_dpp(0, __builtin_bit_cast(int, v), CTRL, 0xf, 0xf, true);
  return v + __builtin_bit_cast(float, o);
}
DI float wave_sum(float v) {
  v = dpp_add<0xB1>(v); v = dpp_add<0x4E>(v); v = dpp_add<0x141>(v); v = dpp_add<0x140>(v);
  u32 u = __builtin_bit_cast(u32, v);
  auto a = __builtin_amdgcn_permlane16_swap(u, u, false, false);
  v = __builtin_bit_cast(float, (u32)a[0]) + __builtin_bit_cast(float, (u32)a[1]);
  u = __builtin_bit_cast(u32, v);
  auto b = __builtin_amdgcn_permlane32_swap(u, u, false, false);
  return __builtin_bit_cast(float, (u32)b[0]) + __builtin_bit_cast(float, (u32)b[1]);
}

struct G1L { u32x4 sx, sg, su; u32 vx, vg, vu, lw; };
DI void g1_issue1(const G1L& L, int st, int k) {
  const u32 lb = L.lw + (st % 3) * G1_STAGE + k * 8192;
  if (k < 2) glds16(lb, L.vx, L.sx, st * 128 + k * 65536);
  else if (k < 4) glds16(lb, L.vg, L.sg, (st * 64 + (k - 2) * 32) * 3584);
  else glds16(lb, L.vu, L.su, (st * 64 + (k - 4) * 32) * 3584);
}
struct Frag { u32x4 bx[2], aw[2]; };

__global__ __launch_bounds__(512, 2) void fused_kernel(const char* __restrict__ x, const unsigned short* __restrict__ wr, const unsigned short* __restrict__ wi,
                                                       const char* __restrict__ wg, const char* __restrict__ wu, const char* __restrict__ wd,
                                                       char* __restrict__ ws, char* __restrict__ out) {
  extern __shared__ __attribute__((aligned(16))) char smem[];
  LAS char* sm = (LAS char*)smem;
  const int b = blockIdx.x, tid = threadIdx.x, lane = tid & 63, w = __builtin_amdgcn_readfirstlane(tid >> 6);
  const int p16 = lane & 15, t = lane >> 4;
  const f32x4 z4 = {0.f, 0.f, 0.f, 0.f};
  u32 pfd = 0;
  {
    uint64_t pc; asm volatile("s_getpc_b64 %0" : "=s"(pc));
    if (b < 8 && tid < CODE_LINES) asm volatile("global_load_dword %0, %1, %2" : "+v"(pfd) : "v"(tid * 128), "s"(pc) : "memory");
  }
  STAMP(0);
  const bool is_gemm = b >= 32;
  const int q_ = b - 32, xc = q_ & 7, r_ = q_ >> 3;
  const int mi = (xc >> 1) * 4 + (r_ & 3), fj = (xc & 1) * 7 + (r_ >> 2);
  G1L L;
  L.sx = mk_srd(x, 2097152); L.sg = mk_srd(wg, 1835008); L.su = mk_srd(wu, 1835008);
  L.lw = (u32)(uintptr_t)sm + w * 1024;
  glds16(L.lw + FS, (b + 256 * w) * 1024 + lane * 16, L.sx, 0);
  if (is_gemm) {
    { int row = w * 8 + (lane >> 3), pos = lane & 7; L.vx = (mi * 128 + row) * 1024 + ((pos ^ ((row >> 1) & 7)) << 4); }
    { int k = w * 4 + t; L.vg = k * 3584 + fj * 256 + ((p16 ^ (2 * (k & 3))) << 4); L.vu = L.vg ^ 128; }
#pragma unroll
    for (int st = 0; st < 3; ++st)
#pragma unroll
      for (int k = 0; k < 6; ++k) g1_issue1(L, st, k);
  }
  const rsrc_t rw = __builtin_amdgcn_make_buffer_rsrc(ws, 0, WS_END, 0x00020000);
  const u32 tag = *(const u32*)(ws + WS_EPOCH) * 0x9E3779B1u + 0x7F4A7C15u;

  {
    u32x2 b1re, b1im;
    {
      float c[4], sn[4];
#pragma unroll
      for (int jj = 0; jj < 4; ++jj) { float rev = (float)((4 * t + jj) * p16) * (1.0f / 16); c[jj] = 0.25f * cosr(rev); sn[jj] = -0.25f * sinr(rev); }
      b1re = u32x2{pk_f16(c[0], c[1]), pk_f16(c[2], c[3])};
      b1im = u32x2{pk_f16(sn[0], sn[1]), pk_f16(sn[2], sn[3])};
    }
    float twc[2][4], tws[2][4];
    u32x4 b2re[2], b2im[2];
#pragma unroll
    for (int hh = 0; hh < 2; ++hh)
#pragma unroll
      for (int r = 0; r < 4; ++r) {
        float nk = (float)((16 * hh + 4 * t + r) * p16);
        twc[hh][r] = cosr(nk * (1.0f / 512)); tws[hh][r] = sinr(nk * (1.0f / 512));
        float c = 0.125f * cosr(nk * (1.0f / 32)), sn = 0.125f * sinr(nk * (1.0f / 32));
        b2re[hh][r] = pk_f16(c, sn); b2im[hh][r] = pk_f16(-sn, c);
      }
    const int jp = p16 >> 1, po = p16 & 1;
    float c0, s0, c1, s1;
    { float rev = (float)(2 * t * jp) * 0.125f; c0 = 0.5f * cosr(rev); s0 = 0.5f * sinr(rev); }
    { float rev = (float)((2 * t + 1) * jp) * 0.125f; c1 = 0.5f * cosr(rev); s1 = 0.5f * sinr(rev); }
    const u32x2 a8 = po ? u32x2{pk_f16(-s0, c0), pk_f16(-s1, c1)} : u32x2{pk_f16(c0, s0), pk_f16(c1, s1)};
    LAS char* img = sm + FS + w * 1024;
    SB();
    if (is_gemm) WAITV(18); else WAITV(0);
    asm volatile("" :: "v"(pfd));
    STAMP(1);
    const u32x4 xv = *(LAS u32x4*)(img + lane * 16);
    float v[8];
#pragma unroll
    for (int k = 0; k < 4; ++k) { v[2 * k] = bf_lo(xv[k]); v[2 * k + 1] = bf_hi(xv[k]); }
    float sum = ((v[0] + v[1]) + (v[2] + v[3])) + ((v[4] + v[5]) + (v[6] + v[7]));
    const float mean = wave_sum(sum) * (1.0f / 512);
    float sq0 = 0.f, sq1 = 0.f;
#pragma unroll
    for (int k = 0; k < 4; ++k) { float d = v[k] - mean, e = v[k + 4] - mean; sq0 += d * d; sq1 += e * e; }
    const float var = wave_sum(sq0 + sq1) * (1.0f / 512);
    const float mb = rnd_bf(mean);
    const float sd = rnd_bf(__builtin_amdgcn_sqrtf(rnd_bf(rnd_bf(var) + 1e-5f)));
    const float rs = __builtin_amdgcn_rcpf(sd);
    if (lane == 0) st_sc1_2(rw, WS_ST + (b + 256 * w) * 8, u32x2{pk_bf16(mb, sd), tag});
    u32x4 xh;
#pragma unroll
    for (int k = 0; k < 4; ++k) {
      u32 dp = pk_bf16(v[2 * k] - mb, v[2 * k + 1] - mb);
      u32 np = pk_bf16(bf_lo(dp) * rs, bf_hi(dp) * rs);
      xh[k] = pk_f16(bf_lo(np), bf_hi(np));
    }
    *(LAS u32x4*)(img + ((lane ^ ((t & 1) << 1)) << 4)) = xh;
    __builtin_amdgcn_fence(__ATOMIC_RELEASE, "wavefront");
    __builtin_amdgcn_wave_barrier();
    f32x4 d1re[2], d1im[2];
#pragma unroll
    for (int hh = 0; hh < 2; ++hh) {
      u32x2 a = tr_read(img + (4 * t + (p16 >> 2)) * 64 + ((hh ^ (t & 1)) << 5) + (p16 & 3) * 8);
      d1re[hh] = mfma16_f16(a, b1re, z4);
      d1im[hh] = mfma16_f16(a, b1im, z4);
    }
    float ny = ((d1re[0][0] - d1re[0][1]) + (d1re[0][2] - d1re[0][3])) + ((d1re[1][0] - d1re[1][1]) + (d1re[1][2] - d1re[1][3]));
    f32x4 d2re = z4, d2im = z4;
#pragma unroll
    for (int hh = 0; hh < 2; ++hh) {
      u32x4 a;
#pragma unroll
      for (int r = 0; r < 4; ++r)
        a[r] = pk_f16(d1re[hh][r] * twc[hh][r] + d1im[hh][r] * tws[hh][r], d1im[hh][r] * twc[hh][r] - d1re[hh][r] * tws[hh][r]);
      d2re = mfma32_f16(a, b2re[hh], d2re);
      d2im = mfma32_f16(a, b2im[hh], d2im);
    }
    {
      int nb = __builtin_bit_cast(int, ny);
      float n1_ = __builtin_bit_cast(float, __builtin_amdgcn_readlane(nb, 16));
      float n2_ = __builtin_bit_cast(float, __builtin_amdgcn_readlane(nb, 32));
      float n3_ = __builtin_bit_cast(float, __builtin_amdgcn_readlane(nb, 48));
      float tot = 0.125f * ((ny + n1_) + (n2_ + n3_));
      if (lane == 0) d2im[0] = tot;
    }
    LAS char* ex = sm + FS + 8192;
    {
      u32x4 o;
#pragma unroll
      for (int r = 0; r < 4; ++r) o[r] = pk_f16(d2re[r], d2im[r]);
      int e = 4 * p16 + t; e ^= (e >> 3) & 3;
      *(LAS u32x4*)(ex + w * 1024 + e * 16) = o;
    }
    BAR();
    {
      const int wq = w & 3, hf = w >> 2;
      int e = 16 * wq + p16; e ^= (e >> 3) & 3;
      u32x4 r0 = *(LAS u32x4*)(ex + (2 * t) * 1024 + e * 16);
      u32x4 r1 = *(LAS u32x4*)(ex + (2 * t + 1) * 1024 + e * 16);
      u32 o[4];
#pragma unroll
      for (int u = 0; u < 4; ++u) {
        f32x4 d = mfma16_f16(a8, u32x2{r0[u], r1[u]}, z4);
        if (u == 0 && wq == 0) {
          float e0 = po ? -s0 : c0, e1 = po ? -s1 : c1;
          u32x2 as = (jp & 3) == 0 ? a8 : (jp < 4 ? u32x2{pk_f16(e0, 0.f), pk_f16(e1, 0.f)} : u32x2{pk_f16(0.f, e0), pk_f16(0.f, e1)});
          f32x4 ds = mfma16_f16(as, u32x2{r0[0], r1[0]}, z4);
          if (p16 == 0) d = ds;
        }
        o[u] = hf ? pk_f16(d[2], d[3]) : pk_f16(d[0], d[1]);
      }
      const u32 ub = WS_U + b * 16384 + (2 * t + hf) * 2048 + (64 * wq + 4 * p16) * 8;
      st_sc1(rw, ub, u32x4{o[0], tag, o[1], tag});
      st_sc1(rw, ub + 16, u32x4{o[2], tag, o[3], tag});
    }
  }
  STAMP(2);

  if (is_gemm) {
    const int wr_ = w >> 1, wt = w & 1, h = lane >> 5;
    int xoff[4], woff[2];
#pragma unroll
    for (int ks = 0; ks < 4; ++ks) { int tok = wt * 64 + (lane & 31); xoff[ks] = tok * 128 + (((2 * ks + h) ^ ((tok >> 1) & 7)) << 4); }
    {
      int arr = t & 1, q = p16 >> 2, p = lane & 3;
#pragma unroll
      for (int m = 0; m < 2; ++m)
        woff[m] = 16384 + arr * 16384 + (8 * h + q) * 256 + (((wr_ * 2 + m) ^ (q | (arr << 2))) << 5) + (p & 1) * 16 + (p >> 1) * 8;
    }
    auto rd = [&](Frag& f, int st, int ks) {
      LAS char* buf = sm + (st % 3) * G1_STAGE;
#pragma unroll
      for (int n = 0; n < 2; ++n) f.bx[n] = *(LAS u32x4*)(buf + xoff[ks] + n * 4096);
#pragma unroll
      for (int m = 0; m < 2; ++m) {
        u32x2 lo = tr_read(buf + woff[m] + ks * 4096), hi = tr_read(buf + woff[m] + ks * 4096 + 1024);
        f.aw[m] = u32x4{lo[0], lo[1], hi[0], hi[1]};
      }
    };
    f32x16 acc[2][2];
#pragma unroll
    for (int m = 0; m < 2; ++m)
#pragma unroll
      for (int n = 0; n < 2; ++n)
#pragma unroll
        for (int k = 0; k < 16; ++k) acc[m][n][k] = 0.f;
    Frag fa, fb;
    auto mm = [&](Frag& f, int ls, int k0) {
      acc[0][0] = mfma3232(f.aw[0], f.bx[0], acc[0][0]);
      SB(); if (ls < 8 && k0 >= 0) g1_issue1(L, ls, k0); SB();
      acc[0][1] = mfma3232(f.aw[0], f.bx[1], acc[0][1]);
      acc[1][1] = mfma3232(f.aw[1], f.bx[1], acc[1][1]);
      SB(); if (ls < 8 && k0 >= 0) g1_issue1(L, ls, k0 + 1); SB();
      acc[1][0] = mfma3232(f.aw[1], f.bx[0], acc[1][0]);
    };
    SB();
    WAITV(12 + 3);
    __builtin_amdgcn_s_barrier();
    STAMP(3);
    rd(fa, 0, 0);
#pragma unroll
    for (int st = 0; st < 8; ++st) {
      const int ls = st == 0 ? 8 : st + 2;
      SB(); rd(fb, st, 1); SB(); mm(fa, ls, 2);
      SB(); rd(fa, st, 2); SB(); mm(fb, ls, 4);
      SB(); rd(fb, st, 3); SB(); mm(fa, ls, -1);
      SB();
      if (st < 7) {
        if (st < 6) WAITV(6); else WAITV(0);
        BAR();
        SB();
        rd(fa, st + 1, 0);
        SB();
      }
      mm(fb, st + 3, 0);
      SB();
    }
    STAMP(4);
    u32x2 stv[2]; u32x4 cs[2][2][4];
    for (int spin = 0; spin < SPIN_CAP; ++spin) {
      asm volatile("" ::: "memory");
      bool ok = true;
#pragma unroll
      for (int n = 0; n < 2; ++n) { stv[n] = ld_sc1_2(rw, WS_ST + (mi * 128 + wt * 64 + n * 32 + (lane & 31)) * 8); }
#pragma unroll
      for (int m = 0; m < 2; ++m)
#pragma unroll
        for (int a = 0; a < 2; ++a)
#pragma unroll
          for (int k = 0; k < 4; ++k) cs[m][a][k] = ld_sc1(rw, WS_CS + (a * F_ + fj * 128 + (wr_ * 2 + m) * 16 + 8 * h + 2 * k) * 8);
#pragma unroll
      for (int n = 0; n < 2; ++n) ok &= stv[n][1] == tag;
#pragma unroll
      for (int m = 0; m < 2; ++m)
#pragma unroll
        for (int a = 0; a < 2; ++a)
#pragma unroll
          for (int k = 0; k < 4; ++k) ok &= (cs[m][a][k][1] == tag) & (cs[m][a][k][3] == tag);
      if (all_ok(ok)) break;
    }
    STAMP(5);
#pragma unroll
    for (int n = 0; n < 2; ++n) {
      const int tok = mi * 128 + wt * 64 + n * 32 + (lane & 31);
      const float nm = -bf_lo(stv[n][0]);
      const float rs = __builtin_amdgcn_rcpf(bf_hi(stv[n][0])), rs2 = rs * rs;
#pragma unroll
      for (int m = 0; m < 2; ++m) {
        float hv[8];
#pragma unroll
        for (int k = 0; k < 8; ++k) {
          const u32 cgu = cs[m][0][k >> 1][(k & 1) * 2], cuu = cs[m][1][k >> 1][(k & 1) * 2];
          float cg = __builtin_bit_cast(float, cgu), cu = __builtin_bit_cast(float, cuu);
          float gg = __builtin_fmaf(nm, cg, acc[m][n][k]), uu = __builtin_fmaf(nm, cu, acc[m][n][8 + k]);
          hv[k] = __builtin_fmaxf(gg, 0.f) * (uu * rs2);
        }
        const int fo = fj * 16 + (wr_ * 2 + m) * 2 + h;
        st_sc1(rw, WS_H + ((tok >> 4) * 56 + (fo >> 2)) * 1024 + (fo & 3) * 256 + (tok & 15) * 16,
               u32x4{pk_bf16(hv[0], hv[1]), pk_bf16(hv[2], hv[3]), pk_bf16(hv[4], hv[5]), pk_bf16(hv[6], hv[7])});
      }
    }
    STAMP(6);
    WAITV(0);
    BAR();
    if (tid == 0) __builtin_amdgcn_raw_buffer_store_b32(tag, rw, WS_F2 + mi * 128 + fj * 4, 0, 16);
    STAMP(7);
  } else if (b < 28) {
    const char* wsrc = b < 14 ? wg : wu;
    const int ct = b < 14 ? b : b - 14;
    const int r0 = tid >> 4;
    float acc[8] = {0, 0, 0, 0, 0, 0, 0, 0};
    u32x4 v[16];
#pragma unroll
    for (int k = 0; k < 16; ++k) v[k] = *(const u32x4*)(wsrc + (size_t)(r0 + 32 * k) * 3584 + ct * 256 + p16 * 16);
#pragma unroll
    for (int k = 0; k < 16; ++k)
#pragma unroll
      for (int q = 0; q < 4; ++q) { acc[2 * q] += bf_lo(v[k][q]); acc[2 * q + 1] += bf_hi(v[k][q]); }
    LAS float* red = (LAS float*)sm;
    *(LAS f32x4*)(red + r0 * 128 + p16 * 8) = f32x4{acc[0], acc[1], acc[2], acc[3]};
    *(LAS f32x4*)(red + r0 * 128 + p16 * 8 + 4) = f32x4{acc[4], acc[5], acc[6], acc[7]};
    BAR();
    if (tid < 128) {
      float sacc = 0.f;
#pragma unroll
      for (int k = 0; k < 32; ++k) sacc += red[k * 128 + tid];
      st_sc1_2(rw, WS_CS + (b * 128 + tid) * 8, u32x2{__builtin_bit_cast(u32, sacc), tag});
    }
    BAR();
  }

  {
    const int jp = b & 7, g = b >> 3;
    LAS char* cb = sm;
    LAS char* fb = sm + 8 * 1040;
    {
      int q = tid & 255, arr = tid >> 8;
      const unsigned short* wsrc = arr ? wi : wr;
      int e0 = (8 * q + jp) * DF_ + 8 * g;
      const u32* pa = (const u32*)(wsrc + (e0 & ~1));
      u32 d0 = pa[0], d1 = pa[1], d2 = pa[2], d3 = pa[3], d4 = pa[4];
      const int i = tid >> 1, hf = tid & 1;
      u32x4 u0, u1;
      for (int spin = 0; spin < SPIN_CAP; ++spin) {
      asm volatile("" ::: "memory");
        const u32 off = WS_U + i * 16384 + jp * 2048 + g * 64 + hf * 32;
        u0 = ld_sc1(rw, off); u1 = ld_sc1(rw, off + 16);
        if (all_ok((u0[1] == tag) & (u0[3] == tag) & (u1[1] == tag) & (u1[3] == tag))) break;
      }
      const int pos = (i & 192) + 4 * (i & 15) + ((i >> 4) & 3);
      *(LAS u32*)(cb + (4 * hf + 0) * 1040 + pos * 4) = u0[0];
      *(LAS u32*)(cb + (4 * hf + 1) * 1040 + pos * 4) = u0[2];
      *(LAS u32*)(cb + (4 * hf + 2) * 1040 + pos * 4) = u1[0];
      *(LAS u32*)(cb + (4 * hf + 3) * 1040 + pos * 4) = u1[2];
      u32 sh = (e0 & 1) * 16;
      u32 f[4] = {__builtin_amdgcn_alignbit(d1, d0, sh), __builtin_amdgcn_alignbit(d2, d1, sh), __builtin_amdgcn_alignbit(d3, d2, sh), __builtin_amdgcn_alignbit(d4, d3, sh)};
      int posq = (q & 192) + 4 * (q & 15) + ((q >> 4) & 3);
#pragma unroll
      for (int k = 0; k < 4; ++k) {
        *(LAS unsigned short*)(fb + (2 * k) * 1040 + posq * 4 + arr * 2) = (unsigned short)(f[k] & 0xffff);
        *(LAS unsigned short*)(fb + (2 * k + 1) * 1040 + posq * 4 + arr * 2) = (unsigned short)(f[k] >> 16);
      }
    }
    STAMP(8);
    u32x4 bc1re, bc1im, ac2re, ac2im, bc3re, bc3im, ac4re, ac4im;
    float t1c[4], t1s[4], t2c[4], t2s[4];
    const float FSC = (2.0f / (2048.0f * 512.0f)) * (4.0f * 8.0f * 2.0f * 4.0f * 4.0f);
#pragma unroll
    for (int r = 0; r < 4; ++r) {
      const int y = 4 * t + r;
      const float n1 = (float)(y * (8 * p16 + jp)), n2 = (float)(p16 * (8 * y + jp));
      { float cc = 0.25f * cosr(n1 * (1.0f / 128)), sn = 0.25f * sinr(n1 * (1.0f / 128)); bc1re[r] = pk_f16(cc, sn); bc1im[r] = pk_f16(-sn, cc); }
      { float rev = (float)(y * p16) * (1.0f / 16); float cc = cosr(rev), sn = sinr(rev);
        ac2re[r] = pk_f16(0.25f * cc, 0.25f * sn); ac2im[r] = pk_f16(-0.25f * sn, 0.25f * cc);
        bc3re[r] = pk_bf16(cc, -sn); bc3im[r] = pk_bf16(sn, cc); }
      { float cc = cosr(n2 * (1.0f / 128)), sn = sinr(n2 * (1.0f / 128)); ac4re[r] = pk_bf16(cc, -sn); ac4im[r] = pk_bf16(sn, cc); }
      t1c[r] = cosr(n1 * (1.0f / 2048)); t1s[r] = sinr(n1 * (1.0f / 2048));
      t2c[r] = FSC * cosr(n2 * (1.0f / 2048)); t2s[r] = FSC * sinr(n2 * (1.0f / 2048));
    }
    BAR();
    const u32x4 uin = *(LAS u32x4*)(cb + w * 1040 + lane * 16);
    const u32x4 fin = *(LAS u32x4*)(fb + w * 1040 + lane * 16);
    const bool special = (g == 0 && w == 0);
    const int npass = (special && (jp & 3) == 0) ? 2 : 1;
    f32x4 ore = z4, oim = z4;
    for (int pass = 0; pass < npass; ++pass) {
      u32x4 a = uin;
      float fr[4], fi[4];
#pragma unroll
      for (int r = 0; r < 4; ++r) { fr[r] = bf_lo(fin[r]); fi[r] = bf_hi(fin[r]); }
      if (special) {
        const int col = (npass == 2) ? (pass ? 256 : 0) : (jp < 4 ? 0 : 256);
#pragma unroll
        for (int r = 0; r < 4; ++r) {
          int ks = 8 * (p16 + 64 * t + 16 * r) + jp, km = (2048 - ks) & 2047;
          float ar = bf_lo(wr[ks * DF_ + col]), ai = bf_lo(wi[ks * DF_ + col]);
          float br = bf_lo(wr[km * DF_ + col]), bi = bf_lo(wi[km * DF_ + col]);
          fr[r] = 0.5f * (ar + br); fi[r] = 0.5f * (ai - bi);
          if (npass == 2) a[r] = pass ? (uin[r] >> 16) : (uin[r] & 0xffffu);
        }
      }
      f32x4 dre = mfma32_f16(a, bc1re, z4), dim = mfma32_f16(a, bc1im, z4);
#pragma unroll
      for (int r = 0; r < 4; ++r) a[r] = pk_f16(dre[r] * t1c[r] + dim[r] * t1s[r], dim[r] * t1c[r] - dre[r] * t1s[r]);
      dre = mfma32_f16(ac2re, a, z4); dim = mfma32_f16(ac2im, a, z4);
#pragma unroll
      for (int r = 0; r < 4; ++r) a[r] = pk_bf16(dre[r] * fr[r] - dim[r] * fi[r], dre[r] * fi[r] + dim[r] * fr[r]);
      dre = mfma32_bf16(a, bc3re, z4); dim = mfma32_bf16(a, bc3im, z4);
#pragma unroll
      for (int r = 0; r < 4; ++r) a[r] = pk_bf16(dre[r] * t2c[r] - dim[r] * t2s[r], dre[r] * t2s[r] + dim[r] * t2c[r]);
      dre = mfma32_bf16(ac4re, a, z4); dim = mfma32_bf16(ac4im, a, z4);
      if (npass == 2) { if (pass == 0) ore = dre; else oim = dre; }
      else { ore = dre; oim = dim; }
    }
    {
      u32x4 o;
#pragma unroll
      for (int r = 0; r < 4; ++r) o[r] = pk_bf16(ore[r], oim[r]);
      *(LAS u32x4*)(cb + w * 1040 + lane * 16) = o;
    }
    BAR();
    {
      int ps = tid >> 1, hf = tid & 1;
      int i = (ps >> 5) + 8 * (ps & 31);
      int pos = (i & 192) + 4 * (i & 15) + ((i >> 4) & 3);
      u32 o[4];
#pragma unroll
      for (int k = 0; k < 4; ++k) o[k] = *(LAS u32*)(cb + (4 * hf + k) * 1040 + pos * 4);
      st_sc1(rw, WS_V + b * 16384 + tid * 32, u32x4{o[0], tag, o[1], tag});
      st_sc1(rw, WS_V + b * 16384 + tid * 32 + 16, u32x4{o[2], tag, o[3], tag});
    }
  }
  STAMP(9);

  const int rt = (b & 7) * 4 + ((b >> 3) & 3), ct = b >> 5;
  u32x4 wf[7][4];
  {
    const char* src = wd + (size_t)(w * 224 + 8 * t) * 1024 + ct * 128 + p16 * 8;
    u32x2 rwv[7][8];
#pragma unroll
    for (int ks = 0; ks < 7; ++ks)
#pragma unroll
      for (int j = 0; j < 8; ++j) rwv[ks][j] = *(const u32x2*)(src + (size_t)(ks * 32 + j) * 1024);
    SB();
#pragma unroll
    for (int ks = 0; ks < 7; ++ks)
#pragma unroll
      for (int kp = 0; kp < 4; ++kp) {
        wf[ks][0][kp] = __builtin_amdgcn_perm(rwv[ks][2 * kp + 1][0], rwv[ks][2 * kp][0], 0x05040100u);
        wf[ks][1][kp] = __builtin_amdgcn_perm(rwv[ks][2 * kp + 1][0], rwv[ks][2 * kp][0], 0x07060302u);
        wf[ks][2][kp] = __builtin_amdgcn_perm(rwv[ks][2 * kp + 1][1], rwv[ks][2 * kp][1], 0x05040100u);
        wf[ks][3][kp] = __builtin_amdgcn_perm(rwv[ks][2 * kp + 1][1], rwv[ks][2 * kp][1], 0x07060302u);
      }
    SB();
  }
  STAMP(10);

  {
    LAS char* yb = sm + FS;
    const int s = b + 256 * w;
    const u32x4 xv = *(const u32x4*)(x + (size_t)s * 1024 + (4 * p16 + t) * 16);
    {
      const int g = 4 * w + (p16 >> 2);
      const u32 off = WS_V + ((b & 7) * 32 + (b >> 3)) * 64 + (p16 & 3) * 16 + (g * 8 + 2 * t) * 16384;
      const int j = p16 >> 1, po = p16 & 1;
      float c0, s0, c1, s1;
      { float rev = (float)(2 * t * j) * 0.125f; c0 = cosr(rev); s0 = sinr(rev); }
      { float rev = (float)((2 * t + 1) * j) * 0.125f; c1 = cosr(rev); s1 = sinr(rev); }
      const u32x2 a8 = po ? u32x2{pk_bf16(s0, c0), pk_bf16(s1, c1)} : u32x2{pk_bf16(c0, -s0), pk_bf16(c1, -s1)};
      u32x4 r0, r1;
      for (int spin = 0; spin < SPIN_CAP; ++spin) {
      asm volatile("" ::: "memory");
        r0 = ld_sc1(rw, off); r1 = ld_sc1(rw, off + 16384);
        if (all_ok((r0[1] == tag) & (r0[3] == tag) & (r1[1] == tag) & (r1[3] == tag))) break;
      }
      STAMP(11);
      u32x2 o0, o1;
#pragma unroll
      for (int u = 0; u < 2; ++u) {
        f32x4 d = mfma16_bf16(a8, u32x2{r0[2 * u], r1[2 * u]}, z4);
        if (u == 0 && w == 0) {
          u32 e0, e1;
          const bool on = (po == 0) ? (t < 2) : (t >= 2);
          if ((t & 1) == 0) e0 = po ? pk_bf16(0.f, 0.5f * c0) : pk_bf16(0.5f * c0, 0.f);
          else e0 = on ? pk_bf16(c0, -s0) : 0u;
          e1 = on ? pk_bf16(c1, -s1) : 0u;
          f32x4 ds = mfma16_bf16(u32x2{e0, e1}, u32x2{r0[0], r1[0]}, z4);
          if (p16 == 0) d = ds;
        }
        o0[u] = pk_bf16(d[0], d[1]); o1[u] = pk_bf16(d[2], d[3]);
      }
      *(LAS u32x2*)(yb + (2 * t) * 1024 + (16 * w + p16) * 8) = o0;
      *(LAS u32x2*)(yb + (2 * t + 1) * 1024 + (16 * w + p16) * 8) = o1;
    }
    u32x4 bi1re[2], bi1im[2], bi2;
    float twc[2][4], tws[2][4];
#pragma unroll
    for (int hh = 0; hh < 2; ++hh) {
      const int n2 = 8 * (p16 >> 2) + 4 * hh + (p16 & 3);
#pragma unroll
      for (int m = 0; m < 4; ++m) {
        float rev = (float)(n2 * (t + 4 * m)) * (1.0f / 32); float cc = cosr(rev), sn = sinr(rev);
        bi1re[hh][m] = pk_bf16(cc, -sn); bi1im[hh][m] = pk_bf16(sn, cc);
        float rev2 = (float)(n2 * (4 * t + m)) * (1.0f / 512);
        twc[hh][m] = cosr(rev2); tws[hh][m] = sinr(rev2);
      }
    }
#pragma unroll
    for (int r = 0; r < 4; ++r) { float rev = (float)(p16 * (4 * t + r)) * (1.0f / 16); bi2[r] = pk_bf16(cosr(rev), -sinr(rev)); }
    BAR();
    u32x4 a;
#pragma unroll
    for (int m = 0; m < 4; ++m) a[m] = *(LAS u32*)(yb + w * 1024 + (lane + 64 * m) * 4);
    const float nyq = bf_hi(*(LAS u32*)(yb + w * 1024));
    u32 ov[4];
#pragma unroll
    for (int hh = 0; hh < 2; ++hh) {
      f32x4 gre = mfma32_bf16(a, bi1re[hh], z4), gim = mfma32_bf16(a, bi1im[hh], z4);
      u32x4 bb;
#pragma unroll
      for (int r = 0; r < 4; ++r) bb[r] = pk_bf16(gre[r] * twc[hh][r] - gim[r] * tws[hh][r], gre[r] * tws[hh][r] + gim[r] * twc[hh][r]);
      f32x4 d = mfma32_bf16(bb, bi2, f32x4{nyq, -nyq, nyq, -nyq});
      ov[2 * hh] = pk_bf16(bf_lo(xv[2 * hh]) + d[0], bf_hi(xv[2 * hh]) + d[1]);
      ov[2 * hh + 1] = pk_bf16(bf_lo(xv[2 * hh + 1]) + d[2], bf_hi(xv[2 * hh + 1]) + d[3]);
    }
    st_sc1(rw, WS_R + s * 2048 + (4 * p16 + t) * 32, u32x4{ov[0], tag, ov[1], tag});
    st_sc1(rw, WS_R + s * 2048 + (4 * p16 + t) * 32 + 16, u32x4{ov[2], tag, ov[3], tag});
  }
  STAMP(12);

  {
    for (int spin = 0; spin < SPIN_CAP; ++spin) {
      asm volatile("" ::: "memory");
      u32 fl = __builtin_amdgcn_raw_buffer_load_b32(rw, WS_F2 + (rt >> 1) * 128 + (lane < 14 ? lane : 0) * 4, 0, 16);
      if (all_ok(fl == tag)) break;
    }
    __builtin_amdgcn_fence(__ATOMIC_ACQUIRE, "wavefront");
    STAMP(13);
    const u32 hs = WS_H + ((rt * 4) * 56 + w * 7) * 1024 + lane * 16;
    u32x4 hf[4][7];
#pragma unroll
    for (int tt = 0; tt < 3; ++tt)
#pragma unroll
      for (int ks = 0; ks < 7; ++ks) hf[tt][ks] = ld_sc1(rw, hs + (tt * 56 + ks) * 1024);
    SB();
#pragma unroll
    for (int tt = 0; tt < 4; ++tt) {
      f32x4 acc[4] = {z4, z4, z4, z4};
#pragma unroll
      for (int ks = 0; ks < 7; ++ks) {
#pragma unroll
        for (int u = 0; u < 4; ++u) acc[u] = mfma32_bf16(wf[ks][u], hf[tt][ks], acc[u]);
        if (tt == 0) { SB(); hf[3][ks] = ld_sc1(rw, hs + (3 * 56 + ks) * 1024); SB(); }
      }
#pragma unroll
      for (int u = 0; u < 4; ++u) *(LAS f32x4*)(sm + w * 16384 + (tt * 4 + u) * 1024 + lane * 16) = acc[u];
      SB();
    }
    STAMP(14);
    const int tt = w >> 1, hh = w & 1;
    const int row = rt * 64 + tt * 16 + p16;
    u32x4 r0, r1;
    for (int spin = 0; spin < SPIN_CAP; ++spin) {
      asm volatile("" ::: "memory");
      const u32 off = WS_R + row * 2048 + ct * 256 + t * 64 + hh * 32;
      r0 = ld_sc1(rw, off); r1 = ld_sc1(rw, off + 16);
      if (all_ok((r0[1] == tag) & (r0[3] == tag) & (r1[1] == tag) & (r1[3] == tag))) break;
    }
    BAR();
    f32x2 sacc[4];
#pragma unroll
    for (int u = 0; u < 4; ++u) {
      sacc[u] = f32x2{0.f, 0.f};
#pragma unroll
      for (int pw = 0; pw < 8; ++pw) sacc[u] += *(LAS f32x2*)(sm + pw * 16384 + (tt * 4 + u) * 1024 + lane * 16 + hh * 8);
    }
    u32x4 o;
    o[0] = pk_bf16(bf_lo(r0[0]) + sacc[0][0], bf_hi(r0[0]) + sacc[1][0]);
    o[1] = pk_bf16(bf_lo(r0[2]) + sacc[2][0], bf_hi(r0[2]) + sacc[3][0]);
    o[2] = pk_bf16(bf_lo(r1[0]) + sacc[0][1], bf_hi(r1[0]) + sacc[1][1]);
    o[3] = pk_bf16(bf_lo(r1[2]) + sacc[2][1], bf_hi(r1[2]) + sacc[3][1]);
    *(u32x4*)(out + (size_t)row * 1024 + ct * 128 + t * 32 + hh * 16) = o;
    if (b == 0 && tid == 0) __builtin_amdgcn_raw_buffer_store_b32(tag, rw, WS_EPOCH, 0, 16);
  }
  STAMP(15);
}

extern "C" void kernel_launch(void* const* d_in, const int* in_sizes, int n_in, void* d_out, int out_size, void* d_ws, size_t ws_size,
                              hipStream_t stream) {
  (void)hipFuncSetAttribute((const void*)fused_kernel, hipFuncAttributeMaxDynamicSharedMemorySize, LDS_BYTES);
  fused_kernel<<<256, 512, LDS_BYTES, stream>>>((const char*)d_in[0], (const unsigned short*)d_in[1], (const unsigned short*)d_in[2], (const char*)d_in[4],
                                                (const char*)d_in[5], (const char*)d_in[6], (char*)d_ws, (char*)d_out);
}
```

```cpp
#include <hip/hip_runtime.h>
#include <stdint.h>

#define LAS __attribute__((address_space(3)))
#define GAS __attribute__((address_space(1)))
#define DI __device__ __forceinline__
typedef unsigned u32;
typedef unsigned u32x2 __attribute__((ext_vector_type(2)));
typedef unsigned u32x4 __attribute__((ext_vector_type(4)));
typedef float f32x2 __attribute__((ext_vector_type(2)));
typedef float f32x4 __attribute__((ext_vector_type(4)));
typedef float f32x16 __attribute__((ext_vector_type(16)));
typedef short s16x4 __attribute__((ext_vector_type(4)));
typedef _Float16 h16x2 __attribute__((ext_vector_type(2)));
typedef _Float16 h16x4 __attribute__((ext_vector_type(4)));
typedef _Float16 h16x8 __attribute__((ext_vector_type(8)));
typedef __bf16 b16x2 __attribute__((ext_vector_type(2)));
typedef __bf16 b16x8 __attribute__((ext_vector_type(8)));
typedef __amdgpu_buffer_rsrc_t rsrc_t;

constexpr int F_ = 1792, DF_ = 257;
constexpr u32 WS_EPOCH = 0;
constexpr u32 WS_F2 = 4096;
constexpr u32 WS_ST = 8192;
constexpr u32 WS_CS = WS_ST + 16384;
constexpr u32 WS_U = 65536;
constexpr u32 WS_V = WS_U + 4194304;
constexpr u32 WS_R = WS_V + 4194304;
constexpr u32 WS_H = WS_R + 4194304;
constexpr u32 WS_END = WS_H + 7340032;
constexpr int LDS_BYTES = 163840;
constexpr int G1_STAGE = 49152;
constexpr int FS = 3 * G1_STAGE;
constexpr int SPIN_CAP = 1 << 17;
constexpr size_t WS_DBG = 64 << 20;
#define STAMP(k) do {} while (0)
#define NST 0

DI u32 pk_f16(float a, float b) { h16x2 p = {(_Float16)a, (_Float16)b}; return __builtin_bit_cast(u32, p); }
DI u32 pk_bf16(float a, float b) { b16x2 p = {(__bf16)a, (__bf16)b}; return __builtin_bit_cast(u32, p); }
DI float bf_lo(u32 v) { return __builtin_bit_cast(float, v << 16); }
DI float bf_hi(u32 v) { return __builtin_bit_cast(float, v & 0xffff0000u); }
DI float rnd_bf(float a) { return bf_lo(pk_bf16(a, a)); }
DI float cosr(float rev) { return __builtin_amdgcn_cosf(rev); }
DI float sinr(float rev) { return __builtin_amdgcn_sinf(rev); }
DI f32x4 mfma16_f16(u32x2 a, u32x2 b, f32x4 c) { return __builtin_amdgcn_mfma_f32_16x16x16f16(__builtin_bit_cast(h16x4, a), __builtin_bit_cast(h16x4, b), c, 0, 0, 0); }
DI f32x4 mfma16_bf16(u32x2 a, u32x2 b, f32x4 c) { return __builtin_amdgcn_mfma_f32_16x16x16bf16_1k(__builtin_bit_cast(s16x4, a), __builtin_bit_cast(s16x4, b), c, 0, 0, 0); }
DI f32x4 mfma32_f16(u32x4 a, u32x4 b, f32x4 c) { return __builtin_amdgcn_mfma_f32_16x16x32_f16(__builtin_bit_cast(h16x8, a), __builtin_bit_cast(h16x8, b), c, 0, 0, 0); }
DI f32x4 mfma32_bf16(u32x4 a, u32x4 b, f32x4 c) { return __builtin_amdgcn_mfma_f32_16x16x32_bf16(__builtin_bit_cast(b16x8, a), __builtin_bit_cast(b16x8, b), c, 0, 0, 0); }
DI f32x16 mfma3232(u32x4 a, u32x4 b, f32x16 c) { return __builtin_amdgcn_mfma_f32_32x32x16_bf16(__builtin_bit_cast(b16x8, a), __builtin_bit_cast(b16x8, b), c, 0, 0, 0); }
DI u32x2 tr_read(LAS char* p) { return __builtin_bit_cast(u32x2, __builtin_amdgcn_ds_read_tr16_b64_v4i16((LAS s16x4*)p)); }
#define SB() __builtin_amdgcn_sched_barrier(0)
#define BAR() asm volatile("s_waitcnt lgkmcnt(0)\n\ts_barrier" ::: "memory")
#define WAITV(n) asm volatile("s_waitcnt vmcnt(%0)" :: "i"(n) : "memory")
DI u32x4 mk_srd(const void* p, u32 bytes) { uint64_t a = (uint64_t)p; return u32x4{(u32)a, (u32)(a >> 32), bytes, 0x00020000u}; }
DI void glds16(u32 ldsaddr, u32 voff, u32x4 srd, u32 soff) {
  asm volatile("s_mov_b32 m0, %0\n\ts_nop 0\n\tbuffer_load_dwordx4 %1, %2, %3 offen lds" :: "s"(ldsaddr), "v"(voff), "s"(srd), "s"(soff) : "memory");
}
DI u32x4 ld_sc1(rsrc_t r, u32 off) { return __builtin_amdgcn_raw_buffer_load_b128(r, off, 0, 16); }
DI u32x2 ld_sc1_2(rsrc_t r, u32 off) { return __builtin_amdgcn_raw_buffer_load_b64(r, off, 0, 16); }
DI void st_sc1(rsrc_t r, u32 off, u32x4 v) { __builtin_amdgcn_raw_buffer_store_b128(v, r, off, 0, 16); }
DI void st_sc1_2(rsrc_t r, u32 off, u32x2 v) { __builtin_amdgcn_raw_buffer_store_b64(v, r, off, 0, 16); }
DI bool all_ok(bool ok) { return __builtin_amdgcn_ballot_w64(!ok) == 0; }

template <int CTRL> DI float dpp_add(float v) {
  int o = __builtin_amdgcn_update_dpp(0, __builtin_bit_cast(int, v), CTRL, 0xf, 0xf, true);
  return v + __builtin_bit_cast(float, o);
}
DI float wave_sum(float v) {
  v = dpp_add<0xB1>(v); v = dpp_add<0x4E>(v); v = dpp_add<0x141>(v); v = dpp_add<0x140>(v);
  u32 u = __builtin_bit_cast(u32, v);
  auto a = __builtin_amdgcn_permlane16_swap(u, u, false, false);
  v = __builtin_bit_cast(float, (u32)a[0]) + __builtin_bit_cast(float, (u32)a[1]);
  u = __builtin_bit_cast(u32, v);
  auto b = __builtin_amdgcn_permlane32_swap(u, u, false, false);
  return __builtin_bit_cast(float, (u32)b[0]) + __builtin_bit_cast(float, (u32)b[1]);
}

struct G1L { u32x4 sx, sg, su; u32 vx, vg, vu, lw; };
DI void g1_issue1(const G1L& L, int st, int k) {
  const u32 lb = L.lw + (st % 3) * G1_STAGE + k * 8192;
  if (k < 2) glds16(lb, L.vx, L.sx, st * 128 + k * 65536);
  else if (k < 4) glds16(lb, L.vg, L.sg, (st * 64 + (k - 2) * 32) * 3584);
  else glds16(lb, L.vu, L.su, (st * 64 + (k - 4) * 32) * 3584);
}
struct Frag { u32x4 bx[2], aw[2]; };

__global__ __launch_bounds__(512, 2) void fused_kernel(const char* __restrict__ x, const unsigned short* __restrict__ wr, const unsigned short* __restrict__ wi,
                                                       const char* __restrict__ wg, const char* __restrict__ wu, const char* __restrict__ wd,
                                                       char* __restrict__ ws, char* __restrict__ out, const char* bx) {
  extern __shared__ __attribute__((aligned(16))) char smem[];
  LAS char* sm = (LAS char*)smem;
  const int b = blockIdx.x, tid = threadIdx.x, lane = tid & 63, w = __builtin_amdgcn_readfirstlane(tid >> 6);
  const int p16 = lane & 15, t = lane >> 4;
  const f32x4 z4 = {0.f, 0.f, 0.f, 0.f};
  u32 pfd = 0;
  {
    uint64_t pc; asm volatile("s_getpc_b64 %0" : "=s"(pc));
    if (b < 8 && tid < CODE_LINES) asm volatile("global_load_dword %0, %1, %2" : "+v"(pfd) : "v"(tid * 128), "s"(pc) : "memory");
  }
  STAMP(0);
  const bool is_gemm = b >= 32;
  const int q_ = b - 32, xc = q_ & 7, r_ = q_ >> 3;
  const int mi = (xc >> 1) * 4 + (r_ & 3), fj = (xc & 1) * 7 + (r_ >> 2);
  G1L L;
  L.sx = mk_srd(x, 2097152); L.sg = mk_srd(wg, 1835008); L.su = mk_srd(wu, 1835008);
  L.lw = (u32)(uintptr_t)sm + w * 1024;
  glds16(L.lw + FS, (b + 256 * w) * 1024 + lane * 16, L.sx, 0);
  if (is_gemm) {
    { int row = w * 8 + (lane >> 3), pos = lane & 7; L.vx = (mi * 128 + row) * 1024 + ((pos ^ ((row >> 1) & 7)) << 4); }
    { int k = w * 4 + t; L.vg = k * 3584 + fj * 256 + ((p16 ^ (2 * (k & 3))) << 4); L.vu = L.vg ^ 128; }
#pragma unroll
    for (int st = 0; st < 3; ++st)
#pragma unroll
      for (int k = 0; k < 6; ++k) g1_issue1(L, st, k);
  }
  const rsrc_t rw = __builtin_amdgcn_make_buffer_rsrc(ws, 0, WS_END, 0x00020000);
  const u32 tag = *(const u32*)(ws + WS_EPOCH) * 0x9E3779B1u + 0x7F4A7C15u;

  {
    u32x2 b1re, b1im;
    {
      float c[4], sn[4];
#pragma unroll
      for (int jj = 0; jj < 4; ++jj) { float rev = (float)((4 * t + jj) * p16) * (1.0f / 16); c[jj] = 0.25f * cosr(rev); sn[jj] = -0.25f * sinr(rev); }
      b1re = u32x2{pk_f16(c[0], c[1]), pk_f16(c[2], c[3])};
      b1im = u32x2{pk_f16(sn[0], sn[1]), pk_f16(sn[2], sn[3])};
    }
    float twc[2][4], tws[2][4];
    u32x4 b2re[2], b2im[2];
#pragma unroll
    for (int hh = 0; hh < 2; ++hh)
#pragma unroll
      for (int r = 0; r < 4; ++r) {
        float nk = (float)((16 * hh + 4 * t + r) * p16);
        twc[hh][r] = cosr(nk * (1.0f / 512)); tws[hh][r] = sinr(nk * (1.0f / 512));
        float c = 0.125f * cosr(nk * (1.0f / 32)), sn = 0.125f * sinr(nk * (1.0f / 32));
        b2re[hh][r] = pk_f16(c, sn); b2im[hh][r] = pk_f16(-sn, c);
      }
    const int jp = p16 >> 1, po = p16 & 1;
    float c0, s0, c1, s1;
    { float rev = (float)(2 * t * jp) * 0.125f; c0 = 0.5f * cosr(rev); s0 = 0.5f * sinr(rev); }
    { float rev = (float)((2 * t + 1) * jp) * 0.125f; c1 = 0.5f * cosr(rev); s1 = 0.5f * sinr(rev); }
    const u32x2 a8 = po ? u32x2{pk_f16(-s0, c0), pk_f16(-s1, c1)} : u32x2{pk_f16(c0, s0), pk_f16(c1, s1)};
    LAS char* img = sm + FS + w * 1024;
    SB();
    if (is_gemm) WAITV(18); else WAITV(0);
    asm volatile("" :: "v"(pfd));
    STAMP(1);
    const u32x4 xv = *(LAS u32x4*)(img + lane * 16);
    float v[8];
#pragma unroll
    for (int k = 0; k < 4; ++k) { v[2 * k] = bf_lo(xv[k]); v[2 * k + 1] = bf_hi(xv[k]); }
    float sum = ((v[0] + v[1]) + (v[2] + v[3])) + ((v[4] + v[5]) + (v[6] + v[7]));
    const float mean = wave_sum(sum) * (1.0f / 512);
    float sq0 = 0.f, sq1 = 0.f;
#pragma unroll
    for (int k = 0; k < 4; ++k) { float d = v[k] - mean, e = v[k + 4] - mean; sq0 += d * d; sq1 += e * e; }
    const float var = wave_sum(sq0 + sq1) * (1.0f / 512);
    const float mb = rnd_bf(mean);
    const float sd = rnd_bf(__builtin_amdgcn_sqrtf(rnd_bf(rnd_bf(var) + 1e-5f)));
    const float rs = __builtin_amdgcn_rcpf(sd);
    if (lane == 0) st_sc1_2(rw, WS_ST + (b + 256 * w) * 8, u32x2{pk_bf16(mb, sd), tag});
    u32x4 xh;
#pragma unroll
    for (int k = 0; k < 4; ++k) {
      u32 dp = pk_bf16(v[2 * k] - mb, v[2 * k + 1] - mb);
      u32 np = pk_bf16(bf_lo(dp) * rs, bf_hi(dp) * rs);
      xh[k] = pk_f16(bf_lo(np), bf_hi(np));
    }
    *(LAS u32x4*)(img + ((lane ^ ((t & 1) << 1)) << 4)) = xh;
    __builtin_amdgcn_fence(__ATOMIC_RELEASE, "wavefront");
    __builtin_amdgcn_wave_barrier();
    f32x4 d1re[2], d1im[2];
#pragma unroll
    for (int hh = 0; hh < 2; ++hh) {
      u32x2 a = tr_read(img + (4 * t + (p16 >> 2)) * 64 + ((hh ^ (t & 1)) << 5) + (p16 & 3) * 8);
      d1re[hh] = mfma16_f16(a, b1re, z4);
      d1im[hh] = mfma16_f16(a, b1im, z4);
    }
    float ny = ((d1re[0][0] - d1re[0][1]) + (d1re[0][2] - d1re[0][3])) + ((d1re[1][0] - d1re[1][1]) + (d1re[1][2] - d1re[1][3]));
    f32x4 d2re = z4, d2im = z4;
#pragma unroll
    for (int hh = 0; hh < 2; ++hh) {
      u32x4 a;
#pragma unroll
      for (int r = 0; r < 4; ++r)
        a[r] = pk_f16(d1re[hh][r] * twc[hh][r] + d1im[hh][r] * tws[hh][r], d1im[hh][r] * twc[hh][r] - d1re[hh][r] * tws[hh][r]);
      d2re = mfma32_f16(a, b2re[hh], d2re);
      d2im = mfma32_f16(a, b2im[hh], d2im);
    }
    {
      int nb = __builtin_bit_cast(int, ny);
      float n1_ = __builtin_bit_cast(float, __builtin_amdgcn_readlane(nb, 16));
      float n2_ = __builtin_bit_cast(float, __builtin_amdgcn_readlane(nb, 32));
      float n3_ = __builtin_bit_cast(float, __builtin_amdgcn_readlane(nb, 48));
      float tot = 0.125f * ((ny + n1_) + (n2_ + n3_));
      if (lane == 0) d2im[0] = tot;
    }
    LAS char* ex = sm + FS + 8192;
    {
      u32x4 o;
#pragma unroll
      for (int r = 0; r < 4; ++r) o[r] = pk_f16(d2re[r], d2im[r]);
      int e = 4 * p16 + t; e ^= (e >> 3) & 3;
      *(LAS u32x4*)(ex + w * 1024 + e * 16) = o;
    }
    BAR();
    {
      const int wq = w & 3, hf = w >> 2;
      int e = 16 * wq + p16; e ^= (e >> 3) & 3;
      u32x4 r0 = *(LAS u32x4*)(ex + (2 * t) * 1024 + e * 16);
      u32x4 r1 = *(LAS u32x4*)(ex + (2 * t + 1) * 1024 + e * 16);
      u32 o[4];
#pragma unroll
      for (int u = 0; u < 4; ++u) {
        f32x4 d = mfma16_f16(a8, u32x2{r0[u], r1[u]}, z4);
        if (u == 0 && wq == 0) {
          float e0 = po ? -s0 : c0, e1 = po ? -s1 : c1;
          u32x2 as = (jp & 3) == 0 ? a8 : (jp < 4 ? u32x2{pk_f16(e0, 0.f), pk_f16(e1, 0.f)} : u32x2{pk_f16(0.f, e0), pk_f16(0.f, e1)});
          f32x4 ds = mfma16_f16(as, u32x2{r0[0], r1[0]}, z4);
          if (p16 == 0) d = ds;
        }
        o[u] = hf ? pk_f16(d[2], d[3]) : pk_f16(d[0], d[1]);
      }
      const u32 ub = WS_U + b * 16384 + (2 * t + hf) * 2048 + (64 * wq + 4 * p16) * 8;
      st_sc1(rw, ub, u32x4{o[0], tag, o[1], tag});
      st_sc1(rw, ub + 16, u32x4{o[2], tag, o[3], tag});
    }
  }
  STAMP(2);

  if (is_gemm) {
    const int wr_ = w >> 1, wt = w & 1, h = lane >> 5;
    int xoff[4], woff[2];
#pragma unroll
    for (int ks = 0; ks < 4; ++ks) { int tok = wt * 64 + (lane & 31); xoff[ks] = tok * 128 + (((2 * ks + h) ^ ((tok >> 1) & 7)) << 4); }
    {
      int arr = t & 1, q = p16 >> 2, p = lane & 3;
#pragma unroll
      for (int m = 0; m < 2; ++m)
        woff[m] = 16384 + arr * 16384 + (8 * h + q) * 256 + (((wr_ * 2 + m) ^ (q | (arr << 2))) << 5) + (p & 1) * 16 + (p >> 1) * 8;
    }
    auto rd = [&](Frag& f, int st, int ks) {
      LAS char* buf = sm + (st % 3) * G1_STAGE;
#pragma unroll
      for (int n = 0; n < 2; ++n) f.bx[n] = *(LAS u32x4*)(buf + xoff[ks] + n * 4096);
#pragma unroll
      for (int m = 0; m < 2; ++m) {
        u32x2 lo = tr_read(buf + woff[m] + ks * 4096), hi = tr_read(buf + woff[m] + ks * 4096 + 1024);
        f.aw[m] = u32x4{lo[0], lo[1], hi[0], hi[1]};
      }
    };
    f32x16 acc[2][2];
#pragma unroll
    for (int m = 0; m < 2; ++m)
#pragma unroll
      for (int n = 0; n < 2; ++n)
#pragma unroll
        for (int k = 0; k < 16; ++k) acc[m][n][k] = 0.f;
    Frag fa, fb;
    auto mm = [&](Frag& f, int ls, int k0) {
      acc[0][0] = mfma3232(f.aw[0], f.bx[0], acc[0][0]);
      SB(); if (ls < 8 && k0 >= 0) g1_issue1(L, ls, k0); SB();
      acc[0][1] = mfma3232(f.aw[0], f.bx[1], acc[0][1]);
      acc[1][1] = mfma3232(f.aw[1], f.bx[1], acc[1][1]);
      SB(); if (ls < 8 && k0 >= 0) g1_issue1(L, ls, k0 + 1); SB();
      acc[1][0] = mfma3232(f.aw[1], f.bx[0], acc[1][0]);
    };
    SB();
    WAITV(12 + 3);
    __builtin_amdgcn_s_barrier();
    STAMP(3);
    rd(fa, 0, 0);
#pragma unroll
    for (int st = 0; st < 8; ++st) {
      const int ls = st == 0 ? 8 : st + 2;
      SB(); rd(fb, st, 1); SB(); mm(fa, ls, 2);
      SB(); rd(fa, st, 2); SB(); mm(fb, ls, 4);
      SB(); rd(fb, st, 3); SB(); mm(fa, ls, -1);
      SB();
      if (st < 7) {
        if (st < 6) WAITV(6); else WAITV(0);
        BAR();
        SB();
        rd(fa, st + 1, 0);
        SB();
      }
      mm(fb, st + 3, 0);
      SB();
    }
    STAMP(4);
    u32x2 stv[2]; u32x4 cs[2][2][4];
    for (int spin = 0; spin < SPIN_CAP; ++spin) {
      asm volatile("" ::: "memory");
      bool ok = true;
#pragma unroll
      for (int n = 0; n < 2; ++n) { stv[n] = ld_sc1_2(rw, WS_ST + (mi * 128 + wt * 64 + n * 32 + (lane & 31)) * 8); }
#pragma unroll
      for (int m = 0; m < 2; ++m)
#pragma unroll
        for (int a = 0; a < 2; ++a)
#pragma unroll
          for (int k = 0; k < 4; ++k) cs[m][a][k] = ld_sc1(rw, WS_CS + (a * F_ + fj * 128 + (wr_ * 2 + m) * 16 + 8 * h + 2 * k) * 8);
#pragma unroll
      for (int n = 0; n < 2; ++n) ok &= stv[n][1] == tag;
#pragma unroll
      for (int m = 0; m < 2; ++m)
#pragma unroll
        for (int a = 0; a < 2; ++a)
#pragma unroll
          for (int k = 0; k < 4; ++k) ok &= (cs[m][a][k][1] == tag) & (cs[m][a][k][3] == tag);
      if (all_ok(ok)) break;
    }
    STAMP(5);
#pragma unroll
    for (int n = 0; n < 2; ++n) {
      const int tok = mi * 128 + wt * 64 + n * 32 + (lane & 31);
      const float nm = -bf_lo(stv[n][0]);
      const float rs = __builtin_amdgcn_rcpf(bf_hi(stv[n][0])), rs2 = rs * rs;
#pragma unroll
      for (int m = 0; m < 2; ++m) {
        float hv[8];
#pragma unroll
        for (int k = 0; k < 8; ++k) {
          const u32 cgu = cs[m][0][k >> 1][(k & 1) * 2], cuu = cs[m][1][k >> 1][(k & 1) * 2];
          float cg = __builtin_bit_cast(float, cgu), cu = __builtin_bit_cast(float, cuu);
          float gg = __builtin_fmaf(nm, cg, acc[m][n][k]), uu = __builtin_fmaf(nm, cu, acc[m][n][8 + k]);
          hv[k] = __builtin_fmaxf(gg, 0.f) * (uu * rs2);
        }
        const int fo = fj * 16 + (wr_ * 2 + m) * 2 + h;
        st_sc1(rw, WS_H + ((tok >> 4) * 56 + (fo >> 2)) * 1024 + (fo & 3) * 256 + (tok & 15) * 16,
               u32x4{pk_bf16(hv[0], hv[1]), pk_bf16(hv[2], hv[3]), pk_bf16(hv[4], hv[5]), pk_bf16(hv[6], hv[7])});
      }
    }
    STAMP(6);
    WAITV(0);
    BAR();
    if (tid == 0) __builtin_amdgcn_raw_buffer_store_b32(tag, rw, WS_F2 + mi * 128 + fj * 4, 0, 16);
    STAMP(7);
  } else if (b < 28) {
    const char* wsrc = b < 14 ? wg : wu;
    const int ct = b < 14 ? b : b - 14;
    const int r0 = tid >> 4;
    float acc[8] = {0, 0, 0, 0, 0, 0, 0, 0};
    u32x4 v[16];
#pragma unroll
    for (int k = 0; k < 16; ++k) v[k] = *(const u32x4*)(wsrc + (size_t)(r0 + 32 * k) * 3584 + ct * 256 + p16 * 16);
#pragma unroll
    for (int k = 0; k < 16; ++k)
#pragma unroll
      for (int q = 0; q < 4; ++q) { acc[2 * q] += bf_lo(v[k][q]); acc[2 * q + 1] += bf_hi(v[k][q]); }
    LAS float* red = (LAS float*)sm;
    *(LAS f32x4*)(red + r0 * 128 + p16 * 8) = f32x4{acc[0], acc[1], acc[2], acc[3]};
    *(LAS f32x4*)(red + r0 * 128 + p16 * 8 + 4) = f32x4{acc[4], acc[5], acc[6], acc[7]};
    BAR();
    if (tid < 128) {
      float sacc = 0.f;
#pragma unroll
      for (int k = 0; k < 32; ++k) sacc += red[k * 128 + tid];
      st_sc1_2(rw, WS_CS + (b * 128 + tid) * 8, u32x2{__builtin_bit_cast(u32, sacc), tag});
    }
    BAR();
  }

  {
    const int jp = b & 7, g = b >> 3;
    LAS char* cb = sm;
    LAS char* fb = sm + 8 * 1040;
    {
      int q = tid & 255, arr = tid >> 8;
      const unsigned short* wsrc = arr ? wi : wr;
      int e0 = (8 * q + jp) * DF_ + 8 * g;
      const u32* pa = (const u32*)(wsrc + (e0 & ~1));
      u32 d0 = pa[0], d1 = pa[1], d2 = pa[2], d3 = pa[3], d4 = pa[4];
      const int i = tid >> 1, hf = tid & 1;
      u32x4 u0, u1;
      for (int spin = 0; spin < SPIN_CAP; ++spin) {
      asm volatile("" ::: "memory");
        const u32 off = WS_U + i * 16384 + jp * 2048 + g * 64 + hf * 32;
        u0 = ld_sc1(rw, off); u1 = ld_sc1(rw, off + 16);
        if (all_ok((u0[1] == tag) & (u0[3] == tag) & (u1[1] == tag) & (u1[3] == tag))) break;
      }
      const int pos = (i & 192) + 4 * (i & 15) + ((i >> 4) & 3);
      *(LAS u32*)(cb + (4 * hf + 0) * 1040 + pos * 4) = u0[0];
      *(LAS u32*)(cb + (4 * hf + 1) * 1040 + pos * 4) = u0[2];
      *(LAS u32*)(cb + (4 * hf + 2) * 1040 + pos * 4) = u1[0];
      *(LAS u32*)(cb + (4 * hf + 3) * 1040 + pos * 4) = u1[2];
      u32 sh = (e0 & 1) * 16;
      u32 f[4] = {__builtin_amdgcn_alignbit(d1, d0, sh), __builtin_amdgcn_alignbit(d2, d1, sh), __builtin_amdgcn_alignbit(d3, d2, sh), __builtin_amdgcn_alignbit(d4, d3, sh)};
      int posq = (q & 192) + 4 * (q & 15) + ((q >> 4) & 3);
#pragma unroll
      for (int k = 0; k < 4; ++k) {
        *(LAS unsigned short*)(fb + (2 * k) * 1040 + posq * 4 + arr * 2) = (unsigned short)(f[k] & 0xffff);
        *(LAS unsigned short*)(fb + (2 * k + 1) * 1040 + posq * 4 + arr * 2) = (unsigned short)(f[k] >> 16);
      }
    }
    STAMP(8);
    u32x4 bc1re, bc1im, ac2re, ac2im, bc3re, bc3im, ac4re, ac4im;
    float t1c[4], t1s[4], t2c[4], t2s[4];
    const float FSC = (2.0f / (2048.0f * 512.0f)) * (4.0f * 8.0f * 2.0f * 4.0f * 4.0f);
#pragma unroll
    for (int r = 0; r < 4; ++r) {
      const int y = 4 * t + r;
      const float n1 = (float)(y * (8 * p16 + jp)), n2 = (float)(p16 * (8 * y + jp));
      { float cc = 0.25f * cosr(n1 * (1.0f / 128)), sn = 0.25f * sinr(n1 * (1.0f / 128)); bc1re[r] = pk_f16(cc, sn); bc1im[r] = pk_f16(-sn, cc); }
      { float rev = (float)(y * p16) * (1.0f / 16); float cc = cosr(rev), sn = sinr(rev);
        ac2re[r] = pk_f16(0.25f * cc, 0.25f * sn); ac2im[r] = pk_f16(-0.25f * sn, 0.25f * cc);
        bc3re[r] = pk_bf16(cc, -sn); bc3im[r] = pk_bf16(sn, cc); }
      { float cc = cosr(n2 * (1.0f / 128)), sn = sinr(n2 * (1.0f / 128)); ac4re[r] = pk_bf16(cc, -sn); ac4im[r] = pk_bf16(sn, cc); }
      t1c[r] = cosr(n1 * (1.0f / 2048)); t1s[r] = sinr(n1 * (1.0f / 2048));
      t2c[r] = FSC * cosr(n2 * (1.0f / 2048)); t2s[r] = FSC * sinr(n2 * (1.0f / 2048));
    }
    BAR();
    const u32x4 uin = *(LAS u32x4*)(cb + w * 1040 + lane * 16);
    const u32x4 fin = *(LAS u32x4*)(fb + w * 1040 + lane * 16);
    const bool special = (g == 0 && w == 0);
    const int npass = (special && (jp & 3) == 0) ? 2 : 1;
    f32x4 ore = z4, oim = z4;
    for (int pass = 0; pass < npass; ++pass) {
      u32x4 a = uin;
      float fr[4], fi[4];
#pragma unroll
      for (int r = 0; r < 4; ++r) { fr[r] = bf_lo(fin[r]); fi[r] = bf_hi(fin[r]); }
      if (special) {
        const int col = (npass == 2) ? (pass ? 256 : 0) : (jp < 4 ? 0 : 256);
#pragma unroll
        for (int r = 0; r < 4; ++r) {
          int ks = 8 * (p16 + 64 * t + 16 * r) + jp, km = (2048 - ks) & 2047;
          float ar = bf_lo(wr[ks * DF_ + col]), ai = bf_lo(wi[ks * DF_ + col]);
          float br = bf_lo(wr[km * DF_ + col]), bi = bf_lo(wi[km * DF_ + col]);
          fr[r] = 0.5f * (ar + br); fi[r] = 0.5f * (ai - bi);
          if (npass == 2) a[r] = pass ? (uin[r] >> 16) : (uin[r] & 0xffffu);
        }
      }
      f32x4 dre = mfma32_f16(a, bc1re, z4), dim = mfma32_f16(a, bc1im, z4);
#pragma unroll
      for (int r = 0; r < 4; ++r) a[r] = pk_f16(dre[r] * t1c[r] + dim[r] * t1s[r], dim[r] * t1c[r] - dre[r] * t1s[r]);
      dre = mfma32_f16(ac2re, a, z4); dim = mfma32_f16(ac2im, a, z4);
#pragma unroll
      for (int r = 0; r < 4; ++r) a[r] = pk_bf16(dre[r] * fr[r] - dim[r] * fi[r], dre[r] * fi[r] + dim[r] * fr[r]);
      dre = mfma32_bf16(a, bc3re, z4); dim = mfma32_bf16(a, bc3im, z4);
#pragma unroll
      for (int r = 0; r < 4; ++r) a[r] = pk_bf16(dre[r] * t2c[r] - dim[r] * t2s[r], dre[r] * t2s[r] + dim[r] * t2c[r]);
      dre = mfma32_bf16(ac4re, a, z4); dim = mfma32_bf16(ac4im, a, z4);
      if (npass == 2) { if (pass == 0) ore = dre; else oim = dre; }
      else { ore = dre; oim = dim; }
    }
    {
      u32x4 o;
#pragma unroll
      for (int r = 0; r < 4; ++r) o[r] = pk_bf16(ore[r], oim[r]);
      *(LAS u32x4*)(cb + w * 1040 + lane * 16) = o;
    }
    BAR();
    {
      int ps = tid >> 1, hf = tid & 1;
      int i = (ps >> 5) + 8 * (ps & 31);
      int pos = (i & 192) + 4 * (i & 15) + ((i >> 4) & 3);
      u32 o[4];
#pragma unroll
      for (int k = 0; k < 4; ++k) o[k] = *(LAS u32*)(cb + (4 * hf + k) * 1040 + pos * 4);
      st_sc1(rw, WS_V + b * 16384 + tid * 32, u32x4{o[0], tag, o[1], tag});
      st_sc1(rw, WS_V + b * 16384 + tid * 32 + 16, u32x4{o[2], tag, o[3], tag});
    }
  }
  STAMP(9);

  const int rt = (b & 7) * 4 + ((b >> 3) & 3), ct = b >> 5;
  u32x4 wf[7][4];
  {
    const char* src = wd + (size_t)(w * 224 + 8 * t) * 1024 + ct * 128 + p16 * 8;
    u32x2 rwv[7][8];
#pragma unroll
    for (int ks = 0; ks < 7; ++ks)
#pragma unroll
      for (int j = 0; j < 8; ++j) rwv[ks][j] = *(const u32x2*)(src + (size_t)(ks * 32 + j) * 1024);
    SB();
#pragma unroll
    for (int ks = 0; ks < 7; ++ks)
#pragma unroll
      for (int kp = 0; kp < 4; ++kp) {
        wf[ks][0][kp] = __builtin_amdgcn_perm(rwv[ks][2 * kp + 1][0], rwv[ks][2 * kp][0], 0x05040100u);
        wf[ks][1][kp] = __builtin_amdgcn_perm(rwv[ks][2 * kp + 1][0], rwv[ks][2 * kp][0], 0x07060302u);
        wf[ks][2][kp] = __builtin_amdgcn_perm(rwv[ks][2 * kp + 1][1], rwv[ks][2 * kp][1], 0x05040100u);
        wf[ks][3][kp] = __builtin_amdgcn_perm(rwv[ks][2 * kp + 1][1], rwv[ks][2 * kp][1], 0x07060302u);
      }
    SB();
  }
  STAMP(10);

  {
    LAS char* yb = sm + FS;
    const int s = b + 256 * w;
    const u32x4 xv = *(const u32x4*)(x + (size_t)s * 1024 + (4 * p16 + t) * 16);
    {
      const int g = 4 * w + (p16 >> 2);
      const u32 off = WS_V + ((b & 7) * 32 + (b >> 3)) * 64 + (p16 & 3) * 16 + (g * 8 + 2 * t) * 16384;
      const int j = p16 >> 1, po = p16 & 1;
      float c0, s0, c1, s1;
      { float rev = (float)(2 * t * j) * 0.125f; c0 = cosr(rev); s0 = sinr(rev); }
      { float rev = (float)((2 * t + 1) * j) * 0.125f; c1 = cosr(rev); s1 = sinr(rev); }
      const u32x2 a8 = po ? u32x2{pk_bf16(s0, c0), pk_bf16(s1, c1)} : u32x2{pk_bf16(c0, -s0), pk_bf16(c1, -s1)};
      u32x4 r0, r1;
      for (int spin = 0; spin < SPIN_CAP; ++spin) {
      asm volatile("" ::: "memory");
        r0 = ld_sc1(rw, off); r1 = ld_sc1(rw, off + 16384);
        if (all_ok((r0[1] == tag) & (r0[3] == tag) & (r1[1] == tag) & (r1[3] == tag))) break;
      }
      STAMP(11);
      u32x2 o0, o1;
#pragma unroll
      for (int u = 0; u < 2; ++u) {
        f32x4 d = mfma16_bf16(a8, u32x2{r0[2 * u], r1[2 * u]}, z4);
        if (u == 0 && w == 0) {
          u32 e0, e1;
          const bool on = (po == 0) ? (t < 2) : (t >= 2);
          if ((t & 1) == 0) e0 = po ? pk_bf16(0.f, 0.5f * c0) : pk_bf16(0.5f * c0, 0.f);
          else e0 = on ? pk_bf16(c0, -s0) : 0u;
          e1 = on ? pk_bf16(c1, -s1) : 0u;
          f32x4 ds = mfma16_bf16(u32x2{e0, e1}, u32x2{r0[0], r1[0]}, z4);
          if (p16 == 0) d = ds;
        }
        o0[u] = pk_bf16(d[0], d[1]); o1[u] = pk_bf16(d[2], d[3]);
      }
      *(LAS u32x2*)(yb + (2 * t) * 1024 + (16 * w + p16) * 8) = o0;
      *(LAS u32x2*)(yb + (2 * t + 1) * 1024 + (16 * w + p16) * 8) = o1;
    }
    u32x4 bi1re[2], bi1im[2], bi2;
    float twc[2][4], tws[2][4];
#pragma unroll
    for (int hh = 0; hh < 2; ++hh) {
      const int n2 = 8 * (p16 >> 2) + 4 * hh + (p16 & 3);
#pragma unroll
      for (int m = 0; m < 4; ++m) {
        float rev = (float)(n2 * (t + 4 * m)) * (1.0f / 32); float cc = cosr(rev), sn = sinr(rev);
        bi1re[hh][m] = pk_bf16(cc, -sn); bi1im[hh][m] = pk_bf16(sn, cc);
        float rev2 = (float)(n2 * (4 * t + m)) * (1.0f / 512);
        twc[hh][m] = cosr(rev2); tws[hh][m] = sinr(rev2);
      }
    }
#pragma unroll
    for (int r = 0; r < 4; ++r) { float rev = (float)(p16 * (4 * t + r)) * (1.0f / 16); bi2[r] = pk_bf16(cosr(rev), -sinr(rev)); }
    BAR();
    u32x4 a;
#pragma unroll
    for (int m = 0; m < 4; ++m) a[m] = *(LAS u32*)(yb + w * 1024 + (lane + 64 * m) * 4);
    const float nyq = bf_hi(*(LAS u32*)(yb + w * 1024));
    u32 ov[4];
#pragma unroll
    for (int hh = 0; hh < 2; ++hh) {
      f32x4 gre = mfma32_bf16(a, bi1re[hh], z4), gim = mfma32_bf16(a, bi1im[hh], z4);
      u32x4 bb;
#pragma unroll
      for (int r = 0; r < 4; ++r) bb[r] = pk_bf16(gre[r] * twc[hh][r] - gim[r] * tws[hh][r], gre[r] * tws[hh][r] + gim[r] * twc[hh][r]);
      f32x4 d = mfma32_bf16(bb, bi2, f32x4{nyq, -nyq, nyq, -nyq});
      ov[2 * hh] = pk_bf16(bf_lo(xv[2 * hh]) + d[0], bf_hi(xv[2 * hh]) + d[1]);
      ov[2 * hh + 1] = pk_bf16(bf_lo(xv[2 * hh + 1]) + d[2], bf_hi(xv[2 * hh + 1]) + d[3]);
    }
    st_sc1(rw, WS_R + s * 2048 + (4 * p16 + t) * 32, u32x4{ov[0], tag, ov[1], tag});
    st_sc1(rw, WS_R + s * 2048 + (4 * p16 + t) * 32 + 16, u32x4{ov[2], tag, ov[3], tag});
  }
  STAMP(12);

  {
    for (int spin = 0; spin < SPIN_CAP; ++spin) {
      asm volatile("" ::: "memory");
      u32 fl = __builtin_amdgcn_raw_buffer_load_b32(rw, WS_F2 + (rt >> 1) * 128 + (lane < 14 ? lane : 0) * 4, 0, 16);
      if (all_ok(fl == tag)) break;
    }
    __builtin_amdgcn_fence(__ATOMIC_ACQUIRE, "wavefront");
    STAMP(13);
    const u32 hs = WS_H + ((rt * 4) * 56 + w * 7) * 1024 + lane * 16;
    u32x4 hf[4][7];
#pragma unroll
    for (int tt = 0; tt < 3; ++tt)
#pragma unroll
      for (int ks = 0; ks < 7; ++ks) hf[tt][ks] = ld_sc1(rw, hs + (tt * 56 + ks) * 1024);
    SB();
#pragma unroll
    for (int tt = 0; tt < 4; ++tt) {
      f32x4 acc[4] = {z4, z4, z4, z4};
#pragma unroll
      for (int ks = 0; ks < 7; ++ks) {
#pragma unroll
        for (int u = 0; u < 4; ++u) acc[u] = mfma32_bf16(wf[ks][u], hf[tt][ks], acc[u]);
        if (tt == 0) { SB(); hf[3][ks] = ld_sc1(rw, hs + (3 * 56 + ks) * 1024); SB(); }
      }
#pragma unroll
      for (int u = 0; u < 4; ++u) *(LAS f32x4*)(sm + w * 16384 + (tt * 4 + u) * 1024 + lane * 16) = acc[u];
      SB();
    }
    STAMP(14);
    const int tt = w >> 1, hh = w & 1;
    const int row = rt * 64 + tt * 16 + p16;
    u32x4 r0, r1;
    for (int spin = 0; spin < SPIN_CAP; ++spin) {
      asm volatile("" ::: "memory");
      const u32 off = WS_R + row * 2048 + ct * 256 + t * 64 + hh * 32;
      r0 = ld_sc1(rw, off); r1 = ld_sc1(rw, off + 16);
      if (all_ok((r0[1] == tag) & (r0[3] == tag) & (r1[1] == tag) & (r1[3] == tag))) break;
    }
    BAR();
    f32x2 sacc[4];
#pragma unroll
    for (int u = 0; u < 4; ++u) {
      sacc[u] = f32x2{0.f, 0.f};
#pragma unroll
      for (int pw = 0; pw < 8; ++pw) sacc[u] += *(LAS f32x2*)(sm + pw * 16384 + (tt * 4 + u) * 1024 + lane * 16 + hh * 8);
    }
    u32x4 o;
    o[0] = pk_bf16(bf_lo(r0[0]) + sacc[0][0], bf_hi(r0[0]) + sacc[1][0]);
    o[1] = pk_bf16(bf_lo(r0[2]) + sacc[2][0], bf_hi(r0[2]) + sacc[3][0]);
    o[2] = pk_bf16(bf_lo(r1[0]) + sacc[0][1], bf_hi(r1[0]) + sacc[1][1]);
    o[3] = pk_bf16(bf_lo(r1[2]) + sacc[2][1], bf_hi(r1[2]) + sacc[3][1]);
    *(u32x4*)(out + (size_t)row * 1024 + ct * 128 + t * 32 + hh * 16) = o;
    if (b == 0 && tid == 0) __builtin_amdgcn_raw_buffer_store_b32(tag, rw, WS_EPOCH, 0, 16);
  }
  if (b == 0 && w == 0) {
    const rsrc_t rb = __builtin_amdgcn_make_buffer_rsrc((void*)(ws + (128u << 20)), 0, 8192, 0x00020000);
    u32 off = 0;
    for (int k = 0; k < 160; ++k) { asm volatile("" ::: "memory"); u32 v = __builtin_amdgcn_raw_buffer_load_b32(rb, off, 0, 16); off = (off + 128 + (v == 0x13572468u ? 4 : 0)) & 4095; }
    if (off == 0x7fffffffu) ws[100] = 1;
  }
}

extern "C" void kernel_launch(void* const* d_in, const int* in_sizes, int n_in, void* d_out, int out_size, void* d_ws, size_t ws_size,
                              hipStream_t stream) {
  (void)hipFuncSetAttribute((const void*)fused_kernel, hipFuncAttributeMaxDynamicSharedMemorySize, LDS_BYTES);
  fused_kernel<<<256, 512, LDS_BYTES, stream>>>((const char*)d_in[0], (const unsigned short*)d_in[1], (const unsigned short*)d_in[2], (const char*)d_in[4],
                                                (const char*)d_in[5], (const char*)d_in[6], (char*)d_ws, (char*)d_out, (const char*)d_in[3]);
}
```

```cpp
#define PROBE 0
#define PROBE_WG 100
#include <hip/hip_runtime.h>
#include <stdint.h>

#define LAS __attribute__((address_space(3)))
#define GAS __attribute__((address_space(1)))
#define DI __device__ __forceinline__
typedef unsigned u32;
typedef unsigned u32x2 __attribute__((ext_vector_type(2)));
typedef unsigned u32x4 __attribute__((ext_vector_type(4)));
typedef float f32x2 __attribute__((ext_vector_type(2)));
typedef float f32x4 __attribute__((ext_vector_type(4)));
typedef float f32x16 __attribute__((ext_vector_type(16)));
typedef short s16x4 __attribute__((ext_vector_type(4)));
typedef _Float16 h16x2 __attribute__((ext_vector_type(2)));
typedef _Float16 h16x4 __attribute__((ext_vector_type(4)));
typedef _Float16 h16x8 __attribute__((ext_vector_type(8)));
typedef __bf16 b16x2 __attribute__((ext_vector_type(2)));
typedef __bf16 b16x8 __attribute__((ext_vector_type(8)));
typedef __amdgpu_buffer_rsrc_t rsrc_t;

constexpr int F_ = 1792, DF_ = 257;
constexpr u32 WS_EPOCH = 0;
constexpr u32 WS_F2 = 4096;
constexpr u32 WS_ST = 8192;
constexpr u32 WS_CS = WS_ST + 16384;
constexpr u32 WS_U = 65536;
constexpr u32 WS_V = WS_U + 4194304;
constexpr u32 WS_R = WS_V + 4194304;
constexpr u32 WS_H = WS_R + 4194304;
constexpr u32 WS_END = WS_H + 7340032;
constexpr int LDS_BYTES = 163840;
constexpr int G1_STAGE = 49152;
constexpr int FS = 3 * G1_STAGE;
constexpr int SPIN_CAP = 1 << 17;
constexpr size_t WS_DBG = 64 << 20;
#define STAMP(k) do { if ((k) == 0) pr_t0 = __builtin_amdgcn_s_memrealtime(); if ((k) == PROBE) pr_tk = __builtin_amdgcn_s_memrealtime(); } while (0)
#define PROBE_END() do { if (blockIdx.x == PROBE_WG && threadIdx.x < 64) { while (__builtin_amdgcn_s_memrealtime() < pr_tk + 6000) __builtin_amdgcn_s_sleep(1); } } while (0)

DI u32 pk_f16(float a, float b) { h16x2 p = {(_Float16)a, (_Float16)b}; return __builtin_bit_cast(u32, p); }
DI u32 pk_bf16(float a, float b) { b16x2 p = {(__bf16)a, (__bf16)b}; return __builtin_bit_cast(u32, p); }
DI float bf_lo(u32 v) { return __builtin_bit_cast(float, v << 16); }
DI float bf_hi(u32 v) { return __builtin_bit_cast(float, v & 0xffff0000u); }
DI float rnd_bf(float a) { return bf_lo(pk_bf16(a, a)); }
DI float cosr(float rev) { return __builtin_amdgcn_cosf(rev); }
DI float sinr(float rev) { return __builtin_amdgcn_sinf(rev); }
DI f32x4 mfma16_f16(u32x2 a, u32x2 b, f32x4 c) { return __builtin_amdgcn_mfma_f32_16x16x16f16(__builtin_bit_cast(h16x4, a), __builtin_bit_cast(h16x4, b), c, 0, 0, 0); }
DI f32x4 mfma16_bf16(u32x2 a, u32x2 b, f32x4 c) { return __builtin_amdgcn_mfma_f32_16x16x16bf16_1k(__builtin_bit_cast(s16x4, a), __builtin_bit_cast(s16x4, b), c, 0, 0, 0); }
DI f32x4 mfma32_f16(u32x4 a, u32x4 b, f32x4 c) { return __builtin_amdgcn_mfma_f32_16x16x32_f16(__builtin_bit_cast(h16x8, a), __builtin_bit_cast(h16x8, b), c, 0, 0, 0); }
DI f32x4 mfma32_bf16(u32x4 a, u32x4 b, f32x4 c) { return __builtin_amdgcn_mfma_f32_16x16x32_bf16(__builtin_bit_cast(b16x8, a), __builtin_bit_cast(b16x8, b), c, 0, 0, 0); }
DI f32x16 mfma3232(u32x4 a, u32x4 b, f32x16 c) { return __builtin_amdgcn_mfma_f32_32x32x16_bf16(__builtin_bit_cast(b16x8, a), __builtin_bit_cast(b16x8, b), c, 0, 0, 0); }
DI u32x2 tr_read(LAS char* p) { return __builtin_bit_cast(u32x2, __builtin_amdgcn_ds_read_tr16_b64_v4i16((LAS s16x4*)p)); }
#define SB() __builtin_amdgcn_sched_barrier(0)
#define BAR() asm volatile("s_waitcnt lgkmcnt(0)\n\ts_barrier" ::: "memory")
#define WAITV(n) asm volatile("s_waitcnt vmcnt(%0)" :: "i"(n) : "memory")
DI u32x4 mk_srd(const void* p, u32 bytes) { uint64_t a = (uint64_t)p; return u32x4{(u32)a, (u32)(a >> 32), bytes, 0x00020000u}; }
DI void glds16(u32 ldsaddr, u32 voff, u32x4 srd, u32 soff) {
  asm volatile("s_mov_b32 m0, %0\n\ts_nop 0\n\tbuffer_load_dwordx4 %1, %2, %3 offen lds" :: "s"(ldsaddr), "v"(voff), "s"(srd), "s"(soff) : "memory");
}
DI u32x4 ld_sc1(rsrc_t r, u32 off) { return __builtin_amdgcn_raw_buffer_load_b128(r, off, 0, 16); }
DI u32x2 ld_sc1_2(rsrc_t r, u32 off) { return __builtin_amdgcn_raw_buffer_load_b64(r, off, 0, 16); }
DI void st_sc1(rsrc_t r, u32 off, u32x4 v) { __builtin_amdgcn_raw_buffer_store_b128(v, r, off, 0, 16); }
DI void st_sc1_2(rsrc_t r, u32 off, u32x2 v) { __builtin_amdgcn_raw_buffer_store_b64(v, r, off, 0, 16); }
DI bool all_ok(bool ok) { return __builtin_amdgcn_ballot_w64(!ok) == 0; }

template <int CTRL> DI float dpp_add(float v) {
  int o = __builtin_amdgcn_update_dpp(0, __builtin_bit_cast(int, v), CTRL, 0xf, 0xf, true);
  return v + __builtin_bit_cast(float, o);
}
DI float wave_sum(float v) {
  v = dpp_add<0xB1>(v); v = dpp_add<0x4E>(v); v = dpp_add<0x141>(v); v = dpp_add<0x140>(v);
  u32 u = __builtin_bit_cast(u32, v);
  auto a = __builtin_amdgcn_permlane16_swap(u, u, false, false);
  v = __builtin_bit_cast(float, (u32)a[0]) + __builtin_bit_cast(float, (u32)a[1]);
  u = __builtin_bit_cast(u32, v);
  auto b = __builtin_amdgcn_permlane32_swap(u, u, false, false);
  return __builtin_bit_cast(float, (u32)b[0]) + __builtin_bit_cast(float, (u32)b[1]);
}

struct G1L { u32x4 sx, sg, su; u32 vx, vg, vu, lw; };
DI void g1_issue1(const G1L& L, int st, int k) {
  const u32 lb = L.lw + (st % 3) * G1_STAGE + k * 8192;
  if (k < 2) glds16(lb, L.vx, L.sx, st * 128 + k * 65536);
  else if (k < 4) glds16(lb, L.vg, L.sg, (st * 64 + (k - 2) * 32) * 3584);
  else glds16(lb, L.vu, L.su, (st * 64 + (k - 4) * 32) * 3584);
}
struct Frag { u32x4 bx[2], aw[2]; };

__global__ __launch_bounds__(512, 2) void fused_kernel(const char* __restrict__ x, const unsigned short* __restrict__ wr, const unsigned short* __restrict__ wi,
                                                       const char* __restrict__ wg, const char* __restrict__ wu, const char* __restrict__ wd,
                                                       char* __restrict__ ws, char* __restrict__ out) {
  extern __shared__ __attribute__((aligned(16))) char smem[];
  LAS char* sm = (LAS char*)smem;
  const int b = blockIdx.x, tid = threadIdx.x, lane = tid & 63, w = __builtin_amdgcn_readfirstlane(tid >> 6);
  const int p16 = lane & 15, t = lane >> 4;
  const f32x4 z4 = {0.f, 0.f, 0.f, 0.f};
  u32 pfd = 0;
  unsigned long long pr_t0 = 0, pr_tk = 0;
  {
    uint64_t pc; asm volatile("s_getpc_b64 %0" : "=s"(pc));
    if (b < 8 && tid < CODE_LINES) asm volatile("global_load_dword %0, %1, %2" : "+v"(pfd) : "v"(tid * 128), "s"(pc) : "memory");
  }
  STAMP(0);
  const bool is_gemm = b >= 32;
  const int q_ = b - 32, xc = q_ & 7, r_ = q_ >> 3;
  const int mi = (xc >> 1) * 4 + (r_ & 3), fj = (xc & 1) * 7 + (r_ >> 2);
  G1L L;
  L.sx = mk_srd(x, 2097152); L.sg = mk_srd(wg, 1835008); L.su = mk_srd(wu, 1835008);
  L.lw = (u32)(uintptr_t)sm + w * 1024;
  glds16(L.lw + FS, (b + 256 * w) * 1024 + lane * 16, L.sx, 0);
  if (is_gemm) {
    { int row = w * 8 + (lane >> 3), pos = lane & 7; L.vx = (mi * 128 + row) * 1024 + ((pos ^ ((row >> 1) & 7)) << 4); }
    { int k = w * 4 + t; L.vg = k * 3584 + fj * 256 + ((p16 ^ (2 * (k & 3))) << 4); L.vu = L.vg ^ 128; }
#pragma unroll
    for (int st = 0; st < 3; ++st)
#pragma unroll
      for (int k = 0; k < 6; ++k) g1_issue1(L, st, k);
  }
  const rsrc_t rw = __builtin_amdgcn_make_buffer_rsrc(ws, 0, WS_END, 0x00020000);
  const u32 tag = *(const u32*)(ws + WS_EPOCH) * 0x9E3779B1u + 0x7F4A7C15u;

  {
    u32x2 b1re, b1im;
    {
      float c[4], sn[4];
#pragma unroll
      for (int jj = 0; jj < 4; ++jj) { float rev = (float)((4 * t + jj) * p16) * (1.0f / 16); c[jj] = 0.25f * cosr(rev); sn[jj] = -0.25f * sinr(rev); }
      b1re = u32x2{pk_f16(c[0], c[1]), pk_f16(c[2], c[3])};
      b1im = u32x2{pk_f16(sn[0], sn[1]), pk_f16(sn[2], sn[3])};
    }
    float twc[2][4], tws[2][4];
    u32x4 b2re[2], b2im[2];
#pragma unroll
    for (int hh = 0; hh < 2; ++hh)
#pragma unroll
      for (int r = 0; r < 4; ++r) {
        float nk = (float)((16 * hh + 4 * t + r) * p16);
        twc[hh][r] = cosr(nk * (1.0f / 512)); tws[hh][r] = sinr(nk * (1.0f / 512));
        float c = 0.125f * cosr(nk * (1.0f / 32)), sn = 0.125f * sinr(nk * (1.0f / 32));
        b2re[hh][r] = pk_f16(c, sn); b2im[hh][r] = pk_f16(-sn, c);
      }
    const int jp = p16 >> 1, po = p16 & 1;
    float c0, s0, c1, s1;
    { float rev = (float)(2 * t * jp) * 0.125f; c0 = 0.5f * cosr(rev); s0 = 0.5f * sinr(rev); }
    { float rev = (float)((2 * t + 1) * jp) * 0.125f; c1 = 0.5f * cosr(rev); s1 = 0.5f * sinr(rev); }
    const u32x2 a8 = po ? u32x2{pk_f16(-s0, c0), pk_f16(-s1, c1)} : u32x2{pk_f16(c0, s0), pk_f16(c1, s1)};
    LAS char* img = sm + FS + w * 1024;
    SB();
    if (is_gemm) WAITV(18); else WAITV(0);
    asm volatile("" :: "v"(pfd));
    STAMP(1);
    const u32x4 xv = *(LAS u32x4*)(img + lane * 16);
    float v[8];
#pragma unroll
    for (int k = 0; k < 4; ++k) { v[2 * k] = bf_lo(xv[k]); v[2 * k + 1] = bf_hi(xv[k]); }
    float sum = ((v[0] + v[1]) + (v[2] + v[3])) + ((v[4] + v[5]) + (v[6] + v[7]));
    const float mean = wave_sum(sum) * (1.0f / 512);
    float sq0 = 0.f, sq1 = 0.f;
#pragma unroll
    for (int k = 0; k < 4; ++k) { float d = v[k] - mean, e = v[k + 4] - mean; sq0 += d * d; sq1 += e * e; }
    const float var = wave_sum(sq0 + sq1) * (1.0f / 512);
    const float mb = rnd_bf(mean);
    const float sd = rnd_bf(__builtin_amdgcn_sqrtf(rnd_bf(rnd_bf(var) + 1e-5f)));
    const float rs = __builtin_amdgcn_rcpf(sd);
    if (lane == 0) st_sc1_2(rw, WS_ST + (b + 256 * w) * 8, u32x2{pk_bf16(mb, sd), tag});
    u32x4 xh;
#pragma unroll
    for (int k = 0; k < 4; ++k) {
      u32 dp = pk_bf16(v[2 * k] - mb, v[2 * k + 1] - mb);
      u32 np = pk_bf16(bf_lo(dp) * rs, bf_hi(dp) * rs);
      xh[k] = pk_f16(bf_lo(np), bf_hi(np));
    }
    *(LAS u32x4*)(img + ((lane ^ ((t & 1) << 1)) << 4)) = xh;
    __builtin_amdgcn_fence(__ATOMIC_RELEASE, "wavefront");
    __builtin_amdgcn_wave_barrier();
    f32x4 d1re[2], d1im[2];
#pragma unroll
    for (int hh = 0; hh < 2; ++hh) {
      u32x2 a = tr_read(img + (4 * t + (p16 >> 2)) * 64 + ((hh ^ (t & 1)) << 5) + (p16 & 3) * 8);
      d1re[hh] = mfma16_f16(a, b1re, z4);
      d1im[hh] = mfma16_f16(a, b1im, z4);
    }
    float ny = ((d1re[0][0] - d1re[0][1]) + (d1re[0][2] - d1re[0][3])) + ((d1re[1][0] - d1re[1][1]) + (d1re[1][2] - d1re[1][3]));
    f32x4 d2re = z4, d2im = z4;
#pragma unroll
    for (int hh = 0; hh < 2; ++hh) {
      u32x4 a;
#pragma unroll
      for (int r = 0; r < 4; ++r)
        a[r] = pk_f16(d1re[hh][r] * twc[hh][r] + d1im[hh][r] * tws[hh][r], d1im[hh][r] * twc[hh][r] - d1re[hh][r] * tws[hh][r]);
      d2re = mfma32_f16(a, b2re[hh], d2re);
      d2im = mfma32_f16(a, b2im[hh], d2im);
    }
    {
      int nb = __builtin_bit_cast(int, ny);
      float n1_ = __builtin_bit_cast(float, __builtin_amdgcn_readlane(nb, 16));
      float n2_ = __builtin_bit_cast(float, __builtin_amdgcn_readlane(nb, 32));
      float n3_ = __builtin_bit_cast(float, __builtin_amdgcn_readlane(nb, 48));
      float tot = 0.125f * ((ny + n1_) + (n2_ + n3_));
      if (lane == 0) d2im[0] = tot;
    }
    LAS char* ex = sm + FS + 8192;
    {
      u32x4 o;
#pragma unroll
      for (int r = 0; r < 4; ++r) o[r] = pk_f16(d2re[r], d2im[r]);
      int e = 4 * p16 + t; e ^= (e >> 3) & 3;
      *(LAS u32x4*)(ex + w * 1024 + e * 16) = o;
    }
    BAR();
    {
      const int wq = w & 3, hf = w >> 2;
      int e = 16 * wq + p16; e ^= (e >> 3) & 3;
      u32x4 r0 = *(LAS u32x4*)(ex + (2 * t) * 1024 + e * 16);
      u32x4 r1 = *(LAS u32x4*)(ex + (2 * t + 1) * 1024 + e * 16);
      u32 o[4];
#pragma unroll
      for (int u = 0; u < 4; ++u) {
        f32x4 d = mfma16_f16(a8, u32x2{r0[u], r1[u]}, z4);
        if (u == 0 && wq == 0) {
          float e0 = po ? -s0 : c0, e1 = po ? -s1 : c1;
          u32x2 as = (jp & 3) == 0 ? a8 : (jp < 4 ? u32x2{pk_f16(e0, 0.f), pk_f16(e1, 0.f)} : u32x2{pk_f16(0.f, e0), pk_f16(0.f, e1)});
          f32x4 ds = mfma16_f16(as, u32x2{r0[0], r1[0]}, z4);
          if (p16 == 0) d = ds;
        }
        o[u] = hf ? pk_f16(d[2], d[3]) : pk_f16(d[0], d[1]);
      }
      const u32 ub = WS_U + b * 16384 + (2 * t + hf) * 2048 + (64 * wq + 4 * p16) * 8;
      st_sc1(rw, ub, u32x4{o[0], tag, o[1], tag});
      st_sc1(rw, ub + 16, u32x4{o[2], tag, o[3], tag});
    }
  }
  STAMP(2);

  if (is_gemm) {
    const int wr_ = w >> 1, wt = w & 1, h = lane >> 5;
    int xoff[4], woff[2];
#pragma unroll
    for (int ks = 0; ks < 4; ++ks) { int tok = wt * 64 + (lane & 31); xoff[ks] = tok * 128 + (((2 * ks + h) ^ ((tok >> 1) & 7)) << 4); }
    {
      int arr = t & 1, q = p16 >> 2, p = lane & 3;
#pragma unroll
      for (int m = 0; m < 2; ++m)
        woff[m] = 16384 + arr * 16384 + (8 * h + q) * 256 + (((wr_ * 2 + m) ^ (q | (arr << 2))) << 5) + (p & 1) * 16 + (p >> 1) * 8;
    }
    auto rd = [&](Frag& f, int st, int ks) {
      LAS char* buf = sm + (st % 3) * G1_STAGE;
#pragma unroll
      for (int n = 0; n < 2; ++n) f.bx[n] = *(LAS u32x4*)(buf + xoff[ks] + n * 4096);
#pragma unroll
      for (int m = 0; m < 2; ++m) {
        u32x2 lo = tr_read(buf + woff[m] + ks * 4096), hi = tr_read(buf + woff[m] + ks * 4096 + 1024);
        f.aw[m] = u32x4{lo[0], lo[1], hi[0], hi[1]};
      }
    };
    f32x16 acc[2][2];
#pragma unroll
    for (int m = 0; m < 2; ++m)
#pragma unroll
      for (int n = 0; n < 2; ++n)
#pragma unroll
        for (int k = 0; k < 16; ++k) acc[m][n][k] = 0.f;
    Frag fa, fb;
    auto mm = [&](Frag& f, int ls, int k0) {
      acc[0][0] = mfma3232(f.aw[0], f.bx[0], acc[0][0]);
      SB(); if (ls < 8 && k0 >= 0) g1_issue1(L, ls, k0); SB();
      acc[0][1] = mfma3232(f.aw[0], f.bx[1], acc[0][1]);
      acc[1][1] = mfma3232(f.aw[1], f.bx[1], acc[1][1]);
      SB(); if (ls < 8 && k0 >= 0) g1_issue1(L, ls, k0 + 1); SB();
      acc[1][0] = mfma3232(f.aw[1], f.bx[0], acc[1][0]);
    };
    SB();
    WAITV(12 + 3);
    __builtin_amdgcn_s_barrier();
    STAMP(3);
    rd(fa, 0, 0);
#pragma unroll
    for (int st = 0; st < 8; ++st) {
      const int ls = st == 0 ? 8 : st + 2;
      SB(); rd(fb, st, 1); SB(); mm(fa, ls, 2);
      SB(); rd(fa, st, 2); SB(); mm(fb, ls, 4);
      SB(); rd(fb, st, 3); SB(); mm(fa, ls, -1);
      SB();
      if (st < 7) {
        if (st < 6) WAITV(6); else WAITV(0);
        BAR();
        SB();
        rd(fa, st + 1, 0);
        SB();
      }
      mm(fb, st + 3, 0);
      SB();
    }
    STAMP(4);
    u32x2 stv[2]; u32x4 cs[2][2][4];
    for (int spin = 0; spin < SPIN_CAP; ++spin) {
      asm volatile("" ::: "memory");
      bool ok = true;
#pragma unroll
      for (int n = 0; n < 2; ++n) { stv[n] = ld_sc1_2(rw, WS_ST + (mi * 128 + wt * 64 + n * 32 + (lane & 31)) * 8); }
#pragma unroll
      for (int m = 0; m < 2; ++m)
#pragma unroll
        for (int a = 0; a < 2; ++a)
#pragma unroll
          for (int k = 0; k < 4; ++k) cs[m][a][k] = ld_sc1(rw, WS_CS + (a * F_ + fj * 128 + (wr_ * 2 + m) * 16 + 8 * h + 2 * k) * 8);
#pragma unroll
      for (int n = 0; n < 2; ++n) ok &= stv[n][1] == tag;
#pragma unroll
      for (int m = 0; m < 2; ++m)
#pragma unroll
        for (int a = 0; a < 2; ++a)
#pragma unroll
          for (int k = 0; k < 4; ++k) ok &= (cs[m][a][k][1] == tag) & (cs[m][a][k][3] == tag);
      if (all_ok(ok)) break;
    }
    STAMP(5);
#pragma unroll
    for (int n = 0; n < 2; ++n) {
      const int tok = mi * 128 + wt * 64 + n * 32 + (lane & 31);
      const float nm = -bf_lo(stv[n][0]);
      const float rs = __builtin_amdgcn_rcpf(bf_hi(stv[n][0])), rs2 = rs * rs;
#pragma unroll
      for (int m = 0; m < 2; ++m) {
        float hv[8];
#pragma unroll
        for (int k = 0; k < 8; ++k) {
          const u32 cgu = cs[m][0][k >> 1][(k & 1) * 2], cuu = cs[m][1][k >> 1][(k & 1) * 2];
          float cg = __builtin_bit_cast(float, cgu), cu = __builtin_bit_cast(float, cuu);
          float gg = __builtin_fmaf(nm, cg, acc[m][n][k]), uu = __builtin_fmaf(nm, cu, acc[m][n][8 + k]);
          hv[k] = __builtin_fmaxf(gg, 0.f) * (uu * rs2);
        }
        const int fo = fj * 16 + (wr_ * 2 + m) * 2 + h;
        st_sc1(rw, WS_H + ((tok >> 4) * 56 + (fo >> 2)) * 1024 + (fo & 3) * 256 + (tok & 15) * 16,
               u32x4{pk_bf16(hv[0], hv[1]), pk_bf16(hv[2], hv[3]), pk_bf16(hv[4], hv[5]), pk_bf16(hv[6], hv[7])});
      }
    }
    STAMP(6);
    WAITV(0);
    BAR();
    if (tid == 0) __builtin_amdgcn_raw_buffer_store_b32(tag, rw, WS_F2 + mi * 128 + fj * 4, 0, 16);
    STAMP(7);
  } else if (b < 28) {
    const char* wsrc = b < 14 ? wg : wu;
    const int ct = b < 14 ? b : b - 14;
    const int r0 = tid >> 4;
    float acc[8] = {0, 0, 0, 0, 0, 0, 0, 0};
    u32x4 v[16];
#pragma unroll
    for (int k = 0; k < 16; ++k) v[k] = *(const u32x4*)(wsrc + (size_t)(r0 + 32 * k) * 3584 + ct * 256 + p16 * 16);
#pragma unroll
    for (int k = 0; k < 16; ++k)
#pragma unroll
      for (int q = 0; q < 4; ++q) { acc[2 * q] += bf_lo(v[k][q]); acc[2 * q + 1] += bf_hi(v[k][q]); }
    LAS float* red = (LAS float*)sm;
    *(LAS f32x4*)(red + r0 * 128 + p16 * 8) = f32x4{acc[0], acc[1], acc[2], acc[3]};
    *(LAS f32x4*)(red + r0 * 128 + p16 * 8 + 4) = f32x4{acc[4], acc[5], acc[6], acc[7]};
    BAR();
    if (tid < 128) {
      float sacc = 0.f;
#pragma unroll
      for (int k = 0; k < 32; ++k) sacc += red[k * 128 + tid];
      st_sc1_2(rw, WS_CS + (b * 128 + tid) * 8, u32x2{__builtin_bit_cast(u32, sacc), tag});
    }
    BAR();
  }

  {
    const int jp = b & 7, g = b >> 3;
    LAS char* cb = sm;
    LAS char* fb = sm + 8 * 1040;
    {
      int q = tid & 255, arr = tid >> 8;
      const unsigned short* wsrc = arr ? wi : wr;
      int e0 = (8 * q + jp) * DF_ + 8 * g;
      const u32* pa = (const u32*)(wsrc + (e0 & ~1));
      u32 d0 = pa[0], d1 = pa[1], d2 = pa[2], d3 = pa[3], d4 = pa[4];
      const int i = tid >> 1, hf = tid & 1;
      u32x4 u0, u1;
      for (int spin = 0; spin < SPIN_CAP; ++spin) {
      asm volatile("" ::: "memory");
        const u32 off = WS_U + i * 16384 + jp * 2048 + g * 64 + hf * 32;
        u0 = ld_sc1(rw, off); u1 = ld_sc1(rw, off + 16);
        if (all_ok((u0[1] == tag) & (u0[3] == tag) & (u1[1] == tag) & (u1[3] == tag))) break;
      }
      const int pos = (i & 192) + 4 * (i & 15) + ((i >> 4) & 3);
      *(LAS u32*)(cb + (4 * hf + 0) * 1040 + pos * 4) = u0[0];
      *(LAS u32*)(cb + (4 * hf + 1) * 1040 + pos * 4) = u0[2];
      *(LAS u32*)(cb + (4 * hf + 2) * 1040 + pos * 4) = u1[0];
      *(LAS u32*)(cb + (4 * hf + 3) * 1040 + pos * 4) = u1[2];
      u32 sh = (e0 & 1) * 16;
      u32 f[4] = {__builtin_amdgcn_alignbit(d1, d0, sh), __builtin_amdgcn_alignbit(d2, d1, sh), __builtin_amdgcn_alignbit(d3, d2, sh), __builtin_amdgcn_alignbit(d4, d3, sh)};
      int posq = (q & 192) + 4 * (q & 15) + ((q >> 4) & 3);
#pragma unroll
      for (int k = 0; k < 4; ++k) {
        *(LAS unsigned short*)(fb + (2 * k) * 1040 + posq * 4 + arr * 2) = (unsigned short)(f[k] & 0xffff);
        *(LAS unsigned short*)(fb + (2 * k + 1) * 1040 + posq * 4 + arr * 2) = (unsigned short)(f[k] >> 16);
      }
    }
    STAMP(8);
    u32x4 bc1re, bc1im, ac2re, ac2im, bc3re, bc3im, ac4re, ac4im;
    float t1c[4], t1s[4], t2c[4], t2s[4];
    const float FSC = (2.0f / (2048.0f * 512.0f)) * (4.0f * 8.0f * 2.0f * 4.0f * 4.0f);
#pragma unroll
    for (int r = 0; r < 4; ++r) {
      const int y = 4 * t + r;
      const float n1 = (float)(y * (8 * p16 + jp)), n2 = (float)(p16 * (8 * y + jp));
      { float cc = 0.25f * cosr(n1 * (1.0f / 128)), sn = 0.25f * sinr(n1 * (1.0f / 128)); bc1re[r] = pk_f16(cc, sn); bc1im[r] = pk_f16(-sn, cc); }
      { float rev = (float)(y * p16) * (1.0f / 16); float cc = cosr(rev), sn = sinr(rev);
        ac2re[r] = pk_f16(0.25f * cc, 0.25f * sn); ac2im[r] = pk_f16(-0.25f * sn, 0.25f * cc);
        bc3re[r] = pk_bf16(cc, -sn); bc3im[r] = pk_bf16(sn, cc); }
      { float cc = cosr(n2 * (1.0f / 128)), sn = sinr(n2 * (1.0f / 128)); ac4re[r] = pk_bf16(cc, -sn); ac4im[r] = pk_bf16(sn, cc); }
      t1c[r] = cosr(n1 * (1.0f / 2048)); t1s[r] = sinr(n1 * (1.0f / 2048));
      t2c[r] = FSC * cosr(n2 * (1.0f / 2048)); t2s[r] = FSC * sinr(n2 * (1.0f / 2048));
    }
    BAR();
    const u32x4 uin = *(LAS u32x4*)(cb + w * 1040 + lane * 16);
    const u32x4 fin = *(LAS u32x4*)(fb + w * 1040 + lane * 16);
    const bool special = (g == 0 && w == 0);
    const int npass = (special && (jp & 3) == 0) ? 2 : 1;
    f32x4 ore = z4, oim = z4;
    for (int pass = 0; pass < npass; ++pass) {
      u32x4 a = uin;
      float fr[4], fi[4];
#pragma unroll
      for (int r = 0; r < 4; ++r) { fr[r] = bf_lo(fin[r]); fi[r] = bf_hi(fin[r]); }
      if (special) {
        const int col = (npass == 2) ? (pass ? 256 : 0) : (jp < 4 ? 0 : 256);
#pragma unroll
        for (int r = 0; r < 4; ++r) {
          int ks = 8 * (p16 + 64 * t + 16 * r) + jp, km = (2048 - ks) & 2047;
          float ar = bf_lo(wr[ks * DF_ + col]), ai = bf_lo(wi[ks * DF_ + col]);
          float br = bf_lo(wr[km * DF_ + col]), bi = bf_lo(wi[km * DF_ + col]);
          fr[r] = 0.5f * (ar + br); fi[r] = 0.5f * (ai - bi);
          if (npass == 2) a[r] = pass ? (uin[r] >> 16) : (uin[r] & 0xffffu);
        }
      }
      f32x4 dre = mfma32_f16(a, bc1re, z4), dim = mfma32_f16(a, bc1im, z4);
#pragma unroll
      for (int r = 0; r < 4; ++r) a[r] = pk_f16(dre[r] * t1c[r] + dim[r] * t1s[r], dim[r] * t1c[r] - dre[r] * t1s[r]);
      dre = mfma32_f16(ac2re, a, z4); dim = mfma32_f16(ac2im, a, z4);
#pragma unroll
      for (int r = 0; r < 4; ++r) a[r] = pk_bf16(dre[r] * fr[r] - dim[r] * fi[r], dre[r] * fi[r] + dim[r] * fr[r]);
      dre = mfma32_bf16(a, bc3re, z4); dim = mfma32_bf16(a, bc3im, z4);
#pragma unroll
      for (int r = 0; r < 4; ++r) a[r] = pk_bf16(dre[r] * t2c[r] - dim[r] * t2s[r], dre[r] * t2s[r] + dim[r] * t2c[r]);
      dre = mfma32_bf16(ac4re, a, z4); dim = mfma32_bf16(ac4im, a, z4);
      if (npass == 2) { if (pass == 0) ore = dre; else oim = dre; }
      else { ore = dre; oim = dim; }
    }
    {
      u32x4 o;
#pragma unroll
      for (int r = 0; r < 4; ++r) o[r] = pk_bf16(ore[r], oim[r]);
      *(LAS u32x4*)(cb + w * 1040 + lane * 16) = o;
    }
    BAR();
    {
      int ps = tid >> 1, hf = tid & 1;
      int i = (ps >> 5) + 8 * (ps & 31);
      int pos = (i & 192) + 4 * (i & 15) + ((i >> 4) & 3);
      u32 o[4];
#pragma unroll
      for (int k = 0; k < 4; ++k) o[k] = *(LAS u32*)(cb + (4 * hf + k) * 1040 + pos * 4);
      st_sc1(rw, WS_V + b * 16384 + tid * 32, u32x4{o[0], tag, o[1], tag});
      st_sc1(rw, WS_V + b * 16384 + tid * 32 + 16, u32x4{o[2], tag, o[3], tag});
    }
  }
  STAMP(9);

  const int rt = (b & 7) * 4 + ((b >> 3) & 3), ct = b >> 5;
  u32x4 wf[7][4];
  {
    const char* src = wd + (size_t)(w * 224 + 8 * t) * 1024 + ct * 128 + p16 * 8;
    u32x2 rwv[7][8];
#pragma unroll
    for (int ks = 0; ks < 7; ++ks)
#pragma unroll
      for (int j = 0; j < 8; ++j) rwv[ks][j] = *(const u32x2*)(src + (size_t)(ks * 32 + j) * 1024);
    SB();
#pragma unroll
    for (int ks = 0; ks < 7; ++ks)
#pragma unroll
      for (int kp = 0; kp < 4; ++kp) {
        wf[ks][0][kp] = __builtin_amdgcn_perm(rwv[ks][2 * kp + 1][0], rwv[ks][2 * kp][0], 0x05040100u);
        wf[ks][1][kp] = __builtin_amdgcn_perm(rwv[ks][2 * kp + 1][0], rwv[ks][2 * kp][0], 0x07060302u);
        wf[ks][2][kp] = __builtin_amdgcn_perm(rwv[ks][2 * kp + 1][1], rwv[ks][2 * kp][1], 0x05040100u);
        wf[ks][3][kp] = __builtin_amdgcn_perm(rwv[ks][2 * kp + 1][1], rwv[ks][2 * kp][1], 0x07060302u);
      }
    SB();
  }
  STAMP(10);

  {
    LAS char* yb = sm + FS;
    const int s = b + 256 * w;
    const u32x4 xv = *(const u32x4*)(x + (size_t)s * 1024 + (4 * p16 + t) * 16);
    {
      const int g = 4 * w + (p16 >> 2);
      const u32 off = WS_V + ((b & 7) * 32 + (b >> 3)) * 64 + (p16 & 3) * 16 + (g * 8 + 2 * t) * 16384;
      const int j = p16 >> 1, po = p16 & 1;
      float c0, s0, c1, s1;
      { float rev = (float)(2 * t * j) * 0.125f; c0 = cosr(rev); s0 = sinr(rev); }
      { float rev = (float)((2 * t + 1) * j) * 0.125f; c1 = cosr(rev); s1 = sinr(rev); }
      const u32x2 a8 = po ? u32x2{pk_bf16(s0, c0), pk_bf16(s1, c1)} : u32x2{pk_bf16(c0, -s0), pk_bf16(c1, -s1)};
      u32x4 r0, r1;
      for (int spin = 0; spin < SPIN_CAP; ++spin) {
      asm volatile("" ::: "memory");
        r0 = ld_sc1(rw, off); r1 = ld_sc1(rw, off + 16384);
        if (all_ok((r0[1] == tag) & (r0[3] == tag) & (r1[1] == tag) & (r1[3] == tag))) break;
      }
      STAMP(11);
      u32x2 o0, o1;
#pragma unroll
      for (int u = 0; u < 2; ++u) {
        f32x4 d = mfma16_bf16(a8, u32x2{r0[2 * u], r1[2 * u]}, z4);
        if (u == 0 && w == 0) {
          u32 e0, e1;
          const bool on = (po == 0) ? (t < 2) : (t >= 2);
          if ((t & 1) == 0) e0 = po ? pk_bf16(0.f, 0.5f * c0) : pk_bf16(0.5f * c0, 0.f);
          else e0 = on ? pk_bf16(c0, -s0) : 0u;
          e1 = on ? pk_bf16(c1, -s1) : 0u;
          f32x4 ds = mfma16_bf16(u32x2{e0, e1}, u32x2{r0[0], r1[0]}, z4);
          if (p16 == 0) d = ds;
        }
        o0[u] = pk_bf16(d[0], d[1]); o1[u] = pk_bf16(d[2], d[3]);
      }
      *(LAS u32x2*)(yb + (2 * t) * 1024 + (16 * w + p16) * 8) = o0;
      *(LAS u32x2*)(yb + (2 * t + 1) * 1024 + (16 * w + p16) * 8) = o1;
    }
    u32x4 bi1re[2], bi1im[2], bi2;
    float twc[2][4], tws[2][4];
#pragma unroll
    for (int hh = 0; hh < 2; ++hh) {
      const int n2 = 8 * (p16 >> 2) + 4 * hh + (p16 & 3);
#pragma unroll
      for (int m = 0; m < 4; ++m) {
        float rev = (float)(n2 * (t + 4 * m)) * (1.0f / 32); float cc = cosr(rev), sn = sinr(rev);
        bi1re[hh][m] = pk_bf16(cc, -sn); bi1im[hh][m] = pk_bf16(sn, cc);
        float rev2 = (float)(n2 * (4 * t + m)) * (1.0f / 512);
        twc[hh][m] = cosr(rev2); tws[hh][m] = sinr(rev2);
      }
    }
#pragma unroll
    for (int r = 0; r < 4; ++r) { float rev = (float)(p16 * (4 * t + r)) * (1.0f / 16); bi2[r] = pk_bf16(cosr(rev), -sinr(rev)); }
    BAR();
    u32x4 a;
#pragma unroll
    for (int m = 0; m < 4; ++m) a[m] = *(LAS u32*)(yb + w * 1024 + (lane + 64 * m) * 4);
    const float nyq = bf_hi(*(LAS u32*)(yb + w * 1024));
    u32 ov[4];
#pragma unroll
    for (int hh = 0; hh < 2; ++hh) {
      f32x4 gre = mfma32_bf16(a, bi1re[hh], z4), gim = mfma32_bf16(a, bi1im[hh], z4);
      u32x4 bb;
#pragma unroll
      for (int r = 0; r < 4; ++r) bb[r] = pk_bf16(gre[r] * twc[hh][r] - gim[r] * tws[hh][r], gre[r] * tws[hh][r] + gim[r] * twc[hh][r]);
      f32x4 d = mfma32_bf16(bb, bi2, f32x4{nyq, -nyq, nyq, -nyq});
      ov[2 * hh] = pk_bf16(bf_lo(xv[2 * hh]) + d[0], bf_hi(xv[2 * hh]) + d[1]);
      ov[2 * hh + 1] = pk_bf16(bf_lo(xv[2 * hh + 1]) + d[2], bf_hi(xv[2 * hh + 1]) + d[3]);
    }
    st_sc1(rw, WS_R + s * 2048 + (4 * p16 + t) * 32, u32x4{ov[0], tag, ov[1], tag});
    st_sc1(rw, WS_R + s * 2048 + (4 * p16 + t) * 32 + 16, u32x4{ov[2], tag, ov[3], tag});
  }
  STAMP(12);

  {
    for (int spin = 0; spin < SPIN_CAP; ++spin) {
      asm volatile("" ::: "memory");
      u32 fl = __builtin_amdgcn_raw_buffer_load_b32(rw, WS_F2 + (rt >> 1) * 128 + (lane < 14 ? lane : 0) * 4, 0, 16);
      if (all_ok(fl == tag)) break;
    }
    __builtin_amdgcn_fence(__ATOMIC_ACQUIRE, "wavefront");
    STAMP(13);
    const u32 hs = WS_H + ((rt * 4) * 56 + w * 7) * 1024 + lane * 16;
    u32x4 hf[4][7];
#pragma unroll
    for (int tt = 0; tt < 3; ++tt)
#pragma unroll
      for (int ks = 0; ks < 7; ++ks) hf[tt][ks] = ld_sc1(rw, hs + (tt * 56 + ks) * 1024);
    SB();
#pragma unroll
    for (int tt = 0; tt < 4; ++tt) {
      f32x4 acc[4] = {z4, z4, z4, z4};
#pragma unroll
      for (int ks = 0; ks < 7; ++ks) {
#pragma unroll
        for (int u = 0; u < 4; ++u) acc[u] = mfma32_bf16(wf[ks][u], hf[tt][ks], acc[u]);
        if (tt == 0) { SB(); hf[3][ks] = ld_sc1(rw, hs + (3 * 56 + ks) * 1024); SB(); }
      }
#pragma unroll
      for (int u = 0; u < 4; ++u) *(LAS f32x4*)(sm + w * 16384 + (tt * 4 + u) * 1024 + lane * 16) = acc[u];
      SB();
    }
    STAMP(14);
    const int tt = w >> 1, hh = w & 1;
    const int row = rt * 64 + tt * 16 + p16;
    u32x4 r0, r1;
    for (int spin = 0; spin < SPIN_CAP; ++spin) {
      asm volatile("" ::: "memory");
      const u32 off = WS_R + row * 2048 + ct * 256 + t * 64 + hh * 32;
      r0 = ld_sc1(rw, off); r1 = ld_sc1(rw, off + 16);
      if (all_ok((r0[1] == tag) & (r0[3] == tag) & (r1[1] == tag) & (r1[3] == tag))) break;
    }
    BAR();
    f32x2 sacc[4];
#pragma unroll
    for (int u = 0; u < 4; ++u) {
      sacc[u] = f32x2{0.f, 0.f};
#pragma unroll
      for (int pw = 0; pw < 8; ++pw) sacc[u] += *(LAS f32x2*)(sm + pw * 16384 + (tt * 4 + u) * 1024 + lane * 16 + hh * 8);
    }
    u32x4 o;
    o[0] = pk_bf16(bf_lo(r0[0]) + sacc[0][0], bf_hi(r0[0]) + sacc[1][0]);
    o[1] = pk_bf16(bf_lo(r0[2]) + sacc[2][0], bf_hi(r0[2]) + sacc[3][0]);
    o[2] = pk_bf16(bf_lo(r1[0]) + sacc[0][1], bf_hi(r1[0]) + sacc[1][1]);
    o[3] = pk_bf16(bf_lo(r1[2]) + sacc[2][1], bf_hi(r1[2]) + sacc[3][1]);
    *(u32x4*)(out + (size_t)row * 1024 + ct * 128 + t * 32 + hh * 16) = o;
    if (b == 0 && tid == 0) __builtin_amdgcn_raw_buffer_store_b32(tag, rw, WS_EPOCH, 0, 16);
  }
  STAMP(15);
  PROBE_END();
}

extern "C" void kernel_launch(void* const* d_in, const int* in_sizes, int n_in, void* d_out, int out_size, void* d_ws, size_t ws_size,
                              hipStream_t stream) {
  (void)hipFuncSetAttribute((const void*)fused_kernel, hipFuncAttributeMaxDynamicSharedMemorySize, LDS_BYTES);
  fused_kernel<<<256, 512, LDS_BYTES, stream>>>((const char*)d_in[0], (const unsigned short*)d_in[1], (const unsigned short*)d_in[2], (const char*)d_in[4],
                                                (const char*)d_in[5], (const char*)d_in[6], (char*)d_ws, (char*)d_out);
}
```

```cpp
#define PROBE_WG 0
#define PROBE_T tA
#include <hip/hip_runtime.h>
#include <stdint.h>

#define LAS __attribute__((address_space(3)))
#define GAS __attribute__((address_space(1)))
#define DI __device__ __forceinline__
typedef unsigned u32;
typedef unsigned u32x2 __attribute__((ext_vector_type(2)));
typedef unsigned u32x4 __attribute__((ext_vector_type(4)));
typedef float f32x2 __attribute__((ext_vector_type(2)));
typedef float f32x4 __attribute__((ext_vector_type(4)));
typedef float f32x16 __attribute__((ext_vector_type(16)));
typedef short s16x4 __attribute__((ext_vector_type(4)));
typedef _Float16 h16x2 __attribute__((ext_vector_type(2)));
typedef _Float16 h16x4 __attribute__((ext_vector_type(4)));
typedef _Float16 h16x8 __attribute__((ext_vector_type(8)));
typedef __bf16 b16x2 __attribute__((ext_vector_type(2)));
typedef __bf16 b16x8 __attribute__((ext_vector_type(8)));
typedef __amdgpu_buffer_rsrc_t rsrc_t;

constexpr int F_ = 1792, DF_ = 257;
constexpr u32 WS_EPOCH = 0;
constexpr u32 WS_F2 = 4096;
constexpr u32 WS_ST = 8192;
constexpr u32 WS_CS = WS_ST + 16384;
constexpr u32 WS_U = 65536;
constexpr u32 WS_V = WS_U + 4194304;
constexpr u32 WS_R = WS_V + 4194304;
constexpr u32 WS_H = WS_R + 4194304;
constexpr u32 WS_END = WS_H + 7340032;
constexpr int LDS_BYTES = 163840;
constexpr int G1_STAGE = 49152;
constexpr int FS = 3 * G1_STAGE;
constexpr int SPIN_CAP = 1 << 17;
constexpr size_t WS_DBG = 64 << 20;
#define STAMP(k) do {} while (0)
#define PROBE_END() do {} while (0)

DI u32 pk_f16(float a, float b) { h16x2 p = {(_Float16)a, (_Float16)b}; return __builtin_bit_cast(u32, p); }
DI u32 pk_bf16(float a, float b) { b16x2 p = {(__bf16)a, (__bf16)b}; return __builtin_bit_cast(u32, p); }
DI float bf_lo(u32 v) { return __builtin_bit_cast(float, v << 16); }
DI float bf_hi(u32 v) { return __builtin_bit_cast(float, v & 0xffff0000u); }
DI float rnd_bf(float a) { return bf_lo(pk_bf16(a, a)); }
DI float cosr(float rev) { return __builtin_amdgcn_cosf(rev); }
DI float sinr(float rev) { return __builtin_amdgcn_sinf(rev); }
DI f32x4 mfma16_f16(u32x2 a, u32x2 b, f32x4 c) { return __builtin_amdgcn_mfma_f32_16x16x16f16(__builtin_bit_cast(h16x4, a), __builtin_bit_cast(h16x4, b), c, 0, 0, 0); }
DI f32x4 mfma16_bf16(u32x2 a, u32x2 b, f32x4 c) { return __builtin_amdgcn_mfma_f32_16x16x16bf16_1k(__builtin_bit_cast(s16x4, a), __builtin_bit_cast(s16x4, b), c, 0, 0, 0); }
DI f32x4 mfma32_f16(u32x4 a, u32x4 b, f32x4 c) { return __builtin_amdgcn_mfma_f32_16x16x32_f16(__builtin_bit_cast(h16x8, a), __builtin_bit_cast(h16x8, b), c, 0, 0, 0); }
DI f32x4 mfma32_bf16(u32x4 a, u32x4 b, f32x4 c) { return __builtin_amdgcn_mfma_f32_16x16x32_bf16(__builtin_bit_cast(b16x8, a), __builtin_bit_cast(b16x8, b), c, 0, 0, 0); }
DI f32x16 mfma3232(u32x4 a, u32x4 b, f32x16 c) { return __builtin_amdgcn_mfma_f32_32x32x16_bf16(__builtin_bit_cast(b16x8, a), __builtin_bit_cast(b16x8, b), c, 0, 0, 0); }
DI u32x2 tr_read(LAS char* p) { return __builtin_bit_cast(u32x2, __builtin_amdgcn_ds_read_tr16_b64_v4i16((LAS s16x4*)p)); }
#define SB() __builtin_amdgcn_sched_barrier(0)
#define BAR() asm volatile("s_waitcnt lgkmcnt(0)\n\ts_barrier" ::: "memory")
#define WAITV(n) asm volatile("s_waitcnt vmcnt(%0)" :: "i"(n) : "memory")
DI u32x4 mk_srd(const void* p, u32 bytes) { uint64_t a = (uint64_t)p; return u32x4{(u32)a, (u32)(a >> 32), bytes, 0x00020000u}; }
DI void glds16(u32 ldsaddr, u32 voff, u32x4 srd, u32 soff) {
  asm volatile("s_mov_b32 m0, %0\n\ts_nop 0\n\tbuffer_load_dwordx4 %1, %2, %3 offen lds" :: "s"(ldsaddr), "v"(voff), "s"(srd), "s"(soff) : "memory");
}
DI u32x4 ld_sc1(rsrc_t r, u32 off) { return __builtin_amdgcn_raw_buffer_load_b128(r, off, 0, 16); }
DI u32x2 ld_sc1_2(rsrc_t r, u32 off) { return __builtin_amdgcn_raw_buffer_load_b64(r, off, 0, 16); }
DI void st_sc1(rsrc_t r, u32 off, u32x4 v) { __builtin_amdgcn_raw_buffer_store_b128(v, r, off, 0, 16); }
DI void st_sc1_2(rsrc_t r, u32 off, u32x2 v) { __builtin_amdgcn_raw_buffer_store_b64(v, r, off, 0, 16); }
DI bool all_ok(bool ok) { return __builtin_amdgcn_ballot_w64(!ok) == 0; }

template <int CTRL> DI float dpp_add(float v) {
  int o = __builtin_amdgcn_update_dpp(0, __builtin_bit_cast(int, v), CTRL, 0xf, 0xf, true);
  return v + __builtin_bit_cast(float, o);
}
DI float wave_sum(float v) {
  v = dpp_add<0xB1>(v); v = dpp_add<0x4E>(v); v = dpp_add<0x141>(v); v = dpp_add<0x140>(v);
  u32 u = __builtin_bit_cast(u32, v);
  auto a = __builtin_amdgcn_permlane16_swap(u, u, false, false);
  v = __builtin_bit_cast(float, (u32)a[0]) + __builtin_bit_cast(float, (u32)a[1]);
  u = __builtin_bit_cast(u32, v);
  auto b = __builtin_amdgcn_permlane32_swap(u, u, false, false);
  return __builtin_bit_cast(float, (u32)b[0]) + __builtin_bit_cast(float, (u32)b[1]);
}

struct G1L { u32x4 sx, sg, su; u32 vx, vg, vu, lw; };
DI void g1_issue1(const G1L& L, int st, int k) {
  const u32 lb = L.lw + (st % 3) * G1_STAGE + k * 8192;
  if (k < 2) glds16(lb, L.vx, L.sx, st * 128 + k * 65536);
  else if (k < 4) glds16(lb, L.vg, L.sg, (st * 64 + (k - 2) * 32) * 3584);
  else glds16(lb, L.vu, L.su, (st * 64 + (k - 4) * 32) * 3584);
}
struct Frag { u32x4 bx[2], aw[2]; };

typedef unsigned u32x16 __attribute__((ext_vector_type(16)));
__global__ __launch_bounds__(512, 2) void fused_kernel(const char* a0, const unsigned short* a1, const unsigned short* a2, const char* a3, const char* a4, const char* a5, char* a6, char* a7) {
  unsigned long long tA, tB; u32x16 ka;
  asm volatile("s_memrealtime %0\n\ts_load_dwordx16 %2, %3, 0x0\n\ts_waitcnt lgkmcnt(0)\n\ts_memrealtime %1\n\ts_waitcnt lgkmcnt(0)" : "=&s"(tA), "=&s"(tB), "=&s"(ka) : "s"(__builtin_amdgcn_kernarg_segment_ptr()) : "memory");
#define KP(i) (((uint64_t)ka[2 * (i) + 1] << 32) | ka[2 * (i)])
  const char* __restrict__ x = (const char*)(const GAS char*)KP(0); const unsigned short* __restrict__ wr = (const unsigned short*)(const GAS unsigned short*)KP(1); const unsigned short* __restrict__ wi = (const unsigned short*)(const GAS unsigned short*)KP(2);
  const char* __restrict__ wg = (const char*)(const GAS char*)KP(3); const char* __restrict__ wu = (const char*)(const GAS char*)KP(4); const char* __restrict__ wd = (const char*)(const GAS char*)KP(5);
  char* __restrict__ ws = (char*)(GAS char*)KP(6); char* __restrict__ out = (char*)(GAS char*)KP(7);
  extern __shared__ __attribute__((aligned(16))) char smem[];
  LAS char* sm = (LAS char*)smem;
  const int b = blockIdx.x, tid = threadIdx.x, lane = tid & 63, w = __builtin_amdgcn_readfirstlane(tid >> 6);
  const int p16 = lane & 15, t = lane >> 4;
  const f32x4 z4 = {0.f, 0.f, 0.f, 0.f};
  u32 pfd = 0;
  unsigned long long pr_t0 = 0, pr_tk = 0;
  {
    uint64_t pc; asm volatile("s_getpc_b64 %0" : "=s"(pc));
    if (b < 8 && tid < CODE_LINES) asm volatile("global_load_dword %0, %1, %2" : "+v"(pfd) : "v"(tid * 128), "s"(pc) : "memory");
  }
  STAMP(0);
  const bool is_gemm = b >= 32;
  const int q_ = b - 32, xc = q_ & 7, r_ = q_ >> 3;
  const int mi = (xc >> 1) * 4 + (r_ & 3), fj = (xc & 1) * 7 + (r_ >> 2);
  G1L L;
  L.sx = mk_srd(x, 2097152); L.sg = mk_srd(wg, 1835008); L.su = mk_srd(wu, 1835008);
  L.lw = (u32)(uintptr_t)sm + w * 1024;
  glds16(L.lw + FS, (b + 256 * w) * 1024 + lane * 16, L.sx, 0);
  if (is_gemm) {
    { int row = w * 8 + (lane >> 3), pos = lane & 7; L.vx = (mi * 128 + row) * 1024 + ((pos ^ ((row >> 1) & 7)) << 4); }
    { int k = w * 4 + t; L.vg = k * 3584 + fj * 256 + ((p16 ^ (2 * (k & 3))) << 4); L.vu = L.vg ^ 128; }
#pragma unroll
    for (int st = 0; st < 3; ++st)
#pragma unroll
      for (int k = 0; k < 6; ++k) g1_issue1(L, st, k);
  }
  const rsrc_t rw = __builtin_amdgcn_make_buffer_rsrc(ws, 0, WS_END, 0x00020000);
  const u32 tag = *(const u32*)(ws + WS_EPOCH) * 0x9E3779B1u + 0x7F4A7C15u;

  {
    u32x2 b1re, b1im;
    {
      float c[4], sn[4];
#pragma unroll
      for (int jj = 0; jj < 4; ++jj) { float rev = (float)((4 * t + jj) * p16) * (1.0f / 16); c[jj] = 0.25f * cosr(rev); sn[jj] = -0.25f * sinr(rev); }
      b1re = u32x2{pk_f16(c[0], c[1]), pk_f16(c[2], c[3])};
      b1im = u32x2{pk_f16(sn[0], sn[1]), pk_f16(sn[2], sn[3])};
    }
    float twc[2][4], tws[2][4];
    u32x4 b2re[2], b2im[2];
#pragma unroll
    for (int hh = 0; hh < 2; ++hh)
#pragma unroll
      for (int r = 0; r < 4; ++r) {
        float nk = (float)((16 * hh + 4 * t + r) * p16);
        twc[hh][r] = cosr(nk * (1.0f / 512)); tws[hh][r] = sinr(nk * (1.0f / 512));
        float c = 0.125f * cosr(nk * (1.0f / 32)), sn = 0.125f * sinr(nk * (1.0f / 32));
        b2re[hh][r] = pk_f16(c, sn); b2im[hh][r] = pk_f16(-sn, c);
      }
    const int jp = p16 >> 1, po = p16 & 1;
    float c0, s0, c1, s1;
    { float rev = (float)(2 * t * jp) * 0.125f; c0 = 0.5f * cosr(rev); s0 = 0.5f * sinr(rev); }
    { float rev = (float)((2 * t + 1) * jp) * 0.125f; c1 = 0.5f * cosr(rev); s1 = 0.5f * sinr(rev); }
    const u32x2 a8 = po ? u32x2{pk_f16(-s0, c0), pk_f16(-s1, c1)} : u32x2{pk_f16(c0, s0), pk_f16(c1, s1)};
    LAS char* img = sm + FS + w * 1024;
    SB();
    if (is_gemm) WAITV(18); else WAITV(0);
    asm volatile("" :: "v"(pfd));
    STAMP(1);
    const u32x4 xv = *(LAS u32x4*)(img + lane * 16);
    float v[8];
#pragma unroll
    for (int k = 0; k < 4; ++k) { v[2 * k] = bf_lo(xv[k]); v[2 * k + 1] = bf_hi(xv[k]); }
    float sum = ((v[0] + v[1]) + (v[2] + v[3])) + ((v[4] + v[5]) + (v[6] + v[7]));
    const float mean = wave_sum(sum) * (1.0f / 512);
    float sq0 = 0.f, sq1 = 0.f;
#pragma unroll
    for (int k = 0; k < 4; ++k) { float d = v[k] - mean, e = v[k + 4] - mean; sq0 += d * d; sq1 += e * e; }
    const float var = wave_sum(sq0 + sq1) * (1.0f / 512);
    const float mb = rnd_bf(mean);
    const float sd = rnd_bf(__builtin_amdgcn_sqrtf(rnd_bf(rnd_bf(var) + 1e-5f)));
    const float rs = __builtin_amdgcn_rcpf(sd);
    if (lane == 0) st_sc1_2(rw, WS_ST + (b + 256 * w) * 8, u32x2{pk_bf16(mb, sd), tag});
    u32x4 xh;
#pragma unroll
    for (int k = 0; k < 4; ++k) {
      u32 dp = pk_bf16(v[2 * k] - mb, v[2 * k + 1] - mb);
      u32 np = pk_bf16(bf_lo(dp) * rs, bf_hi(dp) * rs);
      xh[k] = pk_f16(bf_lo(np), bf_hi(np));
    }
    *(LAS u32x4*)(img + ((lane ^ ((t & 1) << 1)) << 4)) = xh;
    __builtin_amdgcn_fence(__ATOMIC_RELEASE, "wavefront");
    __builtin_amdgcn_wave_barrier();
    f32x4 d1re[2], d1im[2];
#pragma unroll
    for (int hh = 0; hh < 2; ++hh) {
      u32x2 a = tr_read(img + (4 * t + (p16 >> 2)) * 64 + ((hh ^ (t & 1)) << 5) + (p16 & 3) * 8);
      d1re[hh] = mfma16_f16(a, b1re, z4);
      d1im[hh] = mfma16_f16(a, b1im, z4);
    }
    float ny = ((d1re[0][0] - d1re[0][1]) + (d1re[0][2] - d1re[0][3])) + ((d1re[1][0] - d1re[1][1]) + (d1re[1][2] - d1re[1][3]));
    f32x4 d2re = z4, d2im = z4;
#pragma unroll
    for (int hh = 0; hh < 2; ++hh) {
      u32x4 a;
#pragma unroll
      for (int r = 0; r < 4; ++r)
        a[r] = pk_f16(d1re[hh][r] * twc[hh][r] + d1im[hh][r] * tws[hh][r], d1im[hh][r] * twc[hh][r] - d1re[hh][r] * tws[hh][r]);
      d2re = mfma32_f16(a, b2re[hh], d2re);
      d2im = mfma32_f16(a, b2im[hh], d2im);
    }
    {
      int nb = __builtin_bit_cast(int, ny);
      float n1_ = __builtin_bit_cast(float, __builtin_amdgcn_readlane(nb, 16));
      float n2_ = __builtin_bit_cast(float, __builtin_amdgcn_readlane(nb, 32));
      float n3_ = __builtin_bit_cast(float, __builtin_amdgcn_readlane(nb, 48));
      float tot = 0.125f * ((ny + n1_) + (n2_ + n3_));
      if (lane == 0) d2im[0] = tot;
    }
    LAS char* ex = sm + FS + 8192;
    {
      u32x4 o;
#pragma unroll
      for (int r = 0; r < 4; ++r) o[r] = pk_f16(d2re[r], d2im[r]);
      int e = 4 * p16 + t; e ^= (e >> 3) & 3;
      *(LAS u32x4*)(ex + w * 1024 + e * 16) = o;
    }
    BAR();
    {
      const int wq = w & 3, hf = w >> 2;
      int e = 16 * wq + p16; e ^= (e >> 3) & 3;
      u32x4 r0 = *(LAS u32x4*)(ex + (2 * t) * 1024 + e * 16);
      u32x4 r1 = *(LAS u32x4*)(ex + (2 * t + 1) * 1024 + e * 16);
      u32 o[4];
#pragma unroll
      for (int u = 0; u < 4; ++u) {
        f32x4 d = mfma16_f16(a8, u32x2{r0[u], r1[u]}, z4);
        if (u == 0 && wq == 0) {
          float e0 = po ? -s0 : c0, e1 = po ? -s1 : c1;
          u32x2 as = (jp & 3) == 0 ? a8 : (jp < 4 ? u32x2{pk_f16(e0, 0.f), pk_f16(e1, 0.f)} : u32x2{pk_f16(0.f, e0), pk_f16(0.f, e1)});
          f32x4 ds = mfma16_f16(as, u32x2{r0[0], r1[0]}, z4);
          if (p16 == 0) d = ds;
        }
        o[u] = hf ? pk_f16(d[2], d[3]) : pk_f16(d[0], d[1]);
      }
      const u32 ub = WS_U + b * 16384 + (2 * t + hf) * 2048 + (64 * wq + 4 * p16) * 8;
      st_sc1(rw, ub, u32x4{o[0], tag, o[1], tag});
      st_sc1(rw, ub + 16, u32x4{o[2], tag, o[3], tag});
    }
  }
  STAMP(2);

  if (is_gemm) {
    const int wr_ = w >> 1, wt = w & 1, h = lane >> 5;
    int xoff[4], woff[2];
#pragma unroll
    for (int ks = 0; ks < 4; ++ks) { int tok = wt * 64 + (lane & 31); xoff[ks] = tok * 128 + (((2 * ks + h) ^ ((tok >> 1) & 7)) << 4); }
    {
      int arr = t & 1, q = p16 >> 2, p = lane & 3;
#pragma unroll
      for (int m = 0; m < 2; ++m)
        woff[m] = 16384 + arr * 16384 + (8 * h + q) * 256 + (((wr_ * 2 + m) ^ (q | (arr << 2))) << 5) + (p & 1) * 16 + (p >> 1) * 8;
    }
    auto rd = [&](Frag& f, int st, int ks) {
      LAS char* buf = sm + (st % 3) * G1_STAGE;
#pragma unroll
      for (int n = 0; n < 2; ++n) f.bx[n] = *(LAS u32x4*)(buf + xoff[ks] + n * 4096);
#pragma unroll
      for (int m = 0; m < 2; ++m) {
        u32x2 lo = tr_read(buf + woff[m] + ks * 4096), hi = tr_read(buf + woff[m] + ks * 4096 + 1024);
        f.aw[m] = u32x4{lo[0], lo[1], hi[0], hi[1]};
      }
    };
    f32x16 acc[2][2];
#pragma unroll
    for (int m = 0; m < 2; ++m)
#pragma unroll
      for (int n = 0; n < 2; ++n)
#pragma unroll
        for (int k = 0; k < 16; ++k) acc[m][n][k] = 0.f;
    Frag fa, fb;
    auto mm = [&](Frag& f, int ls, int k0) {
      acc[0][0] = mfma3232(f.aw[0], f.bx[0], acc[0][0]);
      SB(); if (ls < 8 && k0 >= 0) g1_issue1(L, ls, k0); SB();
      acc[0][1] = mfma3232(f.aw[0], f.bx[1], acc[0][1]);
      acc[1][1] = mfma3232(f.aw[1], f.bx[1], acc[1][1]);
      SB(); if (ls < 8 && k0 >= 0) g1_issue1(L, ls, k0 + 1); SB();
      acc[1][0] = mfma3232(f.aw[1], f.bx[0], acc[1][0]);
    };
    SB();
    WAITV(12 + 3);
    __builtin_amdgcn_s_barrier();
    STAMP(3);
    rd(fa, 0, 0);
#pragma unroll
    for (int st = 0; st < 8; ++st) {
      const int ls = st == 0 ? 8 : st + 2;
      SB(); rd(fb, st, 1); SB(); mm(fa, ls, 2);
      SB(); rd(fa, st, 2); SB(); mm(fb, ls, 4);
      SB(); rd(fb, st, 3); SB(); mm(fa, ls, -1);
      SB();
      if (st < 7) {
        if (st < 6) WAITV(6); else WAITV(0);
        BAR();
        SB();
        rd(fa, st + 1, 0);
        SB();
      }
      mm(fb, st + 3, 0);
      SB();
    }
    STAMP(4);
    u32x2 stv[2]; u32x4 cs[2][2][4];
    for (int spin = 0; spin < SPIN_CAP; ++spin) {
      asm volatile("" ::: "memory");
      bool ok = true;
#pragma unroll
      for (int n = 0; n < 2; ++n) { stv[n] = ld_sc1_2(rw, WS_ST + (mi * 128 + wt * 64 + n * 32 + (lane & 31)) * 8); }
#pragma unroll
      for (int m = 0; m < 2; ++m)
#pragma unroll
        for (int a = 0; a < 2; ++a)
#pragma unroll
          for (int k = 0; k < 4; ++k) cs[m][a][k] = ld_sc1(rw, WS_CS + (a * F_ + fj * 128 + (wr_ * 2 + m) * 16 + 8 * h + 2 * k) * 8);
#pragma unroll
      for (int n = 0; n < 2; ++n) ok &= stv[n][1] == tag;
#pragma unroll
      for (int m = 0; m < 2; ++m)
#pragma unroll
        for (int a = 0; a < 2; ++a)
#pragma unroll
          for (int k = 0; k < 4; ++k) ok &= (cs[m][a][k][1] == tag) & (cs[m][a][k][3] == tag);
      if (all_ok(ok)) break;
    }
    STAMP(5);
#pragma unroll
    for (int n = 0; n < 2; ++n) {
      const int tok = mi * 128 + wt * 64 + n * 32 + (lane & 31);
      const float nm = -bf_lo(stv[n][0]);
      const float rs = __builtin_amdgcn_rcpf(bf_hi(stv[n][0])), rs2 = rs * rs;
#pragma unroll
      for (int m = 0; m < 2; ++m) {
        float hv[8];
#pragma unroll
        for (int k = 0; k < 8; ++k) {
          const u32 cgu = cs[m][0][k >> 1][(k & 1) * 2], cuu = cs[m][1][k >> 1][(k & 1) * 2];
          float cg = __builtin_bit_cast(float, cgu), cu = __builtin_bit_cast(float, cuu);
          float gg = __builtin_fmaf(nm, cg, acc[m][n][k]), uu = __builtin_fmaf(nm, cu, acc[m][n][8 + k]);
          hv[k] = __builtin_fmaxf(gg, 0.f) * (uu * rs2);
        }
        const int fo = fj * 16 + (wr_ * 2 + m) * 2 + h;
        st_sc1(rw, WS_H + ((tok >> 4) * 56 + (fo >> 2)) * 1024 + (fo & 3) * 256 + (tok & 15) * 16,
               u32x4{pk_bf16(hv[0], hv[1]), pk_bf16(hv[2], hv[3]), pk_bf16(hv[4], hv[5]), pk_bf16(hv[6], hv[7])});
      }
    }
    STAMP(6);
    WAITV(0);
    BAR();
    if (tid == 0) __builtin_amdgcn_raw_buffer_store_b32(tag, rw, WS_F2 + mi * 128 + fj * 4, 0, 16);
    STAMP(7);
  } else if (b < 28) {
    const char* wsrc = b < 14 ? wg : wu;
    const int ct = b < 14 ? b : b - 14;
    const int r0 = tid >> 4;
    float acc[8] = {0, 0, 0, 0, 0, 0, 0, 0};
    u32x4 v[16];
#pragma unroll
    for (int k = 0; k < 16; ++k) v[k] = *(const u32x4*)(wsrc + (size_t)(r0 + 32 * k) * 3584 + ct * 256 + p16 * 16);
#pragma unroll
    for (int k = 0; k < 16; ++k)
#pragma unroll
      for (int q = 0; q < 4; ++q) { acc[2 * q] += bf_lo(v[k][q]); acc[2 * q + 1] += bf_hi(v[k][q]); }
    LAS float* red = (LAS float*)sm;
    *(LAS f32x4*)(red + r0 * 128 + p16 * 8) = f32x4{acc[0], acc[1], acc[2], acc[3]};
    *(LAS f32x4*)(red + r0 * 128 + p16 * 8 + 4) = f32x4{acc[4], acc[5], acc[6], acc[7]};
    BAR();
    if (tid < 128) {
      float sacc = 0.f;
#pragma unroll
      for (int k = 0; k < 32; ++k) sacc += red[k * 128 + tid];
      st_sc1_2(rw, WS_CS + (b * 128 + tid) * 8, u32x2{__builtin_bit_cast(u32, sacc), tag});
    }
    BAR();
  }

  {
    const int jp = b & 7, g = b >> 3;
    LAS char* cb = sm;
    LAS char* fb = sm + 8 * 1040;
    {
      int q = tid & 255, arr = tid >> 8;
      const unsigned short* wsrc = arr ? wi : wr;
      int e0 = (8 * q + jp) * DF_ + 8 * g;
      const u32* pa = (const u32*)(wsrc + (e0 & ~1));
      u32 d0 = pa[0], d1 = pa[1], d2 = pa[2], d3 = pa[3], d4 = pa[4];
      const int i = tid >> 1, hf = tid & 1;
      u32x4 u0, u1;
      for (int spin = 0; spin < SPIN_CAP; ++spin) {
      asm volatile("" ::: "memory");
        const u32 off = WS_U + i * 16384 + jp * 2048 + g * 64 + hf * 32;
        u0 = ld_sc1(rw, off); u1 = ld_sc1(rw, off + 16);
        if (all_ok((u0[1] == tag) & (u0[3] == tag) & (u1[1] == tag) & (u1[3] == tag))) break;
      }
      const int pos = (i & 192) + 4 * (i & 15) + ((i >> 4) & 3);
      *(LAS u32*)(cb + (4 * hf + 0) * 1040 + pos * 4) = u0[0];
      *(LAS u32*)(cb + (4 * hf + 1) * 1040 + pos * 4) = u0[2];
      *(LAS u32*)(cb + (4 * hf + 2) * 1040 + pos * 4) = u1[0];
      *(LAS u32*)(cb + (4 * hf + 3) * 1040 + pos * 4) = u1[2];
      u32 sh = (e0 & 1) * 16;
      u32 f[4] = {__builtin_amdgcn_alignbit(d1, d0, sh), __builtin_amdgcn_alignbit(d2, d1, sh), __builtin_amdgcn_alignbit(d3, d2, sh), __builtin_amdgcn_alignbit(d4, d3, sh)};
      int posq = (q & 192) + 4 * (q & 15) + ((q >> 4) & 3);
#pragma unroll
      for (int k = 0; k < 4; ++k) {
        *(LAS unsigned short*)(fb + (2 * k) * 1040 + posq * 4 + arr * 2) = (unsigned short)(f[k] & 0xffff);
        *(LAS unsigned short*)(fb + (2 * k + 1) * 1040 + posq * 4 + arr * 2) = (unsigned short)(f[k] >> 16);
      }
    }
    STAMP(8);
    u32x4 bc1re, bc1im, ac2re, ac2im, bc3re, bc3im, ac4re, ac4im;
    float t1c[4], t1s[4], t2c[4], t2s[4];
    const float FSC = (2.0f / (2048.0f * 512.0f)) * (4.0f * 8.0f * 2.0f * 4.0f * 4.0f);
#pragma unroll
    for (int r = 0; r < 4; ++r) {
      const int y = 4 * t + r;
      const float n1 = (float)(y * (8 * p16 + jp)), n2 = (float)(p16 * (8 * y + jp));
      { float cc = 0.25f * cosr(n1 * (1.0f / 128)), sn = 0.25f * sinr(n1 * (1.0f / 128)); bc1re[r] = pk_f16(cc, sn); bc1im[r] = pk_f16(-sn, cc); }
      { float rev = (float)(y * p16) * (1.0f / 16); float cc = cosr(rev), sn = sinr(rev);
        ac2re[r] = pk_f16(0.25f * cc, 0.25f * sn); ac2im[r] = pk_f16(-0.25f * sn, 0.25f * cc);
        bc3re[r] = pk_bf16(cc, -sn); bc3im[r] = pk_bf16(sn, cc); }
      { float cc = cosr(n2 * (1.0f / 128)), sn = sinr(n2 * (1.0f / 128)); ac4re[r] = pk_bf16(cc, -sn); ac4im[r] = pk_bf16(sn, cc); }
      t1c[r] = cosr(n1 * (1.0f / 2048)); t1s[r] = sinr(n1 * (1.0f / 2048));
      t2c[r] = FSC * cosr(n2 * (1.0f / 2048)); t2s[r] = FSC * sinr(n2 * (1.0f / 2048));
    }
    BAR();
    const u32x4 uin = *(LAS u32x4*)(cb + w * 1040 + lane * 16);
    const u32x4 fin = *(LAS u32x4*)(fb + w * 1040 + lane * 16);
    const bool special = (g == 0 && w == 0);
    const int npass = (special && (jp & 3) == 0) ? 2 : 1;
    f32x4 ore = z4, oim = z4;
    for (int pass = 0; pass < npass; ++pass) {
      u32x4 a = uin;
      float fr[4], fi[4];
#pragma unroll
      for (int r = 0; r < 4; ++r) { fr[r] = bf_lo(fin[r]); fi[r] = bf_hi(fin[r]); }
      if (special) {
        const int col = (npass == 2) ? (pass ? 256 : 0) : (jp < 4 ? 0 : 256);
#pragma unroll
        for (int r = 0; r < 4; ++r) {
          int ks = 8 * (p16 + 64 * t + 16 * r) + jp, km = (2048 - ks) & 2047;
          float ar = bf_lo(wr[ks * DF_ + col]), ai = bf_lo(wi[ks * DF_ + col]);
          float br = bf_lo(wr[km * DF_ + col]), bi = bf_lo(wi[km * DF_ + col]);
          fr[r] = 0.5f * (ar + br); fi[r] = 0.5f * (ai - bi);
          if (npass == 2) a[r] = pass ? (uin[r] >> 16) : (uin[r] & 0xffffu);
        }
      }
      f32x4 dre = mfma32_f16(a, bc1re, z4), dim = mfma32_f16(a, bc1im, z4);
#pragma unroll
      for (int r = 0; r < 4; ++r) a[r] = pk_f16(dre[r] * t1c[r] + dim[r] * t1s[r], dim[r] * t1c[r] - dre[r] * t1s[r]);
      dre = mfma32_f16(ac2re, a, z4); dim = mfma32_f16(ac2im, a, z4);
#pragma unroll
      for (int r = 0; r < 4; ++r) a[r] = pk_bf16(dre[r] * fr[r] - dim[r] * fi[r], dre[r] * fi[r] + dim[r] * fr[r]);
      dre = mfma32_bf16(a, bc3re, z4); dim = mfma32_bf16(a, bc3im, z4);
#pragma unroll
      for (int r = 0; r < 4; ++r) a[r] = pk_bf16(dre[r] * t2c[r] - dim[r] * t2s[r], dre[r] * t2s[r] + dim[r] * t2c[r]);
      dre = mfma32_bf16(ac4re, a, z4); dim = mfma32_bf16(ac4im, a, z4);
      if (npass == 2) { if (pass == 0) ore = dre; else oim = dre; }
      else { ore = dre; oim = dim; }
    }
    {
      u32x4 o;
#pragma unroll
      for (int r = 0; r < 4; ++r) o[r] = pk_bf16(ore[r], oim[r]);
      *(LAS u32x4*)(cb + w * 1040 + lane * 16) = o;
    }
    BAR();
    {
      int ps = tid >> 1, hf = tid & 1;
      int i = (ps >> 5) + 8 * (ps & 31);
      int pos = (i & 192) + 4 * (i & 15) + ((i >> 4) & 3);
      u32 o[4];
#pragma unroll
      for (int k = 0; k < 4; ++k) o[k] = *(LAS u32*)(cb + (4 * hf + k) * 1040 + pos * 4);
      st_sc1(rw, WS_V + b * 16384 + tid * 32, u32x4{o[0], tag, o[1], tag});
      st_sc1(rw, WS_V + b * 16384 + tid * 32 + 16, u32x4{o[2], tag, o[3], tag});
    }
  }
  STAMP(9);

  const int rt = (b & 7) * 4 + ((b >> 3) & 3), ct = b >> 5;
  u32x4 wf[7][4];
  {
    const char* src = wd + (size_t)(w * 224 + 8 * t) * 1024 + ct * 128 + p16 * 8;
    u32x2 rwv[7][8];
#pragma unroll
    for (int ks = 0; ks < 7; ++ks)
#pragma unroll
      for (int j = 0; j < 8; ++j) rwv[ks][j] = *(const u32x2*)(src + (size_t)(ks * 32 + j) * 1024);
    SB();
#pragma unroll
    for (int ks = 0; ks < 7; ++ks)
#pragma unroll
      for (int kp = 0; kp < 4; ++kp) {
        wf[ks][0][kp] = __builtin_amdgcn_perm(rwv[ks][2 * kp + 1][0], rwv[ks][2 * kp][0], 0x05040100u);
        wf[ks][1][kp] = __builtin_amdgcn_perm(rwv[ks][2 * kp + 1][0], rwv[ks][2 * kp][0], 0x07060302u);
        wf[ks][2][kp] = __builtin_amdgcn_perm(rwv[ks][2 * kp + 1][1], rwv[ks][2 * kp][1], 0x05040100u);
        wf[ks][3][kp] = __builtin_amdgcn_perm(rwv[ks][2 * kp + 1][1], rwv[ks][2 * kp][1], 0x07060302u);
      }
    SB();
  }
  STAMP(10);

  {
    LAS char* yb = sm + FS;
    const int s = b + 256 * w;
    const u32x4 xv = *(const u32x4*)(x + (size_t)s * 1024 + (4 * p16 + t) * 16);
    {
      const int g = 4 * w + (p16 >> 2);
      const u32 off = WS_V + ((b & 7) * 32 + (b >> 3)) * 64 + (p16 & 3) * 16 + (g * 8 + 2 * t) * 16384;
      const int j = p16 >> 1, po = p16 & 1;
      float c0, s0, c1, s1;
      { float rev = (float)(2 * t * j) * 0.125f; c0 = cosr(rev); s0 = sinr(rev); }
      { float rev = (float)((2 * t + 1) * j) * 0.125f; c1 = cosr(rev); s1 = sinr(rev); }
      const u32x2 a8 = po ? u32x2{pk_bf16(s0, c0), pk_bf16(s1, c1)} : u32x2{pk_bf16(c0, -s0), pk_bf16(c1, -s1)};
      u32x4 r0, r1;
      for (int spin = 0; spin < SPIN_CAP; ++spin) {
      asm volatile("" ::: "memory");
        r0 = ld_sc1(rw, off); r1 = ld_sc1(rw, off + 16384);
        if (all_ok((r0[1] == tag) & (r0[3] == tag) & (r1[1] == tag) & (r1[3] == tag))) break;
      }
      STAMP(11);
      u32x2 o0, o1;
#pragma unroll
      for (int u = 0; u < 2; ++u) {
        f32x4 d = mfma16_bf16(a8, u32x2{r0[2 * u], r1[2 * u]}, z4);
        if (u == 0 && w == 0) {
          u32 e0, e1;
          const bool on = (po == 0) ? (t < 2) : (t >= 2);
          if ((t & 1) == 0) e0 = po ? pk_bf16(0.f, 0.5f * c0) : pk_bf16(0.5f * c0, 0.f);
          else e0 = on ? pk_bf16(c0, -s0) : 0u;
          e1 = on ? pk_bf16(c1, -s1) : 0u;
          f32x4 ds = mfma16_bf16(u32x2{e0, e1}, u32x2{r0[0], r1[0]}, z4);
          if (p16 == 0) d = ds;
        }
        o0[u] = pk_bf16(d[0], d[1]); o1[u] = pk_bf16(d[2], d[3]);
      }
      *(LAS u32x2*)(yb + (2 * t) * 1024 + (16 * w + p16) * 8) = o0;
      *(LAS u32x2*)(yb + (2 * t + 1) * 1024 + (16 * w + p16) * 8) = o1;
    }
    u32x4 bi1re[2], bi1im[2], bi2;
    float twc[2][4], tws[2][4];
#pragma unroll
    for (int hh = 0; hh < 2; ++hh) {
      const int n2 = 8 * (p16 >> 2) + 4 * hh + (p16 & 3);
#pragma unroll
      for (int m = 0; m < 4; ++m) {
        float rev = (float)(n2 * (t + 4 * m)) * (1.0f / 32); float cc = cosr(rev), sn = sinr(rev);
        bi1re[hh][m] = pk_bf16(cc, -sn); bi1im[hh][m] = pk_bf16(sn, cc);
        float rev2 = (float)(n2 * (4 * t + m)) * (1.0f / 512);
        twc[hh][m] = cosr(rev2); tws[hh][m] = sinr(rev2);
      }
    }
#pragma unroll
    for (int r = 0; r < 4; ++r) { float rev = (float)(p16 * (4 * t + r)) * (1.0f / 16); bi2[r] = pk_bf16(cosr(rev), -sinr(rev)); }
    BAR();
    u32x4 a;
#pragma unroll
    for (int m = 0; m < 4; ++m) a[m] = *(LAS u32*)(yb + w * 1024 + (lane + 64 * m) * 4);
    const float nyq = bf_hi(*(LAS u32*)(yb + w * 1024));
    u32 ov[4];
#pragma unroll
    for (int hh = 0; hh < 2; ++hh) {
      f32x4 gre = mfma32_bf16(a, bi1re[hh], z4), gim = mfma32_bf16(a, bi1im[hh], z4);
      u32x4 bb;
#pragma unroll
      for (int r = 0; r < 4; ++r) bb[r] = pk_bf16(gre[r] * twc[hh][r] - gim[r] * tws[hh][r], gre[r] * tws[hh][r] + gim[r] * twc[hh][r]);
      f32x4 d = mfma32_bf16(bb, bi2, f32x4{nyq, -nyq, nyq, -nyq});
      ov[2 * hh] = pk_bf16(bf_lo(xv[2 * hh]) + d[0], bf_hi(xv[2 * hh]) + d[1]);
      ov[2 * hh + 1] = pk_bf16(bf_lo(xv[2 * hh + 1]) + d[2], bf_hi(xv[2 * hh + 1]) + d[3]);
    }
    st_sc1(rw, WS_R + s * 2048 + (4 * p16 + t) * 32, u32x4{ov[0], tag, ov[1], tag});
    st_sc1(rw, WS_R + s * 2048 + (4 * p16 + t) * 32 + 16, u32x4{ov[2], tag, ov[3], tag});
  }
  STAMP(12);

  {
    for (int spin = 0; spin < SPIN_CAP; ++spin) {
      asm volatile("" ::: "memory");
      u32 fl = __builtin_amdgcn_raw_buffer_load_b32(rw, WS_F2 + (rt >> 1) * 128 + (lane < 14 ? lane : 0) * 4, 0, 16);
      if (all_ok(fl == tag)) break;
    }
    __builtin_amdgcn_fence(__ATOMIC_ACQUIRE, "wavefront");
    STAMP(13);
    const u32 hs = WS_H + ((rt * 4) * 56 + w * 7) * 1024 + lane * 16;
    u32x4 hf[4][7];
#pragma unroll
    for (int tt = 0; tt < 3; ++tt)
#pragma unroll
      for (int ks = 0; ks < 7; ++ks) hf[tt][ks] = ld_sc1(rw, hs + (tt * 56 + ks) * 1024);
    SB();
#pragma unroll
    for (int tt = 0; tt < 4; ++tt) {
      f32x4 acc[4] = {z4, z4, z4, z4};
#pragma unroll
      for (int ks = 0; ks < 7; ++ks) {
#pragma unroll
        for (int u = 0; u < 4; ++u) acc[u] = mfma32_bf16(wf[ks][u], hf[tt][ks], acc[u]);
        if (tt == 0) { SB(); hf[3][ks] = ld_sc1(rw, hs + (3 * 56 + ks) * 1024); SB(); }
      }
#pragma unroll
      for (int u = 0; u < 4; ++u) *(LAS f32x4*)(sm + w * 16384 + (tt * 4 + u) * 1024 + lane * 16) = acc[u];
      SB();
    }
    STAMP(14);
    const int tt = w >> 1, hh = w & 1;
    const int row = rt * 64 + tt * 16 + p16;
    u32x4 r0, r1;
    for (int spin = 0; spin < SPIN_CAP; ++spin) {
      asm volatile("" ::: "memory");
      const u32 off = WS_R + row * 2048 + ct * 256 + t * 64 + hh * 32;
      r0 = ld_sc1(rw, off); r1 = ld_sc1(rw, off + 16);
      if (all_ok((r0[1] == tag) & (r0[3] == tag) & (r1[1] == tag) & (r1[3] == tag))) break;
    }
    BAR();
    f32x2 sacc[4];
#pragma unroll
    for (int u = 0; u < 4; ++u) {
      sacc[u] = f32x2{0.f, 0.f};
#pragma unroll
      for (int pw = 0; pw < 8; ++pw) sacc[u] += *(LAS f32x2*)(sm + pw * 16384 + (tt * 4 + u) * 1024 + lane * 16 + hh * 8);
    }
    u32x4 o;
    o[0] = pk_bf16(bf_lo(r0[0]) + sacc[0][0], bf_hi(r0[0]) + sacc[1][0]);
    o[1] = pk_bf16(bf_lo(r0[2]) + sacc[2][0], bf_hi(r0[2]) + sacc[3][0]);
    o[2] = pk_bf16(bf_lo(r1[0]) + sacc[0][1], bf_hi(r1[0]) + sacc[1][1]);
    o[3] = pk_bf16(bf_lo(r1[2]) + sacc[2][1], bf_hi(r1[2]) + sacc[3][1]);
    *(u32x4*)(out + (size_t)row * 1024 + ct * 128 + t * 32 + hh * 16) = o;
    if (b == 0 && tid == 0) __builtin_amdgcn_raw_buffer_store_b32(tag, rw, WS_EPOCH, 0, 16);
  }
  STAMP(15);
  if (blockIdx.x == PROBE_WG && threadIdx.x < 64) { while (__builtin_amdgcn_s_memrealtime() < PROBE_T + 6000) __builtin_amdgcn_s_sleep(1); }
}

extern "C" void kernel_launch(void* const* d_in, const int* in_sizes, int n_in, void* d_out, int out_size, void* d_ws, size_t ws_size,
                              hipStream_t stream) {
  (void)hipFuncSetAttribute((const void*)fused_kernel, hipFuncAttributeMaxDynamicSharedMemorySize, LDS_BYTES);
  fused_kernel<<<256, 512, LDS_BYTES, stream>>>((const char*)d_in[0], (const unsigned short*)d_in[1], (const unsigned short*)d_in[2], (const char*)d_in[4],
                                                (const char*)d_in[5], (const char*)d_in[6], (char*)d_ws, (char*)d_out);
}
```

```cpp
#define PROBE_WG 0
#define PROBE_T tA
#include <hip/hip_runtime.h>
#include <stdint.h>

#define LAS __attribute__((address_space(3)))
#define GAS __attribute__((address_space(1)))
#define DI __device__ __forceinline__
typedef unsigned u32;
typedef unsigned u32x2 __attribute__((ext_vector_type(2)));
typedef unsigned u32x4 __attribute__((ext_vector_type(4)));
typedef float f32x2 __attribute__((ext_vector_type(2)));
typedef float f32x4 __attribute__((ext_vector_type(4)));
typedef float f32x16 __attribute__((ext_vector_type(16)));
typedef short s16x4 __attribute__((ext_vector_type(4)));
typedef _Float16 h16x2 __attribute__((ext_vector_type(2)));
typedef _Float16 h16x4 __attribute__((ext_vector_type(4)));
typedef _Float16 h16x8 __attribute__((ext_vector_type(8)));
typedef __bf16 b16x2 __attribute__((ext_vector_type(2)));
typedef __bf16 b16x8 __attribute__((ext_vector_type(8)));
typedef __amdgpu_buffer_rsrc_t rsrc_t;

constexpr int F_ = 1792, DF_ = 257;
constexpr u32 WS_EPOCH = 0;
constexpr u32 WS_F2 = 4096;
constexpr u32 WS_ST = 8192;
constexpr u32 WS_CS = WS_ST + 16384;
constexpr u32 WS_U = 65536;
constexpr u32 WS_V = WS_U + 4194304;
constexpr u32 WS_R = WS_V + 4194304;
constexpr u32 WS_H = WS_R + 4194304;
constexpr u32 WS_END = WS_H + 7340032;
constexpr int LDS_BYTES = 163840;
constexpr int G1_STAGE = 49152;
constexpr int FS = 3 * G1_STAGE;
constexpr int SPIN_CAP = 1 << 17;
constexpr size_t WS_DBG = 64 << 20;
#define STAMP(k) do {} while (0)
#define PROBE_END() do {} while (0)

DI u32 pk_f16(float a, float b) { h16x2 p = {(_Float16)a, (_Float16)b}; return __builtin_bit_cast(u32, p); }
DI u32 pk_bf16(float a, float b) { b16x2 p = {(__bf16)a, (__bf16)b}; return __builtin_bit_cast(u32, p); }
DI float bf_lo(u32 v) { return __builtin_bit_cast(float, v << 16); }
DI float bf_hi(u32 v) { return __builtin_bit_cast(float, v & 0xffff0000u); }
DI float rnd_bf(float a) { return bf_lo(pk_bf16(a, a)); }
DI float cosr(float rev) { return __builtin_amdgcn_cosf(rev); }
DI float sinr(float rev) { return __builtin_amdgcn_sinf(rev); }
DI f32x4 mfma16_f16(u32x2 a, u32x2 b, f32x4 c) { return __builtin_amdgcn_mfma_f32_16x16x16f16(__builtin_bit_cast(h16x4, a), __builtin_bit_cast(h16x4, b), c, 0, 0, 0); }
DI f32x4 mfma16_bf16(u32x2 a, u32x2 b, f32x4 c) { return __builtin_amdgcn_mfma_f32_16x16x16bf16_1k(__builtin_bit_cast(s16x4, a), __builtin_bit_cast(s16x4, b), c, 0, 0, 0); }
DI f32x4 mfma32_f16(u32x4 a, u32x4 b, f32x4 c) { return __builtin_amdgcn_mfma_f32_16x16x32_f16(__builtin_bit_cast(h16x8, a), __builtin_bit_cast(h16x8, b), c, 0, 0, 0); }
DI f32x4 mfma32_bf16(u32x4 a, u32x4 b, f32x4 c) { return __builtin_amdgcn_mfma_f32_16x16x32_bf16(__builtin_bit_cast(b16x8, a), __builtin_bit_cast(b16x8, b), c, 0, 0, 0); }
DI f32x16 mfma3232(u32x4 a, u32x4 b, f32x16 c) { return __builtin_amdgcn_mfma_f32_32x32x16_bf16(__builtin_bit_cast(b16x8, a), __builtin_bit_cast(b16x8, b), c, 0, 0, 0); }
DI u32x2 tr_read(LAS char* p) { return __builtin_bit_cast(u32x2, __builtin_amdgcn_ds_read_tr16_b64_v4i16((LAS s16x4*)p)); }
#define SB() __builtin_amdgcn_sched_barrier(0)
#define BAR() asm volatile("s_waitcnt lgkmcnt(0)\n\ts_barrier" ::: "memory")
#define WAITV(n) asm volatile("s_waitcnt vmcnt(%0)" :: "i"(n) : "memory")
DI u32x4 mk_srd(const void* p, u32 bytes) { uint64_t a = (uint64_t)p; return u32x4{(u32)a, (u32)(a >> 32), bytes, 0x00020000u}; }
DI void glds16(u32 ldsaddr, u32 voff, u32x4 srd, u32 soff) {
  asm volatile("s_mov_b32 m0, %0\n\ts_nop 0\n\tbuffer_load_dwordx4 %1, %2, %3 offen lds" :: "s"(ldsaddr), "v"(voff), "s"(srd), "s"(soff) : "memory");
}
DI u32x4 ld_sc1(rsrc_t r, u32 off) { return __builtin_amdgcn_raw_buffer_load_b128(r, off, 0, 16); }
DI u32x2 ld_sc1_2(rsrc_t r, u32 off) { return __builtin_amdgcn_raw_buffer_load_b64(r, off, 0, 16); }
DI void st_sc1(rsrc_t r, u32 off, u32x4 v) { __builtin_amdgcn_raw_buffer_store_b128(v, r, off, 0, 16); }
DI void st_sc1_2(rsrc_t r, u32 off, u32x2 v) { __builtin_amdgcn_raw_buffer_store_b64(v, r, off, 0, 16); }
DI bool all_ok(bool ok) { return __builtin_amdgcn_ballot_w64(!ok) == 0; }

template <int CTRL> DI float dpp_add(float v) {
  int o = __builtin_amdgcn_update_dpp(0, __builtin_bit_cast(int, v), CTRL, 0xf, 0xf, true);
  return v + __builtin_bit_cast(float, o);
}
DI float wave_sum(float v) {
  v = dpp_add<0xB1>(v); v = dpp_add<0x4E>(v); v = dpp_add<0x141>(v); v = dpp_add<0x140>(v);
  u32 u = __builtin_bit_cast(u32, v);
  auto a = __builtin_amdgcn_permlane16_swap(u, u, false, false);
  v = __builtin_bit_cast(float, (u32)a[0]) + __builtin_bit_cast(float, (u32)a[1]);
  u = __builtin_bit_cast(u32, v);
  auto b = __builtin_amdgcn_permlane32_swap(u, u, false, false);
  return __builtin_bit_cast(float, (u32)b[0]) + __builtin_bit_cast(float, (u32)b[1]);
}

struct G1L { u32x4 sx, sg, su; u32 vx, vg, vu, lw; };
DI void g1_issue1(const G1L& L, int st, int k) {
  const u32 lb = L.lw + (st % 3) * G1_STAGE + k * 8192;
  if (k < 2) glds16(lb, L.vx, L.sx, st * 128 + k * 65536);
  else if (k < 4) glds16(lb, L.vg, L.sg, (st * 64 + (k - 2) * 32) * 3584);
  else glds16(lb, L.vu, L.su, (st * 64 + (k - 4) * 32) * 3584);
}
struct Frag { u32x4 bx[2], aw[2]; };

typedef unsigned u32x16 __attribute__((ext_vector_type(16)));
__global__ __launch_bounds__(512, 2) void fused_kernel(const char* a0, const unsigned short* a1, const unsigned short* a2, const char* a3, const char* a4, const char* a5, char* a6, char* a7) {
  unsigned long long tA, tB; u32x16 ka;
  asm volatile("s_memrealtime %0\n\ts_load_dwordx16 %2, %3, 0x0\n\ts_waitcnt lgkmcnt(0)\n\ts_memrealtime %1\n\ts_waitcnt lgkmcnt(0)" : "=&s"(tA), "=&s"(tB), "=&s"(ka) : "s"(__builtin_amdgcn_kernarg_segment_ptr()) : "memory");
#define KP(i) (((uint64_t)ka[2 * (i) + 1] << 32) | ka[2 * (i)])
  const char* __restrict__ x = (const char*)(const GAS char*)KP(0); const unsigned short* __restrict__ wr = (const unsigned short*)(const GAS unsigned short*)KP(1); const unsigned short* __restrict__ wi = (const unsigned short*)(const GAS unsigned short*)KP(2);
  const char* __restrict__ wg = (const char*)(const GAS char*)KP(3); const char* __restrict__ wu = (const char*)(const GAS char*)KP(4); const char* __restrict__ wd = (const char*)(const GAS char*)KP(5);
  char* __restrict__ ws = (char*)(GAS char*)KP(6); char* __restrict__ out = (char*)(GAS char*)KP(7);
  extern __shared__ __attribute__((aligned(16))) char smem[];
  LAS char* sm = (LAS char*)smem;
  const int b = blockIdx.x, tid = threadIdx.x, lane = tid & 63, w = __builtin_amdgcn_readfirstlane(tid >> 6);
  const int p16 = lane & 15, t = lane >> 4;
  const f32x4 z4 = {0.f, 0.f, 0.f, 0.f};
  u32 pfd = 0;
  unsigned long long pr_t0 = 0, pr_tk = 0;
  {
    uint64_t pc; asm volatile("s_getpc_b64 %0" : "=s"(pc));
    if (b < 8 && tid < CODE_LINES) asm volatile("global_load_dword %0, %1, %2" : "+v"(pfd) : "v"(tid * 128), "s"(pc) : "memory");
  }
  STAMP(0);
  const bool is_gemm = b >= 32;
  const int q_ = b - 32, xc = q_ & 7, r_ = q_ >> 3;
  const int mi = (xc >> 1) * 4 + (r_ & 3), fj = (xc & 1) * 7 + (r_ >> 2);
  G1L L;
  L.sx = mk_srd(x, 2097152); L.sg = mk_srd(wg, 1835008); L.su = mk_srd(wu, 1835008);
  L.lw = (u32)(uintptr_t)sm + w * 1024;
  glds16(L.lw + FS, (b + 256 * w) * 1024 + lane * 16, L.sx, 0);
  if (is_gemm) {
    { int row = w * 8 + (lane >> 3), pos = lane & 7; L.vx = (mi * 128 + row) * 1024 + ((pos ^ ((row >> 1) & 7)) << 4); }
    { int k = w * 4 + t; L.vg = k * 3584 + fj * 256 + ((p16 ^ (2 * (k & 3))) << 4); L.vu = L.vg ^ 128; }
#pragma unroll
    for (int st = 0; st < 3; ++st)
#pragma unroll
      for (int k = 0; k < 6; ++k) g1_issue1(L, st, k);
  }
  const rsrc_t rw = __builtin_amdgcn_make_buffer_rsrc(ws, 0, WS_END, 0x00020000);
  const u32 tag = *(const u32*)(ws + WS_EPOCH) * 0x9E3779B1u + 0x7F4A7C15u;

  {
    u32x2 b1re, b1im;
    {
      float c[4], sn[4];
#pragma unroll
      for (int jj = 0; jj < 4; ++jj) { float rev = (float)((4 * t + jj) * p16) * (1.0f / 16); c[jj] = 0.25f * cosr(rev); sn[jj] = -0.25f * sinr(rev); }
      b1re = u32x2{pk_f16(c[0], c[1]), pk_f16(c[2], c[3])};
      b1im = u32x2{pk_f16(sn[0], sn[1]), pk_f16(sn[2], sn[3])};
    }
    float twc[2][4], tws[2][4];
    u32x4 b2re[2], b2im[2];
#pragma unroll
    for (int hh = 0; hh < 2; ++hh)
#pragma unroll
      for (int r = 0; r < 4; ++r) {
        float nk = (float)((16 * hh + 4 * t + r) * p16);
        twc[hh][r] = cosr(nk * (1.0f / 512)); tws[hh][r] = sinr(nk * (1.0f / 512));
        float c = 0.125f * cosr(nk * (1.0f / 32)), sn = 0.125f * sinr(nk * (1.0f / 32));
        b2re[hh][r] = pk_f16(c, sn); b2im[hh][r] = pk_f16(-sn, c);
      }
    const int jp = p16 >> 1, po = p16 & 1;
    float c0, s0, c1, s1;
    { float rev = (float)(2 * t * jp) * 0.125f; c0 = 0.5f * cosr(rev); s0 = 0.5f * sinr(rev); }
    { float rev = (float)((2 * t + 1) * jp) * 0.125f; c1 = 0.5f * cosr(rev); s1 = 0.5f * sinr(rev); }
    const u32x2 a8 = po ? u32x2{pk_f16(-s0, c0), pk_f16(-s1, c1)} : u32x2{pk_f16(c0, s0), pk_f16(c1, s1)};
    LAS char* img = sm + FS + w * 1024;
    SB();
    if (is_gemm) WAITV(18); else WAITV(0);
    asm volatile("" :: "v"(pfd));
    STAMP(1);
    const u32x4 xv = *(LAS u32x4*)(img + lane * 16);
    float v[8];
#pragma unroll
    for (int k = 0; k < 4; ++k) { v[2 * k] = bf_lo(xv[k]); v[2 * k + 1] = bf_hi(xv[k]); }
    float sum = ((v[0] + v[1]) + (v[2] + v[3])) + ((v[4] + v[5]) + (v[6] + v[7]));
    const float mean = wave_sum(sum) * (1.0f / 512);
    float sq0 = 0.f, sq1 = 0.f;
#pragma unroll
    for (int k = 0; k < 4; ++k) { float d = v[k] - mean, e = v[k + 4] - mean; sq0 += d * d; sq1 += e * e; }
    const float var = wave_sum(sq0 + sq1) * (1.0f / 512);
    const float mb = rnd_bf(mean);
    const float sd = rnd_bf(__builtin_amdgcn_sqrtf(rnd_bf(rnd_bf(var) + 1e-5f)));
    const float rs = __builtin_amdgcn_rcpf(sd);
    if (lane == 0) st_sc1_2(rw, WS_ST + (b + 256 * w) * 8, u32x2{pk_bf16(mb, sd), tag});
    u32x4 xh;
#pragma unroll
    for (int k = 0; k < 4; ++k) {
      u32 dp = pk_bf16(v[2 * k] - mb, v[2 * k + 1] - mb);
      u32 np = pk_bf16(bf_lo(dp) * rs, bf_hi(dp) * rs);
      xh[k] = pk_f16(bf_lo(np), bf_hi(np));
    }
    *(LAS u32x4*)(img + ((lane ^ ((t & 1) << 1)) << 4)) = xh;
    __builtin_amdgcn_fence(__ATOMIC_RELEASE, "wavefront");
    __builtin_amdgcn_wave_barrier();
    f32x4 d1re[2], d1im[2];
#pragma unroll
    for (int hh = 0; hh < 2; ++hh) {
      u32x2 a = tr_read(img + (4 * t + (p16 >> 2)) * 64 + ((hh ^ (t & 1)) << 5) + (p16 & 3) * 8);
      d1re[hh] = mfma16_f16(a, b1re, z4);
      d1im[hh] = mfma16_f16(a, b1im, z4);
    }
    float ny = ((d1re[0][0] - d1re[0][1]) + (d1re[0][2] - d1re[0][3])) + ((d1re[1][0] - d1re[1][1]) + (d1re[1][2] - d1re[1][3]));
    f32x4 d2re = z4, d2im = z4;
#pragma unroll
    for (int hh = 0; hh < 2; ++hh) {
      u32x4 a;
#pragma unroll
      for (int r = 0; r < 4; ++r)
        a[r] = pk_f16(d1re[hh][r] * twc[hh][r] + d1im[hh][r] * tws[hh][r], d1im[hh][r] * twc[hh][r] - d1re[hh][r] * tws[hh][r]);
      d2re = mfma32_f16(a, b2re[hh], d2re);
      d2im = mfma32_f16(a, b2im[hh], d2im);
    }
    {
      int nb = __builtin_bit_cast(int, ny);
      float n1_ = __builtin_bit_cast(float, __builtin_amdgcn_readlane(nb, 16));
      float n2_ = __builtin_bit_cast(float, __builtin_amdgcn_readlane(nb, 32));
      float n3_ = __builtin_bit_cast(float, __builtin_amdgcn_readlane(nb, 48));
      float tot = 0.125f * ((ny + n1_) + (n2_ + n3_));
      if (lane == 0) d2im[0] = tot;
    }
    LAS char* ex = sm + FS + 8192;
    {
      u32x4 o;
#pragma unroll
      for (int r = 0; r < 4; ++r) o[r] = pk_f16(d2re[r], d2im[r]);
      int e = 4 * p16 + t; e ^= (e >> 3) & 3;
      *(LAS u32x4*)(ex + w * 1024 + e * 16) = o;
    }
    BAR();
    {
      const int wq = w & 3, hf = w >> 2;
      int e = 16 * wq + p16; e ^= (e >> 3) & 3;
      u32x4 r0 = *(LAS u32x4*)(ex + (2 * t) * 1024 + e * 16);
      u32x4 r1 = *(LAS u32x4*)(ex + (2 * t + 1) * 1024 + e * 16);
      u32 o[4];
#pragma unroll
      for (int u = 0; u < 4; ++u) {
        f32x4 d = mfma16_f16(a8, u32x2{r0[u], r1[u]}, z4);
        if (u == 0 && wq == 0) {
          float e0 = po ? -s0 : c0, e1 = po ? -s1 : c1;
          u32x2 as = (jp & 3) == 0 ? a8 : (jp < 4 ? u32x2{pk_f16(e0, 0.f), pk_f16(e1, 0.f)} : u32x2{pk_f16(0.f, e0), pk_f16(0.f, e1)});
          f32x4 ds = mfma16_f16(as, u32x2{r0[0], r1[0]}, z4);
          if (p16 == 0) d = ds;
        }
        o[u] = hf ? pk_f16(d[2], d[3]) : pk_f16(d[0], d[1]);
      }
      const u32 ub = WS_U + b * 16384 + (2 * t + hf) * 2048 + (64 * wq + 4 * p16) * 8;
      st_sc1(rw, ub, u32x4{o[0], tag, o[1], tag});
      st_sc1(rw, ub + 16, u32x4{o[2], tag, o[3], tag});
    }
  }
  STAMP(2);

  if (is_gemm) {
    const int wr_ = w >> 1, wt = w & 1, h = lane >> 5;
    int xoff[4], woff[2];
#pragma unroll
    for (int ks = 0; ks < 4; ++ks) { int tok = wt * 64 + (lane & 31); xoff[ks] = tok * 128 + (((2 * ks + h) ^ ((tok >> 1) & 7)) << 4); }
    {
      int arr = t & 1, q = p16 >> 2, p = lane & 3;
#pragma unroll
      for (int m = 0; m < 2; ++m)
        woff[m] = 16384 + arr * 16384 + (8 * h + q) * 256 + (((wr_ * 2 + m) ^ (q | (arr << 2))) << 5) + (p & 1) * 16 + (p >> 1) * 8;
    }
    auto rd = [&](Frag& f, int st, int ks) {
      LAS char* buf = sm + (st % 3) * G1_STAGE;
#pragma unroll
      for (int n = 0; n < 2; ++n) f.bx[n] = *(LAS u32x4*)(buf + xoff[ks] + n * 4096);
#pragma unroll
      for (int m = 0; m < 2; ++m) {
        u32x2 lo = tr_read(buf + woff[m] + ks * 4096), hi = tr_read(buf + woff[m] + ks * 4096 + 1024);
        f.aw[m] = u32x4{lo[0], lo[1], hi[0], hi[1]};
      }
    };
    f32x16 acc[2][2];
#pragma unroll
    for (int m = 0; m < 2; ++m)
#pragma unroll
      for (int n = 0; n < 2; ++n)
#pragma unroll
        for (int k = 0; k < 16; ++k) acc[m][n][k] = 0.f;
    Frag fa, fb;
    auto mm = [&](Frag& f, int ls, int k0) {
      acc[0][0] = mfma3232(f.aw[0], f.bx[0], acc[0][0]);
      SB(); if (ls < 8 && k0 >= 0) g1_issue1(L, ls, k0); SB();
      acc[0][1] = mfma3232(f.aw[0], f.bx[1], acc[0][1]);
      acc[1][1] = mfma3232(f.aw[1], f.bx[1], acc[1][1]);
      SB(); if (ls < 8 && k0 >= 0) g1_issue1(L, ls, k0 + 1); SB();
      acc[1][0] = mfma3232(f.aw[1], f.bx[0], acc[1][0]);
    };
    SB();
    WAITV(12 + 3);
    __builtin_amdgcn_s_barrier();
    STAMP(3);
    rd(fa, 0, 0);
#pragma unroll
    for (int st = 0; st < 8; ++st) {
      const int ls = st == 0 ? 8 : st + 2;
      SB(); rd(fb, st, 1); SB(); mm(fa, ls, 2);
      SB(); rd(fa, st, 2); SB(); mm(fb, ls, 4);
      SB(); rd(fb, st, 3); SB(); mm(fa, ls, -1);
      SB();
      if (st < 7) {
        if (st < 6) WAITV(6); else WAITV(0);
        BAR();
        SB();
        rd(fa, st + 1, 0);
        SB();
      }
      mm(fb, st + 3, 0);
      SB();
    }
    STAMP(4);
    u32x2 stv[2]; u32x4 cs[2][2][4];
    for (int spin = 0; spin < SPIN_CAP; ++spin) {
      asm volatile("" ::: "memory");
      bool ok = true;
#pragma unroll
      for (int n = 0; n < 2; ++n) { stv[n] = ld_sc1_2(rw, WS_ST + (mi * 128 + wt * 64 + n * 32 + (lane & 31)) * 8); }
#pragma unroll
      for (int m = 0; m < 2; ++m)
#pragma unroll
        for (int a = 0; a < 2; ++a)
#pragma unroll
          for (int k = 0; k < 4; ++k) cs[m][a][k] = ld_sc1(rw, WS_CS + (a * F_ + fj * 128 + (wr_ * 2 + m) * 16 + 8 * h + 2 * k) * 8);
#pragma unroll
      for (int n = 0; n < 2; ++n) ok &= stv[n][1] == tag;
#pragma unroll
      for (int m = 0; m < 2; ++m)
#pragma unroll
        for (int a = 0; a < 2; ++a)
#pragma unroll
          for (int k = 0; k < 4; ++k) ok &= (cs[m][a][k][1] == tag) & (cs[m][a][k][3] == tag);
      if (all_ok(ok)) break;
    }
    STAMP(5);
#pragma unroll
    for (int n = 0; n < 2; ++n) {
      const int tok = mi * 128 + wt * 64 + n * 32 + (lane & 31);
      const float nm = -bf_lo(stv[n][0]);
      const float rs = __builtin_amdgcn_rcpf(bf_hi(stv[n][0])), rs2 = rs * rs;
#pragma unroll
      for (int m = 0; m < 2; ++m) {
        float hv[8];
#pragma unroll
        for (int k = 0; k < 8; ++k) {
          const u32 cgu = cs[m][0][k >> 1][(k & 1) * 2], cuu = cs[m][1][k >> 1][(k & 1) * 2];
          float cg = __builtin_bit_cast(float, cgu), cu = __builtin_bit_cast(float, cuu);
          float gg = __builtin_fmaf(nm, cg, acc[m][n][k]), uu = __builtin_fmaf(nm, cu, acc[m][n][8 + k]);
          hv[k] = __builtin_fmaxf(gg, 0.f) * (uu * rs2);
        }
        const int fo = fj * 16 + (wr_ * 2 + m) * 2 + h;
        st_sc1(rw, WS_H + ((tok >> 4) * 56 + (fo >> 2)) * 1024 + (fo & 3) * 256 + (tok & 15) * 16,
               u32x4{pk_bf16(hv[0], hv[1]), pk_bf16(hv[2], hv[3]), pk_bf16(hv[4], hv[5]), pk_bf16(hv[6], hv[7])});
      }
    }
    STAMP(6);
    WAITV(0);
    BAR();
    if (tid == 0) __builtin_amdgcn_raw_buffer_store_b32(tag, rw, WS_F2 + mi * 128 + fj * 4, 0, 16);
    STAMP(7);
  } else if (b < 28) {
    const char* wsrc = b < 14 ? wg : wu;
    const int ct = b < 14 ? b : b - 14;
    const int r0 = tid >> 4;
    float acc[8] = {0, 0, 0, 0, 0, 0, 0, 0};
    u32x4 v[16];
#pragma unroll
    for (int k = 0; k < 16; ++k) v[k] = *(const u32x4*)(wsrc + (size_t)(r0 + 32 * k) * 3584 + ct * 256 + p16 * 16);
#pragma unroll
    for (int k = 0; k < 16; ++k)
#pragma unroll
      for (int q = 0; q < 4; ++q) { acc[2 * q] += bf_lo(v[k][q]); acc[2 * q + 1] += bf_hi(v[k][q]); }
    LAS float* red = (LAS float*)sm;
    *(LAS f32x4*)(red + r0 * 128 + p16 * 8) = f32x4{acc[0], acc[1], acc[2], acc[3]};
    *(LAS f32x4*)(red + r0 * 128 + p16 * 8 + 4) = f32x4{acc[4], acc[5], acc[6], acc[7]};
    BAR();
    if (tid < 128) {
      float sacc = 0.f;
#pragma unroll
      for (int k = 0; k < 32; ++k) sacc += red[k * 128 + tid];
      st_sc1_2(rw, WS_CS + (b * 128 + tid) * 8, u32x2{__builtin_bit_cast(u32, sacc), tag});
    }
    BAR();
  }

  {
    const int jp = b & 7, g = b >> 3;
    LAS char* cb = sm;
    LAS char* fb = sm + 8 * 1040;
    {
      int q = tid & 255, arr = tid >> 8;
      const unsigned short* wsrc = arr ? wi : wr;
      int e0 = (8 * q + jp) * DF_ + 8 * g;
      const u32* pa = (const u32*)(wsrc + (e0 & ~1));
      u32 d0 = pa[0], d1 = pa[1], d2 = pa[2], d3 = pa[3], d4 = pa[4];
      const int i = tid >> 1, hf = tid & 1;
      u32x4 u0, u1;
      for (int spin = 0; spin < SPIN_CAP; ++spin) {
      asm volatile("" ::: "memory");
        const u32 off = WS_U + i * 16384 + jp * 2048 + g * 64 + hf * 32;
        u0 = ld_sc1(rw, off); u1 = ld_sc1(rw, off + 16);
        if (all_ok((u0[1] == tag) & (u0[3] == tag) & (u1[1] == tag) & (u1[3] == tag))) break;
      }
      const int pos = (i & 192) + 4 * (i & 15) + ((i >> 4) & 3);
      *(LAS u32*)(cb + (4 * hf + 0) * 1040 + pos * 4) = u0[0];
      *(LAS u32*)(cb + (4 * hf + 1) * 1040 + pos * 4) = u0[2];
      *(LAS u32*)(cb + (4 * hf + 2) * 1040 + pos * 4) = u1[0];
      *(LAS u32*)(cb + (4 * hf + 3) * 1040 + pos * 4) = u1[2];
      u32 sh = (e0 & 1) * 16;
      u32 f[4] = {__builtin_amdgcn_alignbit(d1, d0, sh), __builtin_amdgcn_alignbit(d2, d1, sh), __builtin_amdgcn_alignbit(d3, d2, sh), __builtin_amdgcn_alignbit(d4, d3, sh)};
      int posq = (q & 192) + 4 * (q & 15) + ((q >> 4) & 3);
#pragma unroll
      for (int k = 0; k < 4; ++k) {
        *(LAS unsigned short*)(fb + (2 * k) * 1040 + posq * 4 + arr * 2) = (unsigned short)(f[k] & 0xffff);
        *(LAS unsigned short*)(fb + (2 * k + 1) * 1040 + posq * 4 + arr * 2) = (unsigned short)(f[k] >> 16);
      }
    }
    STAMP(8);
    u32x4 bc1re, bc1im, ac2re, ac2im, bc3re, bc3im, ac4re, ac4im;
    float t1c[4], t1s[4], t2c[4], t2s[4];
    const float FSC = (2.0f / (2048.0f * 512.0f)) * (4.0f * 8.0f * 2.0f * 4.0f * 4.0f);
#pragma unroll
    for (int r = 0; r < 4; ++r) {
      const int y = 4 * t + r;
      const float n1 = (float)(y * (8 * p16 + jp)), n2 = (float)(p16 * (8 * y + jp));
      { float cc = 0.25f * cosr(n1 * (1.0f / 128)), sn = 0.25f * sinr(n1 * (1.0f / 128)); bc1re[r] = pk_f16(cc, sn); bc1im[r] = pk_f16(-sn, cc); }
      { float rev = (float)(y * p16) * (1.0f / 16); float cc = cosr(rev), sn = sinr(rev);
        ac2re[r] = pk_f16(0.25f * cc, 0.25f * sn); ac2im[r] = pk_f16(-0.25f * sn, 0.25f * cc);
        bc3re[r] = pk_bf16(cc, -sn); bc3im[r] = pk_bf16(sn, cc); }
      { float cc = cosr(n2 * (1.0f / 128)), sn = sinr(n2 * (1.0f / 128)); ac4re[r] = pk_bf16(cc, -sn); ac4im[r] = pk_bf16(sn, cc); }
      t1c[r] = cosr(n1 * (1.0f / 2048)); t1s[r] = sinr(n1 * (1.0f / 2048));
      t2c[r] = FSC * cosr(n2 * (1.0f / 2048)); t2s[r] = FSC * sinr(n2 * (1.0f / 2048));
    }
    BAR();
    const u32x4 uin = *(LAS u32x4*)(cb + w * 1040 + lane * 16);
    const u32x4 fin = *(LAS u32x4*)(fb + w * 1040 + lane * 16);
    const bool special = (g == 0 && w == 0);
    const int npass = (special && (jp & 3) == 0) ? 2 : 1;
    f32x4 ore = z4, oim = z4;
    for (int pass = 0; pass < npass; ++pass) {
      u32x4 a = uin;
      float fr[4], fi[4];
#pragma unroll
      for (int r = 0; r < 4; ++r) { fr[r] = bf_lo(fin[r]); fi[r] = bf_hi(fin[r]); }
      if (special) {
        const int col = (npass == 2) ? (pass ? 256 : 0) : (jp < 4 ? 0 : 256);
#pragma unroll
        for (int r = 0; r < 4; ++r) {
          int ks = 8 * (p16 + 64 * t + 16 * r) + jp, km = (2048 - ks) & 2047;
          float ar = bf_lo(wr[ks * DF_ + col]), ai = bf_lo(wi[ks * DF_ + col]);
          float br = bf_lo(wr[km * DF_ + col]), bi = bf_lo(wi[km * DF_ + col]);
          fr[r] = 0.5f * (ar + br); fi[r] = 0.5f * (ai - bi);
          if (npass == 2) a[r] = pass ? (uin[r] >> 16) : (uin[r] & 0xffffu);
        }
      }
      f32x4 dre = mfma32_f16(a, bc1re, z4), dim = mfma32_f16(a, bc1im, z4);
#pragma unroll
      for (int r = 0; r < 4; ++r) a[r] = pk_f16(dre[r] * t1c[r] + dim[r] * t1s[r], dim[r] * t1c[r] - dre[r] * t1s[r]);
      dre = mfma32_f16(ac2re, a, z4); dim = mfma32_f16(ac2im, a, z4);
#pragma unroll
      for (int r = 0; r < 4; ++r) a[r] = pk_bf16(dre[r] * fr[r] - dim[r] * fi[r], dre[r] * fi[r] + dim[r] * fr[r]);
      dre = mfma32_bf16(a, bc3re, z4); dim = mfma32_bf16(a, bc3im, z4);
#pragma unroll
      for (int r = 0; r < 4; ++r) a[r] = pk_bf16(dre[r] * t2c[r] - dim[r] * t2s[r], dre[r] * t2s[r] + dim[r] * t2c[r]);
      dre = mfma32_bf16(ac4re, a, z4); dim = mfma32_bf16(ac4im, a, z4);
      if (npass == 2) { if (pass == 0) ore = dre; else oim = dre; }
      else { ore = dre; oim = dim; }
    }
    {
      u32x4 o;
#pragma unroll
      for (int r = 0; r < 4; ++r) o[r] = pk_bf16(ore[r], oim[r]);
      *(LAS u32x4*)(cb + w * 1040 + lane * 16) = o;
    }
    BAR();
    {
      int ps = tid >> 1, hf = tid & 1;
      int i = (ps >> 5) + 8 * (ps & 31);
      int pos = (i & 192) + 4 * (i & 15) + ((i >> 4) & 3);
      u32 o[4];
#pragma unroll
      for (int k = 0; k < 4; ++k) o[k] = *(LAS u32*)(cb + (4 * hf + k) * 1040 + pos * 4);
      st_sc1(rw, WS_V + b * 16384 + tid * 32, u32x4{o[0], tag, o[1], tag});
      st_sc1(rw, WS_V + b * 16384 + tid * 32 + 16, u32x4{o[2], tag, o[3], tag});
    }
  }
  STAMP(9);

  const int rt = (b & 7) * 4 + ((b >> 3) & 3), ct = b >> 5;
  u32x4 wf[7][4];
  {
    const char* src = wd + (size_t)(w * 224 + 8 * t) * 1024 + ct * 128 + p16 * 8;
    u32x2 rwv[7][8];
#pragma unroll
    for (int ks = 0; ks < 7; ++ks)
#pragma unroll
      for (int j = 0; j < 8; ++j) rwv[ks][j] = *(const u32x2*)(src + (size_t)(ks * 32 + j) * 1024);
    SB();
#pragma unroll
    for (int ks = 0; ks < 7; ++ks)
#pragma unroll
      for (int kp = 0; kp < 4; ++kp) {
        wf[ks][0][kp] = __builtin_amdgcn_perm(rwv[ks][2 * kp + 1][0], rwv[ks][2 * kp][0], 0x05040100u);
        wf[ks][1][kp] = __builtin_amdgcn_perm(rwv[ks][2 * kp + 1][0], rwv[ks][2 * kp][0], 0x07060302u);
        wf[ks][2][kp] = __builtin_amdgcn_perm(rwv[ks][2 * kp + 1][1], rwv[ks][2 * kp][1], 0x05040100u);
        wf[ks][3][kp] = __builtin_amdgcn_perm(rwv[ks][2 * kp + 1][1], rwv[ks][2 * kp][1], 0x07060302u);
      }
    SB();
  }
  STAMP(10);

  {
    LAS char* yb = sm + FS;
    const int s = b + 256 * w;
    const u32x4 xv = *(const u32x4*)(x + (size_t)s * 1024 + (4 * p16 + t) * 16);
    {
      const int g = 4 * w + (p16 >> 2);
      const u32 off = WS_V + ((b & 7) * 32 + (b >> 3)) * 64 + (p16 & 3) * 16 + (g * 8 + 2 * t) * 16384;
      const int j = p16 >> 1, po = p16 & 1;
      float c0, s0, c1, s1;
      { float rev = (float)(2 * t * j) * 0.125f; c0 = cosr(rev); s0 = sinr(rev); }
      { float rev = (float)((2 * t + 1) * j) * 0.125f; c1 = cosr(rev); s1 = sinr(rev); }
      const u32x2 a8 = po ? u32x2{pk_bf16(s0, c0), pk_bf16(s1, c1)} : u32x2{pk_bf16(c0, -s0), pk_bf16(c1, -s1)};
      u32x4 r0, r1;
      for (int spin = 0; spin < SPIN_CAP; ++spin) {
      asm volatile("" ::: "memory");
        r0 = ld_sc1(rw, off); r1 = ld_sc1(rw, off + 16384);
        if (all_ok((r0[1] == tag) & (r0[3] == tag) & (r1[1] == tag) & (r1[3] == tag))) break;
      }
      STAMP(11);
      u32x2 o0, o1;
#pragma unroll
      for (int u = 0; u < 2; ++u) {
        f32x4 d = mfma16_bf16(a8, u32x2{r0[2 * u], r1[2 * u]}, z4);
        if (u == 0 && w == 0) {
          u32 e0, e1;
          const bool on = (po == 0) ? (t < 2) : (t >= 2);
          if ((t & 1) == 0) e0 = po ? pk_bf16(0.f, 0.5f * c0) : pk_bf16(0.5f * c0, 0.f);
          else e0 = on ? pk_bf16(c0, -s0) : 0u;
          e1 = on ? pk_bf16(c1, -s1) : 0u;
          f32x4 ds = mfma16_bf16(u32x2{e0, e1}, u32x2{r0[0], r1[0]}, z4);
          if (p16 == 0) d = ds;
        }
        o0[u] = pk_bf16(d[0], d[1]); o1[u] = pk_bf16(d[2], d[3]);
      }
      *(LAS u32x2*)(yb + (2 * t) * 1024 + (16 * w + p16) * 8) = o0;
      *(LAS u32x2*)(yb + (2 * t + 1) * 1024 + (16 * w + p16) * 8) = o1;
    }
    u32x4 bi1re[2], bi1im[2], bi2;
    float twc[2][4], tws[2][4];
#pragma unroll
    for (int hh = 0; hh < 2; ++hh) {
      const int n2 = 8 * (p16 >> 2) + 4 * hh + (p16 & 3);
#pragma unroll
      for (int m = 0; m < 4; ++m) {
        float rev = (float)(n2 * (t + 4 * m)) * (1.0f / 32); float cc = cosr(rev), sn = sinr(rev);
        bi1re[hh][m] = pk_bf16(cc, -sn); bi1im[hh][m] = pk_bf16(sn, cc);
        float rev2 = (float)(n2 * (4 * t + m)) * (1.0f / 512);
        twc[hh][m] = cosr(rev2); tws[hh][m] = sinr(rev2);
      }
    }
#pragma unroll
    for (int r = 0; r < 4; ++r) { float rev = (float)(p16 * (4 * t + r)) * (1.0f / 16); bi2[r] = pk_bf16(cosr(rev), -sinr(rev)); }
    BAR();
    u32x4 a;
#pragma unroll
    for (int m = 0; m < 4; ++m) a[m] = *(LAS u32*)(yb + w * 1024 + (lane + 64 * m) * 4);
    const float nyq = bf_hi(*(LAS u32*)(yb + w * 1024));
    u32 ov[4];
#pragma unroll
    for (int hh = 0; hh < 2; ++hh) {
      f32x4 gre = mfma32_bf16(a, bi1re[hh], z4), gim = mfma32_bf16(a, bi1im[hh], z4);
      u32x4 bb;
#pragma unroll
      for (int r = 0; r < 4; ++r) bb[r] = pk_bf16(gre[r] * twc[hh][r] - gim[r] * tws[hh][r], gre[r] * tws[hh][r] + gim[r] * twc[hh][r]);
      f32x4 d = mfma32_bf16(bb, bi2, f32x4{nyq, -nyq, nyq, -nyq});
      ov[2 * hh] = pk_bf16(bf_lo(xv[2 * hh]) + d[0], bf_hi(xv[2 * hh]) + d[1]);
      ov[2 * hh + 1] = pk_bf16(bf_lo(xv[2 * hh + 1]) + d[2], bf_hi(xv[2 * hh + 1]) + d[3]);
    }
    st_sc1(rw, WS_R + s * 2048 + (4 * p16 + t) * 32, u32x4{ov[0], tag, ov[1], tag});
    st_sc1(rw, WS_R + s * 2048 + (4 * p16 + t) * 32 + 16, u32x4{ov[2], tag, ov[3], tag});
  }
  STAMP(12);

  {
    for (int spin = 0; spin < SPIN_CAP; ++spin) {
      asm volatile("" ::: "memory");
      u32 fl = __builtin_amdgcn_raw_buffer_load_b32(rw, WS_F2 + (rt >> 1) * 128 + (lane < 14 ? lane : 0) * 4, 0, 16);
      if (all_ok(fl == tag)) break;
    }
    __builtin_amdgcn_fence(__ATOMIC_ACQUIRE, "wavefront");
    STAMP(13);
    const u32 hs = WS_H + ((rt * 4) * 56 + w * 7) * 1024 + lane * 16;
    u32x4 hf[4][7];
#pragma unroll
    for (int tt = 0; tt < 3; ++tt)
#pragma unroll
      for (int ks = 0; ks < 7; ++ks) hf[tt][ks] = ld_sc1(rw, hs + (tt * 56 + ks) * 1024);
    SB();
#pragma unroll
    for (int tt = 0; tt < 4; ++tt) {
      f32x4 acc[4] = {z4, z4, z4, z4};
#pragma unroll
      for (int ks = 0; ks < 7; ++ks) {
#pragma unroll
        for (int u = 0; u < 4; ++u) acc[u] = mfma32_bf16(wf[ks][u], hf[tt][ks], acc[u]);
        if (tt == 0) { SB(); hf[3][ks] = ld_sc1(rw, hs + (3 * 56 + ks) * 1024); SB(); }
      }
#pragma unroll
      for (int u = 0; u < 4; ++u) *(LAS f32x4*)(sm + w * 16384 + (tt * 4 + u) * 1024 + lane * 16) = acc[u];
      SB();
    }
    STAMP(14);
    const int tt = w >> 1, hh = w & 1;
    const int row = rt * 64 + tt * 16 + p16;
    u32x4 r0, r1;
    for (int spin = 0; spin < SPIN_CAP; ++spin) {
      asm volatile("" ::: "memory");
      const u32 off = WS_R + row * 2048 + ct * 256 + t * 64 + hh * 32;
      r0 = ld_sc1(rw, off); r1 = ld_sc1(rw, off + 16);
      if (all_ok((r0[1] == tag) & (r0[3] == tag) & (r1[1] == tag) & (r1[3] == tag))) break;
    }
    BAR();
    f32x2 sacc[4];
#pragma unroll
    for (int u = 0; u < 4; ++u) {
      sacc[u] = f32x2{0.f, 0.f};
#pragma unroll
      for (int pw = 0; pw < 8; ++pw) sacc[u] += *(LAS f32x2*)(sm + pw * 16384 + (tt * 4 + u) * 1024 + lane * 16 + hh * 8);
    }
    u32x4 o;
    o[0] = pk_bf16(bf_lo(r0[0]) + sacc[0][0], bf_hi(r0[0]) + sacc[1][0]);
    o[1] = pk_bf16(bf_lo(r0[2]) + sacc[2][0], bf_hi(r0[2]) + sacc[3][0]);
    o[2] = pk_bf16(bf_lo(r1[0]) + sacc[0][1], bf_hi(r1[0]) + sacc[1][1]);
    o[3] = pk_bf16(bf_lo(r1[2]) + sacc[2][1], bf_hi(r1[2]) + sacc[3][1]);
    *(u32x4*)(out + (size_t)row * 1024 + ct * 128 + t * 32 + hh * 16) = o;
    if (b == 0 && tid == 0) __builtin_amdgcn_raw_buffer_store_b32(tag, rw, WS_EPOCH, 0, 16);
  }
  STAMP(15);
  if (blockIdx.x == 0 && threadIdx.x == 0) *(unsigned long long*)(ws + 2048) = tA;
}

__global__ void k_a(char* ws) { unsigned long long t = __builtin_amdgcn_s_memrealtime(); if (threadIdx.x == 0) *(unsigned long long*)(ws + 2048) = t; }
__global__ void k_z(char* ws) { unsigned long long t = *(volatile unsigned long long*)(ws + 2048); while (__builtin_amdgcn_s_memrealtime() < t + 6000) __builtin_amdgcn_s_sleep(1); }
extern "C" void kernel_launch(void* const* d_in, const int* in_sizes, int n_in, void* d_out, int out_size, void* d_ws, size_t ws_size,
                              hipStream_t stream) {
  (void)hipFuncSetAttribute((const void*)fused_kernel, hipFuncAttributeMaxDynamicSharedMemorySize, LDS_BYTES);
  fused_kernel<<<256, 512, LDS_BYTES, stream>>>((const char*)d_in[0], (const unsigned short*)d_in[1], (const unsigned short*)d_in[2], (const char*)d_in[4],
                                                (const char*)d_in[5], (const char*)d_in[6], (char*)d_ws, (char*)d_out);
  k_z<<<1, 64, 0, stream>>>((char*)d_ws);
}
```

```cpp
#include <hip/hip_runtime.h>
#include <stdint.h>

#define LAS __attribute__((address_space(3)))
#define GAS __attribute__((address_space(1)))
#define DI __device__ __forceinline__
typedef unsigned u32;
typedef unsigned u32x2 __attribute__((ext_vector_type(2)));
typedef unsigned u32x4 __attribute__((ext_vector_type(4)));
typedef float f32x2 __attribute__((ext_vector_type(2)));
typedef float f32x4 __attribute__((ext_vector_type(4)));
typedef float f32x16 __attribute__((ext_vector_type(16)));
typedef short s16x4 __attribute__((ext_vector_type(4)));
typedef _Float16 h16x2 __attribute__((ext_vector_type(2)));
typedef _Float16 h16x4 __attribute__((ext_vector_type(4)));
typedef _Float16 h16x8 __attribute__((ext_vector_type(8)));
typedef __bf16 b16x2 __attribute__((ext_vector_type(2)));
typedef __bf16 b16x8 __attribute__((ext_vector_type(8)));
typedef __amdgpu_buffer_rsrc_t rsrc_t;

constexpr int F_ = 1792, DF_ = 257;
constexpr u32 WS_EPOCH = 0;
constexpr u32 WS_F2 = 4096;
constexpr u32 WS_ST = 16384;
constexpr u32 WS_CS = WS_ST + 16384;
constexpr u32 WS_U = 262144;
constexpr u32 WS_V = WS_U + 4194304;
constexpr u32 WS_R = WS_V + 4194304;
constexpr u32 WS_SF = 196608;
constexpr u32 WS_H = WS_R + 4194304;
constexpr u32 WS_END = WS_H + 7340032;
constexpr int LDS_BYTES = 163840;
constexpr int G1_STAGE = 49152;
constexpr int FS = 3 * G1_STAGE;
#define SPIN_CAP (1 << 17)
constexpr int CODE_LINES = 286;
#define HPRIO 2
#define S1PRE 2
#define ROTM 3
constexpr float XS = 1.0f / 16;
constexpr float FSC = (2.0f / (2048.0f * 512.0f)) * 16.0f;
constexpr size_t WS_DBG = 64 << 20;
#define STAMP(k) do {} while (0)
#define STAMP_DECL
#define STAMP_DUMP() do {} while (0)

#if defined(__HIP_DEVICE_COMPILE__)
#pragma clang attribute push(__attribute__((target("no-packed-fp32-ops"))), apply_to = function)
#endif
DI u32 pk_f16(float a, float b) { h16x2 p = {(_Float16)a, (_Float16)b}; return __builtin_bit_cast(u32, p); }
DI u32 pk_bf16(float a, float b) { b16x2 p = {(__bf16)a, (__bf16)b}; return __builtin_bit_cast(u32, p); }
DI float bf_lo(u32 v) { return __builtin_bit_cast(float, v << 16); }
DI float bf_hi(u32 v) { return __builtin_bit_cast(float, v & 0xffff0000u); }
DI float rnd_bf(float a) { return bf_lo(pk_bf16(a, a)); }
DI float cosr(float rev) { return __builtin_amdgcn_cosf(rev); }
DI float sinr(float rev) { return __builtin_amdgcn_sinf(rev); }
DI f32x4 mfma16_f16(u32x2 a, u32x2 b, f32x4 c) { return __builtin_amdgcn_mfma_f32_16x16x16f16(__builtin_bit_cast(h16x4, a), __builtin_bit_cast(h16x4, b), c, 0, 0, 0); }
DI f32x4 mfma16_bf16(u32x2 a, u32x2 b, f32x4 c) { return __builtin_amdgcn_mfma_f32_16x16x16bf16_1k(__builtin_bit_cast(s16x4, a), __builtin_bit_cast(s16x4, b), c, 0, 0, 0); }
DI f32x4 mfma32_f16(u32x4 a, u32x4 b, f32x4 c) { return __builtin_amdgcn_mfma_f32_16x16x32_f16(__builtin_bit_cast(h16x8, a), __builtin_bit_cast(h16x8, b), c, 0, 0, 0); }
DI f32x4 mfma32_bf16(u32x4 a, u32x4 b, f32x4 c) { return __builtin_amdgcn_mfma_f32_16x16x32_bf16(__builtin_bit_cast(b16x8, a), __builtin_bit_cast(b16x8, b), c, 0, 0, 0); }
DI f32x16 mfma3232(u32x4 a, u32x4 b, f32x16 c) { return __builtin_amdgcn_mfma_f32_32x32x16_bf16(__builtin_bit_cast(b16x8, a), __builtin_bit_cast(b16x8, b), c, 0, 0, 0); }
DI u32x2 tr_read(LAS char* p) { return __builtin_bit_cast(u32x2, __builtin_amdgcn_ds_read_tr16_b64_v4i16((LAS s16x4*)p)); }
#define SB() __builtin_amdgcn_sched_barrier(0)
#define BAR() asm volatile("s_waitcnt lgkmcnt(0)\n\ts_barrier" ::: "memory")
#define WAITV(n) asm volatile("s_waitcnt vmcnt(%0)" :: "i"(n) : "memory")
#define PIN(x) asm volatile("" : "+v"(x))
DI u32x4 mk_srd(const void* p, u32 bytes) { uint64_t a = (uint64_t)p; return u32x4{(u32)a, (u32)(a >> 32), bytes, 0x00020000u}; }
DI void glds16(u32 ldsbase, int imm, u32 voff, u32x4 srd, u32 soff) {
  asm volatile("s_add_u32 m0, %0, %1\n\ts_nop 0\n\tbuffer_load_dwordx4 %2, %3, %4 offen lds" :: "s"(ldsbase), "i"(imm), "v"(voff), "s"(srd), "s"(soff) : "memory", "scc");
}
DI void glds4(u32 ldsaddr, u32 voff, u32x4 srd, u32 soff) {
  asm volatile("s_mov_b32 m0, %0\n\ts_nop 0\n\tbuffer_load_dword %1, %2, %3 offen lds" :: "s"(ldsaddr), "v"(voff), "s"(srd), "s"(soff) : "memory");
}
#define ALD4(dst, voff, srd, IMM, FL) asm volatile("buffer_load_dwordx4 %0, %1, %2, 0 offen offset:" #IMM FL : "=v"(dst) : "v"(voff), "s"(srd) : "memory")
#define ALD2(dst, voff, srd, IMM, FL) asm volatile("buffer_load_dwordx2 %0, %1, %2, 0 offen offset:" #IMM FL : "=v"(dst) : "v"(voff), "s"(srd) : "memory")
#define ALD1(dst, voff, srd, IMM, FL) asm volatile("buffer_load_dword %0, %1, %2, 0 offen offset:" #IMM FL : "=v"(dst) : "v"(voff), "s"(srd) : "memory")
DI u32x4 ld_sc1(rsrc_t r, u32 off) { return __builtin_amdgcn_raw_buffer_load_b128(r, off, 0, 16); }
DI u32x2 ld_sc1_2(rsrc_t r, u32 off) { return __builtin_amdgcn_raw_buffer_load_b64(r, off, 0, 16); }
DI void st_sc1(rsrc_t r, u32 off, u32x4 v) { __builtin_amdgcn_raw_buffer_store_b128(v, r, off, 0, 16); }
DI void st_sc1_2(rsrc_t r, u32 off, u32x2 v) { __builtin_amdgcn_raw_buffer_store_b64(v, r, off, 0, 16); }
DI bool all_ok(bool ok) { return __builtin_amdgcn_ballot_w64(!ok) == 0; }
DI bool tags_ok(u32x4 a, u32 tag) { return (a[1] == tag) & (a[3] == tag); }

template <int CTRL> DI float dpp_add(float v) {
  int o = __builtin_amdgcn_update_dpp(0, __builtin_bit_cast(int, v), CTRL, 0xf, 0xf, true);
  return v + __builtin_bit_cast(float, o);
}
DI float row_sum(float v) { v = dpp_add<0xB1>(v); v = dpp_add<0x4E>(v); v = dpp_add<0x141>(v); return dpp_add<0x140>(v); }
DI float xrow_sum(float v) {
  u32 u = __builtin_bit_cast(u32, v);
  auto a = __builtin_amdgcn_permlane16_swap(u, u, false, false);
  v = __builtin_bit_cast(float, (u32)a[0]) + __builtin_bit_cast(float, (u32)a[1]);
  u = __builtin_bit_cast(u32, v);
  auto b = __builtin_amdgcn_permlane32_swap(u, u, false, false);
  return __builtin_bit_cast(float, (u32)b[0]) + __builtin_bit_cast(float, (u32)b[1]);
}
template <bool NEG, bool BF> DI u32x4 cmul_pack(f32x4 re, f32x4 im, f32x4 c, f32x4 s) {
  f32x4 a = NEG ? re * c + im * s : re * c - im * s;
  f32x4 b = NEG ? im * c - re * s : im * c + re * s;
  u32x4 o;
#pragma unroll
  for (int r = 0; r < 4; ++r) o[r] = BF ? pk_bf16(a[r], b[r]) : pk_f16(a[r], b[r]);
  return o;
}
DI u32 rot16(u32 v) { return __builtin_amdgcn_alignbit(v, v, 16); }
template <bool NEG> DI void cmul_half(f32x4& a, f32x4& b, f32x4 re, f32x4 im, f32x4 c, f32x4 s) {
  a = NEG ? re * c + im * s : re * c - im * s;
  b = NEG ? im * c - re * s : im * c + re * s;
}
template <bool BF> DI u32x4 pack4(f32x4 a, f32x4 b) {
  u32x4 o;
#pragma unroll
  for (int r = 0; r < 4; ++r) o[r] = BF ? pk_bf16(a[r], b[r]) : pk_f16(a[r], b[r]);
  return o;
}
DI void glds_a(u32 ldsbase, int imm, u32 v0, u32 v1, u32x4 srd, u32 soff) {
  asm volatile("s_add_u32 m0, %0, %1\n\ts_nop 0\n\tbuffer_load_dwordx4 %2, %4, %5 offen lds\n\tbuffer_load_dwordx4 %3, %4, %5 offen offset:1024 lds"
               :: "s"(ldsbase), "i"(imm), "v"(v0), "v"(v1), "s"(srd), "s"(soff) : "memory", "scc");
}
DI void glds_b(u32 v2, u32 v3, u32x4 srd, u32 soff) {
  asm volatile("buffer_load_dwordx4 %0, %2, %3 offen offset:2048 lds\n\tbuffer_load_dwordx4 %1, %2, %3 offen offset:3072 lds"
               :: "v"(v2), "v"(v3), "s"(srd), "s"(soff) : "memory");
}
DI u32x4 cmulc_f16(f32x4 re, f32x4 im, u32x4 tw) {
  u32 z0 = pk_f16(re[0], im[0]), z1 = pk_f16(re[1], im[1]), z2 = pk_f16(re[2], im[2]), z3 = pk_f16(re[3], im[3]);
  u32 o0, o1, o2, o3;
  const u32 t0 = tw[0], t1 = tw[1], t2 = tw[2], t3 = tw[3];
#define PKM " op_sel_hi:[1,0]\n\t"
#define PKF " op_sel:[1,1,0] op_sel_hi:[0,1,1] neg_hi:[0,1,0]\n\t"
  asm("v_pk_mul_f16 %0, %4, %8" PKM "v_pk_mul_f16 %1, %5, %9" PKM "v_pk_mul_f16 %2, %6, %10" PKM "v_pk_mul_f16 %3, %7, %11" PKM
      "v_pk_fma_f16 %0, %4, %8, %0" PKF "v_pk_fma_f16 %1, %5, %9, %1" PKF "v_pk_fma_f16 %2, %6, %10, %2" PKF "v_pk_fma_f16 %3, %7, %11, %3" PKF "s_nop 1"
      : "=&v"(o0), "=&v"(o1), "=&v"(o2), "=&v"(o3) : "v"(z0), "v"(z1), "v"(z2), "v"(z3), "v"(t0), "v"(t1), "v"(t2), "v"(t3));
  return u32x4{o0, o1, o2, o3};
}
template <int N> struct IC { static constexpr int v = N; };
template <int A, int B, class Fn> DI void static_for(Fn&& f) { if constexpr (A < B) { f(IC<A>{}); static_for<A + 1, B>(f); } }

struct Frag { u32x4 bx[4], aw[2]; };
#define ALD4S(dst, voff, srd, SOFF, FL) asm volatile("buffer_load_dwordx4 %0, %1, %2, %3 offen" FL : "=v"(dst) : "v"(voff), "s"(srd), "s"(SOFF) : "memory")
#define ALD2S(dst, voff, srd, SOFF, FL) asm volatile("buffer_load_dwordx2 %0, %1, %2, %3 offen" FL : "=v"(dst) : "v"(voff), "s"(srd), "s"(SOFF) : "memory")
#define RAWBAR() do { asm volatile("s_barrier" ::: "memory"); } while (0)

__global__ __launch_bounds__(512, 2) void fused_kernel(const char* __restrict__ x, const unsigned short* __restrict__ wr, const unsigned short* __restrict__ wi,
                                                       const char* __restrict__ wg, const char* __restrict__ wu, const char* __restrict__ wd,
                                                       char* __restrict__ ws, char* __restrict__ out) {
  extern __shared__ __attribute__((aligned(16))) char smem[];
  LAS char* sm = (LAS char*)smem;
  STAMP_DECL
  STAMP(0);
  const int b = blockIdx.x, tid = threadIdx.x, lane = tid & 63, w = __builtin_amdgcn_readfirstlane(tid >> 6);
  const int hw = w & 3, p16 = lane & 15, t = lane >> 4;
  const f32x4 z4 = {0.f, 0.f, 0.f, 0.f};
  const u32 lds0 = (u32)(uintptr_t)sm;
  if (b < 8) {
    uint64_t pc; asm volatile("s_getpc_b64 %0" : "=s"(pc));
    glds4(lds0, tid * 128, u32x4{(u32)pc, (u32)(pc >> 32), CODE_LINES * 128, 0x00020000u}, 0);
  }
  const u32x4 sx = mk_srd(x, 2097152);
  if (w >= 4) {
    glds16(lds0 + hw * 1024, FS + 0, (b + 256 * hw) * 1024 + lane * 16, sx, 0);
    glds16(lds0 + hw * 1024, FS + 4096, (b + 256 * hw) * 1024 + lane * 16, sx, 1048576);
  }
  SB();
  const bool is_gemm = b >= 32;
  const int xc = b & 7, r_ = (b - 32) >> 3;
  const int mi = (xc >> 1) * 4 + (r_ & 3), fj = (xc & 1) * 7 + (r_ >> 2);
  const int rot = ((r_ & 3) + (r_ >> 2)) & ROTM;
  const u32x4 sg = mk_srd(wg, 1835008), su = mk_srd(wu, 1835008);
  const u32 lw = lds0 + hw * 4096;
  u32 vx[4], vg[4], vu[4];
#pragma unroll
  for (int j = 0; j < 4; ++j) {
    vx[j] = (mi * 128 + hw * 32 + j * 7 + (lane >> 3)) * 1024 + (((lane & 7) ^ ((4 * j + t) & 7)) << 4);
    vg[j] = (hw * 16 + j * 4 + t) * 3584 - j * 1024 + fj * 256 + ((p16 ^ (2 * t)) << 4);
    vu[j] = vg[j] ^ 128;
  }
  auto g1_issue = [&](int st, int k) {
    const int lb = (st % 3) * G1_STAGE + (k >> 1) * 16384, sk = st ^ rot;
    if (k == 0) glds_a(lw, lb, vx[0], vx[1], sx, sk * 128);
    if (k == 1) glds_b(vx[2], vx[3], sx, sk * 128);
    if (k == 2) glds_a(lw, lb, vg[0], vg[1], sg, sk * 64 * 3584);
    if (k == 3) glds_b(vg[2], vg[3], sg, sk * 64 * 3584);
    if (k == 4) glds_a(lw, lb, vu[0], vu[1], su, sk * 64 * 3584);
    if (k == 5) glds_b(vu[2], vu[3], su, sk * 64 * 3584);
  };
  auto load_tag = [&]() {
    u32 epoch;
    asm volatile("s_load_dword %0, %1, 0x0\n\ts_waitcnt lgkmcnt(0)" : "=s"(epoch) : "s"(ws) : "memory");
    return epoch * 0x9E3779B1u + 0x7F4A7C15u;
  };
  const rsrc_t rw = __builtin_amdgcn_make_buffer_rsrc(ws, 0, WS_END, 0x00020000);
  const int jp = b & 7, g = b >> 3;
  const int rt = (b & 7) * 4 + ((b >> 3) & 3), ct = b >> 5;
  auto wslot = [&](int s) { return (s < 3 ? 2 * G1_STAGE : s < 6 ? -3 * 16384 : G1_STAGE - 6 * 16384) + s * 16384; };
  u32x4 wf[7][4];
  const int wbase = hw * 4096 + t * 1024 + (p16 >> 2) * 128 + (p16 & 3) * 8 + ((((p16 >> 3) | ((t & 1) << 1))) << 5);
  auto wd_read = [&](int ks, int s) {
#pragma unroll
    for (int u = 0; u < 4; ++u) {
      LAS char* p = sm + wslot(s) + (wbase ^ (u << 5));
      u32x2 lo = tr_read(p), hi = tr_read(p + 512);
      wf[ks][u] = u32x4{lo[0], lo[1], hi[0], hi[1]};
    }
  };
  u32 tag;
  const int kr = (w + 2 * (rt & 3)) & 7;
  const u32 hs = WS_H + ((rt * 4) * 56 + kr * 7) * 1024 + lane * 16;
  u32x4 hf[4][7];
  const float pf = (float)p16, tf = (float)t;

  auto helper = [&](auto G_) {
    constexpr bool G = decltype(G_)::v;
    const int ht = tid & 255;
    __builtin_amdgcn_s_setprio(HPRIO);
    const u32x4 sw = mk_srd(ws, WS_END), sd_ = mk_srd(wd, 1835008);
    const u32x4 sfr_ = mk_srd(wr, 2048 * DF_ * 2), sfi_ = mk_srd(wi, 2048 * DF_ * 2);
    SB();
#define DMA(st, k) do { if (G) { SB(); g1_issue(st, k); SB(); } } while (0)
#define GWAIT(n) do { if (G) WAITV(n); } while (0)
#define WAITG(n) do { if (G) WAITV(n); else WAITV(0); } while (0)
#define GBAR() do { if (G) RAWBAR(); } while (0)
    u32x4 b2re[2], b2im[2], tw[2], ac2re, ac2im, bc3re, bc3im, qa8, qa8i, bc1re, bc1im, ac4re, ac4im, t1, bi1re[2], bi1im[2];
    u32x2 b1re, b1im;
    f32x4 t2c, t2s, t4c[2], t4s[2];
    {
      const float d5 = pf * (1.0f / 512), a5 = 4.0f * tf * d5;
#pragma unroll
      for (int r = 0; r < 4; ++r) {
        const float th = a5 + r * d5, c = cosr(th * 32.0f), s = sinr(th * 32.0f);
        ac2re[r] = pk_f16(c, s); ac2im[r] = rot16(ac2re[r]) ^ 0x8000u;
        bc3im[r] = pk_bf16(s, c); bc3re[r] = rot16(bc3im[r]) ^ 0x80000000u;
#pragma unroll
        for (int hh = 0; hh < 2; ++hh) {
          const float tq = th + hh * (16.0f * d5);
          tw[hh][r] = pk_f16(cosr(tq), sinr(tq));
          b2re[hh][r] = pk_f16(cosr(tq * 16.0f), sinr(tq * 16.0f)); b2im[hh][r] = rot16(b2re[hh][r]) ^ 0x8000u;
        }
      }
      b1re = u32x2{__builtin_amdgcn_perm(ac2re[1], ac2re[0], 0x05040100u), __builtin_amdgcn_perm(ac2re[3], ac2re[2], 0x05040100u)};
      b1im = u32x2{__builtin_amdgcn_perm(ac2im[1], ac2im[0], 0x05040100u), __builtin_amdgcn_perm(ac2im[3], ac2im[2], 0x05040100u)};
      const int po = p16 & 1, j8 = p16 >> 1;
      const float j8f = (float)j8 * 0.125f;
      const float ra = 2.0f * tf * j8f, rb = ra + j8f;
      const float ca = cosr(ra), sa = sinr(ra), cb = cosr(rb), sb = sinr(rb);
      const u32 fa0 = pk_f16(ca, sa), fb0 = pk_f16(cb, sb);
      const u32 fa = po ? (rot16(fa0) ^ 0x8000u) : fa0, fb = po ? (rot16(fb0) ^ 0x8000u) : fb0;
      const u32 sel = (j8 & 3) == 0 ? 0x03020100u : (j8 < 4 ? 0x0c0c0100u : 0x01000c0cu);
      qa8 = u32x4{fa, fb, __builtin_amdgcn_perm(0u, fa, sel), __builtin_amdgcn_perm(0u, fb, sel)};
      const u32 ia = pk_bf16(sa, ca), ib = pk_bf16(sb, cb), pa = rot16(ia) ^ 0x80000000u, pb = rot16(ib) ^ 0x80000000u;
      const bool on = (po == 0) ? (t < 2) : (t >= 2);
      const u32 hc = pk_bf16(0.5f * ca, 0.f);
      qa8i = u32x4{po ? ia : pa, po ? ib : pb, (t & 1) == 0 ? (po ? hc << 16 : hc) : (on ? pa : 0u), on ? pb : 0u};
      PIN(ac2re); PIN(ac2im); PIN(bc3re); PIN(bc3im); PIN(tw[0]); PIN(tw[1]); PIN(b2re[0]); PIN(b2re[1]); PIN(b2im[0]); PIN(b2im[1]); PIN(b1re); PIN(b1im); PIN(qa8); PIN(qa8i);
    }
    SB();
    tag = load_tag();
    STAMP(21);
    LAS char* const img = sm + FS + hw * 1024;
    u32 stat[2];
    WAITV(0);
    STAMP(1);
    {
      float v[2][8], mean[2], var[2];
#pragma unroll
      for (int a = 0; a < 2; ++a) {
        const u32x4 xv = *(LAS u32x4*)(img + a * 4096 + lane * 16);
#pragma unroll
        for (int k = 0; k < 4; ++k) { v[a][2 * k] = bf_lo(xv[k]); v[a][2 * k + 1] = bf_hi(xv[k]); }
      }
#pragma unroll
      for (int a = 0; a < 2; ++a) mean[a] = xrow_sum(row_sum(((v[a][0] + v[a][1]) + (v[a][2] + v[a][3])) + ((v[a][4] + v[a][5]) + (v[a][6] + v[a][7])))) * (1.0f / 512);
#pragma unroll
      for (int a = 0; a < 2; ++a) {
        float q0 = 0.f, q1 = 0.f;
#pragma unroll
        for (int k = 0; k < 4; ++k) { float d = v[a][k] - mean[a], e = v[a][k + 4] - mean[a]; q0 += d * d; q1 += e * e; }
        var[a] = xrow_sum(row_sum(q0 + q1)) * (1.0f / 512);
      }
#pragma unroll
      for (int a = 0; a < 2; ++a) {
        stat[a] = pk_bf16(mean[a], __builtin_amdgcn_sqrtf(rnd_bf(rnd_bf(var[a]) + 1e-5f)));
        const float sc = __builtin_amdgcn_rcpf(bf_hi(stat[a])) * XS, of = -bf_lo(stat[a]) * sc;
        u32x4 xh;
#pragma unroll
        for (int k = 0; k < 4; ++k) xh[k] = pk_f16(__builtin_fmaf(v[a][2 * k], sc, of), __builtin_fmaf(v[a][2 * k + 1], sc, of));
        *(LAS u32x4*)(img + a * 4096 + ((lane ^ ((t & 1) << 1)) << 4)) = xh;
      }
      asm volatile("" ::: "memory");
      STAMP(23);
      u32x2 tra[2][2];
#pragma unroll
      for (int a = 0; a < 2; ++a)
#pragma unroll
        for (int hh = 0; hh < 2; ++hh) tra[a][hh] = tr_read(img + a * 4096 + (4 * t + (p16 >> 2)) * 64 + ((hh ^ (t & 1)) << 5) + (p16 & 3) * 8);
      f32x4 d1re[2][2], d1im[2][2], d2re[2], d2im[2];
#pragma unroll
      for (int a = 0; a < 2; ++a)
#pragma unroll
        for (int hh = 0; hh < 2; ++hh) { d1re[a][hh] = mfma16_f16(tra[a][hh], b1re, z4); d1im[a][hh] = mfma16_f16(tra[a][hh], b1im, z4); }
      DMA(2, 0); DMA(2, 1);
#pragma unroll
      for (int a = 0; a < 2; ++a) {
#pragma unroll
        for (int hh = 0; hh < 2; ++hh) {
          u32x4 ta = cmulc_f16(d1re[a][hh], d1im[a][hh], tw[hh]);
          d2re[a] = mfma32_f16(ta, b2re[hh], hh ? d2re[a] : z4); d2im[a] = mfma32_f16(ta, b2im[hh], hh ? d2im[a] : z4);
        }
        DMA(2, 2 + a);
      }
      float ny[2];
#pragma unroll
      for (int a = 0; a < 2; ++a) { f32x4 q = d1re[a][0] + d1re[a][1]; ny[a] = xrow_sum((q[0] - q[1]) + (q[2] - q[3])); }
      DMA(2, 4);
      int e = 4 * p16 + t; e ^= (e >> 3) & 3;
#pragma unroll
      for (int a = 0; a < 2; ++a) {
        if (lane == 0) d2im[a][0] = ny[a];
        *(LAS u32x4*)(img + a * 4096 + e * 16) = pack4<false>(d2re[a], d2im[a]);
      }
      DMA(2, 5);
    }
    STAMP(24);
    BAR();
    STAMP(2);
    {
      u32x2 e0[2], e1[2];
#pragma unroll
      for (int k = 0; k < 2; ++k) {
        int e = 16 * hw + 8 * k + (p16 >> 1); e ^= (e >> 3) & 3;
        e0[k] = *(LAS u32x2*)(sm + FS + (2 * t) * 1024 + e * 16 + (p16 & 1) * 8);
        e1[k] = *(LAS u32x2*)(sm + FS + (2 * t + 1) * 1024 + e * 16 + (p16 & 1) * 8);
      }
      DMA(3, 0); DMA(3, 1);
      f32x4 d8[4];
#pragma unroll
      for (int u = 0; u < 4; ++u) d8[u] = mfma16_f16(u32x2{qa8[0], qa8[1]}, u32x2{e0[u >> 1][u & 1], e1[u >> 1][u & 1]}, z4);
      f32x4 d8s = mfma16_f16(u32x2{qa8[2], qa8[3]}, u32x2{e0[0][0], e1[0][0]}, z4);
      DMA(3, 2); DMA(3, 3); DMA(3, 4); DMA(3, 5);
      if ((hw | p16) == 0) d8[0] = d8s;
      const u32 ub = WS_U + b * 16384 + (2 * t) * 2048 + hw * 512 + p16 * 16;
#pragma unroll
      for (int hf = 0; hf < 2; ++hf)
#pragma unroll
        for (int k = 0; k < 2; ++k)
          st_sc1(rw, ub + hf * 2048 + k * 256, u32x4{pk_f16(d8[2 * k][2 * hf], d8[2 * k][2 * hf + 1]), tag, pk_f16(d8[2 * k + 1][2 * hf], d8[2 * k + 1][2 * hf + 1]), tag});
      if (lane < 2) st_sc1_2(rw, WS_ST + b * 64 + hw * 8 + lane * 32, u32x2{lane ? stat[1] : stat[0], tag});
    }
    STAMP(19);
    {
      const u32 vo = (((8 * (8 * g + 2 * hw + (lane >> 5)) + jp) * DF_ * 2 + 14) & ~15u) + (lane & 31) * 16;
      glds16(lds0 + hw * 2048, FS + 8192, vo, sfr_, 0); glds16(lds0 + hw * 2048, FS + 8192 + 1024, vo, sfi_, 0);
    }
    GWAIT(12 + 5 + 2);
    GBAR();
    STAMP(3);
    {
      const float jf = (float)jp;
      const float d1 = (8.0f * pf + jf) * (1.0f / 2048), a1 = 4.0f * tf * d1, d2 = pf * (1.0f / 256), a2 = (32.0f * tf + jf) * (pf * (1.0f / 2048));
      DMA(4, 0); DMA(4, 1);
#pragma unroll
      for (int r = 0; r < 4; ++r) {
        DMA(4, 2 + r);
        const float n1 = a1 + r * d1, n2 = a2 + r * d2;
        bc1re[r] = pk_f16(cosr(n1 * 16.0f), sinr(n1 * 16.0f)); bc1im[r] = rot16(bc1re[r]) ^ 0x8000u;
        t1[r] = pk_f16(cosr(n1), sinr(n1));
        ac4im[r] = pk_bf16(sinr(n2 * 16.0f), cosr(n2 * 16.0f)); ac4re[r] = rot16(ac4im[r]) ^ 0x80000000u;
        t2c[r] = cosr(n2); t2s[r] = sinr(n2);
      }
      PIN(bc1re); PIN(bc1im); PIN(ac4re); PIN(ac4im); PIN(t1); PIN(t2c); PIN(t2s);
    }
    STAMP(25);
    GWAIT(5 + 2 + 12);
    GBAR();
    STAMP(4);
    u32x4 fdr, fdi, ug[4];
    const u32 ffo = ((8 * ht + jp) * DF_ + 8 * g) * 2;
#pragma unroll
    for (int hh = 0; hh < 2; ++hh) {
      const float n2 = (float)(8 * (p16 >> 2) + (p16 & 3)) + 4.0f * hh;
#pragma unroll
      for (int m = 0; m < 4; ++m) {
        if (m != 3) DMA(5, 3 * hh + m);
        float rev = n2 * ((tf + 4.0f * m) * (1.0f / 32));
        bi1im[hh][m] = pk_bf16(sinr(rev), cosr(rev)); bi1re[hh][m] = rot16(bi1im[hh][m]) ^ 0x80000000u;
        float rev2 = n2 * ((4.0f * tf + m) * (1.0f / 512));
        t4c[hh][m] = cosr(rev2); t4s[hh][m] = sinr(rev2);
      }
      PIN(bi1re[hh]); PIN(bi1im[hh]); PIN(t4c[hh]); PIN(t4s[hh]);
    }
    const int uq = ht & 3;
    const int ui = (((ht >> 2) & 3) << 4) + (ht >> 4);
    const u32 uoff = WS_U + ui * 16384 + jp * 2048 + g * 64 + uq * 16;
    auto caddr = [&](int i) { return (LAS char*)(sm + FS + uq * 2048 + ((((i & 192) + 4 * (i & 15) + ((i >> 4) & 3)) * 4) ^ (uq << 5))); };
    if (!G) {
#pragma nounroll
      for (int spin = 0; spin < SPIN_CAP; ++spin) {
        asm volatile("" ::: "memory");
        u32x2 q = ld_sc1_2(rw, WS_ST + (b + 224) * 64 + hw * 8);
        if (all_ok(q[1] == tag)) break;
        __builtin_amdgcn_s_sleep(8);
      }
      __builtin_amdgcn_s_sleep(24);
    }
    SB();
    ALD4(fdr, ffo, sfr_, 0, "");
#pragma unroll
    for (int k = 0; k < 4; ++k) ALD4S(ug[k], uoff, sw, k * 64 * 16384, " sc1");
    STAMP(26);
    WAITG(12 + 5);
    RAWBAR();
    STAMP(5);
    ALD4(fdi, ffo, sfi_, 0, "");
#pragma unroll
    for (int k = 0; k < 6; ++k) DMA(6, k);
    STAMP(27);
    WAITG(13);
    PIN(ug[0]); PIN(ug[1]); PIN(ug[2]); PIN(ug[3]);
#define CHK1 all_ok(tags_ok(ug[0], tag) & tags_ok(ug[1], tag) & tags_ok(ug[2], tag) & tags_ok(ug[3], tag))
    const bool uok = CHK1;
    {
      LAS char* cp = caddr(ui);
#pragma unroll
      for (int k = 0; k < 4; ++k) { *(LAS u32*)(cp + k * 256) = ug[k][0]; *(LAS u32*)(cp + k * 256 + 1024) = ug[k][2]; }
    }
    GWAIT(12);
    PIN(fdr); PIN(fdi);
    {
      LAS char* fp = sm + FS + 8192 + ((ht & 192) + 4 * (ht & 15) + ((ht >> 4) & 3)) * 4;
#pragma unroll
      for (int k = 0; k < 4; ++k) {
        *(LAS u32*)(fp + (2 * k) * 1024) = __builtin_amdgcn_perm(fdi[k], fdr[k], 0x05040100u);
        *(LAS u32*)(fp + (2 * k + 1) * 1024) = __builtin_amdgcn_perm(fdi[k], fdr[k], 0x07060302u);
      }
    }
    if (__builtin_expect(!uok, 0)) {
#pragma nounroll
      for (int spin = 0; spin < SPIN_CAP; ++spin) {
        asm volatile("" ::: "memory");
#pragma unroll
        for (int k = 0; k < 4; ++k) ug[k] = ld_sc1(rw, uoff + k * 64 * 16384);
        if (CHK1) break;
      }
      LAS char* cp = caddr(ui);
#pragma unroll
      for (int k = 0; k < 4; ++k) { *(LAS u32*)(cp + k * 256) = ug[k][0]; *(LAS u32*)(cp + k * 256 + 1024) = ug[k][2]; }
    }
    u32x4 fsp[2];
    const bool special = !G && b < 8 && hw == 0;
    if (special) {
#pragma nounroll
      for (int spin = 0; spin < SPIN_CAP; ++spin) {
        asm volatile("" ::: "memory");
        bool ok = true;
#pragma unroll
        for (int c = 0; c < 2; ++c) {
          const u32x4 fa = ld_sc1(rw, WS_SF + jp * 4096 + c * 2048 + lane * 32), fb = ld_sc1(rw, WS_SF + jp * 4096 + c * 2048 + lane * 32 + 16);
          ok &= tags_ok(fa, tag) & tags_ok(fb, tag);
          fsp[c] = u32x4{fa[0], fa[2], fb[0], fb[2]};
        }
        if (all_ok(ok)) break;
      }
    }
    STAMP(28);
    BAR();
    STAMP(6);
    LAS char* const colp = sm + FS + hw * 2048 + ((lane * 16) ^ (hw << 5));
    {
      u32x4 uin[2], fin[2];
#pragma unroll
      for (int a = 0; a < 2; ++a) { uin[a] = *(LAS u32x4*)(colp + a * 1024); fin[a] = *(LAS u32x4*)(sm + FS + 8192 + (2 * hw + a) * 1024 + lane * 16); }
      const bool two = special && (jp & 3) == 0;
      f32x4 dre[2], dim[2], keep;
      auto col_xform = [&](auto NA_) {
        constexpr int NA = decltype(NA_)::v;
        u32x4 ta[2];
        DMA(7, 0);
#pragma unroll
        for (int a = 0; a < NA; ++a) { dre[a] = mfma32_f16(uin[a], bc1re, z4); dim[a] = mfma32_f16(uin[a], bc1im, z4); }
        DMA(7, 1);
#pragma unroll
        for (int a = 0; a < NA; ++a) { ta[a] = cmulc_f16(dre[a], dim[a], t1); dre[a] = mfma32_f16(ac2re, ta[a], z4); dim[a] = mfma32_f16(ac2im, ta[a], z4); }
        DMA(7, 2);
#pragma unroll
        for (int a = 0; a < NA; ++a) {
          f32x4 fr_, fi_;
#pragma unroll
          for (int r = 0; r < 4; ++r) { fr_[r] = bf_lo(fin[a][r]); fi_[r] = bf_hi(fin[a][r]); }
          ta[a] = cmul_pack<false, true>(dre[a], dim[a], fr_, fi_); dre[a] = mfma32_bf16(ta[a], bc3re, z4); dim[a] = mfma32_bf16(ta[a], bc3im, z4);
          DMA(7, 3 + a);
        }
#pragma unroll
        for (int a = 0; a < NA; ++a) {
          ta[a] = cmul_pack<false, true>(dre[a], dim[a], t2c, t2s); dre[a] = mfma32_bf16(ac4re, ta[a], z4); dim[a] = mfma32_bf16(ac4im, ta[a], z4);
          if (a == 0) DMA(7, 5);
        }
      };
      if (!G) {
        if (special) {
          fin[0] = jp > 4 ? fsp[1] : fsp[0];
          if (two) {
            const u32x4 us = uin[0];
            uin[0] = us >> 16; fin[0] = fsp[1];
            col_xform(IC<1>{});
            keep = dre[0];
            uin[0] = us & 0xffffu; fin[0] = fsp[0];
          }
        }
      }
      col_xform(IC<2>{});
      if (!G) { if (two) dim[0] = keep; }
#pragma unroll
      for (int a = 0; a < 2; ++a) *(LAS u32x4*)(colp + a * 1024) = pack4<true>(dre[a], dim[a]);
    }
    STAMP(29);
    GWAIT(12);
    BAR();
    STAMP(7);
    u32 vwd[4];
#pragma unroll
    for (int j = 0; j < 4; ++j) vwd[j] = (j * 7 + (lane >> 3)) * 1024 + ct * 128 + (((lane & 7) ^ (((lane >> 4) & 1) << 1) ^ ((j & 1) << 2)) << 4);
    const u32 kown = kr * 224 * 1024, koth = (kr ^ 4) * 224 * 1024;
    auto wd_issue = [&](int own, int ks, int s) {
      SB(); glds_a(lw, wslot(s), vwd[0], vwd[1], sd_, (own ? kown : koth) + ks * 32768); glds_b(vwd[2], vwd[3], sd_, (own ? kown : koth) + ks * 32768); SB();
    };
    u32x2 csv[4], stv;
    const u32 csoff = WS_CS + ((t & 1) * F_ + fj * 128 + hw * 32 + (lane >> 5) * 16 + ((p16 >> 2) & 1) * 8 + (p16 >> 3) * 4 + (p16 & 3)) * 8;
    const u32 stoff = WS_ST + (((mi & 1) * 128 + hw * 32 + (lane & 31)) * 8 + (mi >> 1)) * 8;
    {
      const int ps = ht >> 2;
      u32 vo[4][2];
#pragma unroll
      for (int k = 0; k < 4; ++k) { LAS char* cp = caddr((ps >> 5) + 2 * k + 8 * (ps & 31)); vo[k][0] = *(LAS u32*)cp; vo[k][1] = *(LAS u32*)(cp + 1024); }
      if (G) {
        ALD2S(csv[0], csoff, sw, 0, " sc1"); ALD2S(csv[1], csoff, sw, 2 * F_ * 8, " sc1"); ALD2S(csv[2], csoff, sw, 4 * F_ * 8, " sc1"); ALD2S(csv[3], csoff, sw, 6 * F_ * 8, " sc1");
        ALD2(stv, stoff, sw, 0, " sc1");
      }
#pragma unroll
      for (int k = 0; k < 4; ++k) st_sc1(rw, WS_V + b * 16384 + k * 4096 + ht * 16, u32x4{vo[k][0], tag, vo[k][1], tag});
    }
    STAMP(20);
    wd_issue(1, 0, 0); wd_issue(1, 1, 1); wd_issue(1, 2, 2);
    if (G) {
      WAITV(4 + 12);
      PIN(csv[0]); PIN(csv[1]); PIN(csv[2]); PIN(csv[3]); PIN(stv);
#define CHK3 all_ok((stv[1] == tag) & (csv[0][1] == tag) & (csv[1][1] == tag) & (csv[2][1] == tag) & (csv[3][1] == tag))
      if (__builtin_expect(!CHK3, 0)) {
#pragma nounroll
        for (int spin = 0; spin < SPIN_CAP; ++spin) {
          asm volatile("" ::: "memory");
#pragma unroll
          for (int k = 0; k < 4; ++k) csv[k] = ld_sc1_2(rw, csoff + k * 2 * F_ * 8);
          stv = ld_sc1_2(rw, stoff);
          if (CHK3) break;
        }
      }
      const u32 c0 = csv[0][0], c1 = csv[1][0], c2 = csv[2][0], c3 = csv[3][0];
      const float cs = (__builtin_bit_cast(float, c0) + __builtin_bit_cast(float, c1)) + (__builtin_bit_cast(float, c2) + __builtin_bit_cast(float, c3));
      const float ch = rnd_bf(cs);
      LAS char* cp = sm + FS + 8192 + hw * 2048 + lane * 4;
      *(LAS u32*)cp = pk_bf16(ch, cs - ch);
      *(LAS u32*)(cp + 256) = __builtin_amdgcn_perm(0u, stv[0], 0x01000100u) ^ 0x80008000u;
    }
    BAR();
    STAMP(8);
    wd_issue(0, 0, 3); wd_issue(0, 1, 4); wd_issue(0, 2, 5);
    WAITV(12);
    wd_read(0, 0); wd_read(1, 1); wd_read(2, 2);
    u32x4 xr[2], rv[2][2];
    const u32 voff = WS_V + ((b & 7) * 32 + (b >> 3)) * 64 + (p16 & 3) * 16 + ((8 * hw + (p16 >> 2)) * 8 + 2 * t) * 16384;
    if (!G) {
#pragma nounroll
      for (int spin = 0; spin < SPIN_CAP; ++spin) {
        asm volatile("" ::: "memory");
        u32x2 q = ld_sc1_2(rw, WS_V + (b + 224) * 16384 + hw * 4096);
        if (all_ok(q[1] == tag)) break;
        __builtin_amdgcn_s_sleep(8);
      }
      __builtin_amdgcn_s_sleep(24);
    }
    GBAR();
    STAMP(9);
    GBAR();
    STAMP(10);
    {
      const u32 xo = (b + 256 * hw) * 1024 + (4 * p16 + t) * 16;
      ALD4S(rv[0][0], voff, sw, 0, " sc1"); ALD4S(rv[0][1], voff, sw, 16384, " sc1"); ALD4S(rv[1][0], voff, sw, 32 * 16384, " sc1"); ALD4S(rv[1][1], voff, sw, 33 * 16384, " sc1");
      ALD4S(xr[0], xo, sx, 0, ""); ALD4S(xr[1], xo, sx, 1048576, "");
    }
    asm volatile("s_waitcnt lgkmcnt(0)" ::: "memory");
    wd_issue(1, 3, 0); wd_issue(1, 4, 1); wd_issue(1, 5, 2);
    wd_issue(0, 3, 6); wd_issue(0, 4, 7); wd_issue(0, 5, 8);
    STAMP(11);
    WAITV(24);
    PIN(rv[0][0]); PIN(rv[0][1]); PIN(rv[1][0]); PIN(rv[1][1]); PIN(xr[0]); PIN(xr[1]);
#define CHK4 all_ok(tags_ok(rv[0][0], tag) & tags_ok(rv[0][1], tag) & tags_ok(rv[1][0], tag) & tags_ok(rv[1][1], tag))
    if (__builtin_expect(!CHK4, 0)) {
#pragma nounroll
      for (int spin = 0; spin < SPIN_CAP; ++spin) {
        asm volatile("" ::: "memory");
#pragma unroll
        for (int k = 0; k < 2; ++k) { rv[k][0] = ld_sc1(rw, voff + k * 32 * 16384); rv[k][1] = ld_sc1(rw, voff + k * 32 * 16384 + 16384); }
        if (CHK4) break;
        __builtin_amdgcn_s_sleep(4);
      }
    }
    STAMP(12);
    LAS char* const yb = sm + FS + 8192;
#pragma unroll
    for (int k = 0; k < 2; ++k) {
      f32x4 da = mfma16_bf16(u32x2{qa8i[0], qa8i[1]}, u32x2{rv[k][0][0], rv[k][1][0]}, z4), db = mfma16_bf16(u32x2{qa8i[0], qa8i[1]}, u32x2{rv[k][0][2], rv[k][1][2]}, z4);
      if (k == 0) { f32x4 ds = mfma16_bf16(u32x2{qa8i[2], qa8i[3]}, u32x2{rv[0][0][0], rv[0][1][0]}, z4); if ((hw | p16) == 0) da = ds; }
      *(LAS u32x2*)(yb + (2 * t) * 1024 + (32 * hw + 16 * k + p16) * 8) = u32x2{pk_bf16(da[0], da[1]), pk_bf16(db[0], db[1])};
      *(LAS u32x2*)(yb + (2 * t + 1) * 1024 + (32 * hw + 16 * k + p16) * 8) = u32x2{pk_bf16(da[2], da[3]), pk_bf16(db[2], db[3])};
    }
    WAITV(12);
    wd_read(3, 0); wd_read(4, 1); wd_read(5, 2);
    BAR();
    wd_issue(1, 6, 0); wd_issue(0, 6, 1);
    STAMP(13);
#pragma unroll
    for (int a = 0; a < 2; ++a) {
      u32x4 av;
#pragma unroll
      for (int m = 0; m < 4; ++m) av[m] = *(LAS u32*)(yb + (hw + 4 * a) * 1024 + (lane + 64 * m) * 4);
      const float nyq = bf_hi(*(LAS u32*)(yb + (hw + 4 * a) * 1024));
#pragma unroll
      for (int hh = 0; hh < 2; ++hh) {
        f32x4 gre = mfma32_bf16(av, bi1re[hh], z4), gim = mfma32_bf16(av, bi1im[hh], z4);
        u32x4 bb = cmul_pack<false, true>(gre, gim, t4c[hh], t4s[hh]);
        f32x4 d = mfma32_bf16(bb, bc3re, f32x4{nyq, -nyq, nyq, -nyq});
        st_sc1(rw, WS_R + (b + 256 * hw + 1024 * a) * 2048 + hh * 1024 + (4 * p16 + t) * 16,
               u32x4{pk_bf16(__builtin_fmaf(d[0], FSC, bf_lo(xr[a][2 * hh])), __builtin_fmaf(d[1], FSC, bf_hi(xr[a][2 * hh]))), tag,
                     pk_bf16(__builtin_fmaf(d[2], FSC, bf_lo(xr[a][2 * hh + 1])), __builtin_fmaf(d[3], FSC, bf_hi(xr[a][2 * hh + 1]))), tag});
      }
    }
    SB();
    WAITV(4);
    __builtin_amdgcn_s_setprio(0);
    STAMP(14);
  };

  if (w < 4) {
    if (is_gemm) {
#pragma unroll
      for (int k = 0; k < 6; ++k) g1_issue(0, k);
      tag = load_tag();
      const int h = lane >> 5;
      int xoff[4], woff[2];
#pragma unroll
      for (int ks = 0; ks < 4; ++ks) { int tok = lane & 31; xoff[ks] = tok * 128 + (((2 * ks + h) ^ ((tok >> 1) & 7)) << 4); PIN(xoff[ks]); }
      {
        int arr = t & 1, q = p16 >> 2, p = lane & 3;
#pragma unroll
        for (int m = 0; m < 2; ++m) {
          woff[m] = 16384 + arr * 16384 + (8 * h + q) * 256 + (((hw * 2 + m) ^ (q | (arr << 2))) << 5) + (p & 1) * 16 + (p >> 1) * 8;
          PIN(woff[m]);
        }
      }
      auto rdx = [&](Frag& f, int st, int ks, int n) { f.bx[n] = *(LAS u32x4*)(sm + (st % 3) * G1_STAGE + xoff[ks] + n * 4096); };
      auto rdw = [&](Frag& f, int st, int ks, int m) {
        LAS char* buf = sm + (st % 3) * G1_STAGE;
        u32x2 lo = tr_read(buf + woff[m] + ks * 4096), hi = tr_read(buf + woff[m] + ks * 4096 + 1024);
        f.aw[m] = u32x4{lo[0], lo[1], hi[0], hi[1]};
      };
      f32x16 acc[2][4];
#pragma unroll
      for (int m = 0; m < 2; ++m)
#pragma unroll
        for (int n = 0; n < 4; ++n)
#pragma unroll
          for (int k = 0; k < 16; ++k) acc[m][n][k] = 0.f;
      Frag fr[2];
      u32 qa[2], qb[4];
      SB();
      STAMP(21);
      g1_issue(1, 0); g1_issue(1, 1);
      WAITV(4);
      {
        volatile LAS u32* rv_ = (volatile LAS u32*)(sm + FS + 16384 - 16);
        rv_[hw] = tag;
#pragma nounroll
        for (int spin = 0; spin < SPIN_CAP; ++spin) { if (all_ok(rv_[lane & 3] == tag)) break; }
        asm volatile("" ::: "memory");
      }
      STAMP(1);
      rdw(fr[0], 0, 0, 0); rdx(fr[0], 0, 0, 0); rdx(fr[0], 0, 0, 1); rdx(fr[0], 0, 0, 2); rdx(fr[0], 0, 0, 3); rdw(fr[0], 0, 0, 1);
      static_for<0, 31>([&](auto KS_) {
        constexpr int KS = decltype(KS_)::v, st = KS >> 2, ks = KS & 3;
        constexpr int nst = ks < 3 ? st : st + 1, nks = (ks + 1) & 3;
        Frag& f = fr[KS & 1]; Frag& g = fr[(KS + 1) & 1];
        SB();
        if constexpr (ks == 3) { if constexpr (st == 0) WAITV(0); BAR(); STAMP(2 + st); SB(); }
        acc[0][0] = mfma3232(f.aw[0], f.bx[0], acc[0][0]); SB(); rdw(g, nst, nks, 0); SB();
        acc[0][1] = mfma3232(f.aw[0], f.bx[1], acc[0][1]); SB(); rdx(g, nst, nks, 0); SB();
        if constexpr (KS == 0 && S1PRE != 6) { g1_issue(1, 2); SB(); }
        if constexpr (KS == 1 && S1PRE != 6) { g1_issue(1, 5); SB(); }
        acc[0][2] = mfma3232(f.aw[0], f.bx[2], acc[0][2]); SB(); rdx(g, nst, nks, 1); SB();
        acc[0][3] = mfma3232(f.aw[0], f.bx[3], acc[0][3]); SB(); rdx(g, nst, nks, 2); SB();
        if constexpr (KS == 0 && S1PRE != 6) { g1_issue(1, 3); SB(); }
        acc[1][3] = mfma3232(f.aw[1], f.bx[3], acc[1][3]); SB(); rdx(g, nst, nks, 3); SB();
        acc[1][2] = mfma3232(f.aw[1], f.bx[2], acc[1][2]); SB(); rdw(g, nst, nks, 1); SB();
        acc[1][1] = mfma3232(f.aw[1], f.bx[1], acc[1][1]); SB();
        if constexpr (KS == 0 && S1PRE != 6) { g1_issue(1, 4); SB(); }
        if constexpr (KS == 27) {
          LAS char* cp = sm + FS + 8192 + (lane & 31) * 4;
          qa[0] = *(LAS u32*)(cp + hw * 2048); qa[1] = *(LAS u32*)(cp + hw * 2048 + 128);
#pragma unroll
          for (int n = 0; n < 4; ++n) qb[n] = *(LAS u32*)(cp + n * 2048 + 256);
          SB();
        }
        acc[1][0] = mfma3232(f.aw[1], f.bx[0], acc[1][0]); SB();
        if constexpr (KS == 28) {
#pragma unroll
          for (int m = 0; m < 2; ++m)
#pragma unroll
            for (int n = 0; n < 4; ++n) acc[m][n] = mfma3232(u32x4{h ? 0u : qa[m], 0u, 0u, 0u}, u32x4{qb[n], 0u, 0u, 0u}, acc[m][n]);
          SB();
        }
      });
      BAR();
      __builtin_amdgcn_s_setprio(3);
      STAMP(9);
      auto epi = [&](int m, int n) {
        u32x4 o;
#pragma unroll
        for (int k = 0; k < 4; ++k)
          o[k] = pk_bf16(__builtin_fmaxf(acc[m][n][2 * k], 0.f) * acc[m][n][8 + 2 * k], __builtin_fmaxf(acc[m][n][2 * k + 1], 0.f) * acc[m][n][9 + 2 * k]);
        st_sc1(rw, WS_H + ((mi * 8 + n * 2 + (t & 1)) * 56 + fj * 4 + hw) * 1024 + (m * 2 + h) * 256 + p16 * 16, o);
      };
      {
        Frag& f = fr[1];
#pragma unroll
        for (int q = 0; q < 8; ++q) {
          acc[q & 1][q >> 1] = mfma3232(f.aw[q & 1], f.bx[q >> 1], acc[q & 1][q >> 1]);
          SB();
          if (q >= 2 && (q & 1) == 0) { epi((q / 2 - 1) & 1, (q / 2 - 1) >> 1); SB(); }
        }
#pragma unroll
        for (int q = 3; q < 8; ++q) { epi(q & 1, q >> 1); SB(); }
      }
      RAWBAR();
      STAMP(10);
      WAITV(4);
      if (lane == 0) __builtin_amdgcn_raw_buffer_store_b32(tag, rw, WS_F2 + (mi * 2) * 256 + (fj * 4 + hw) * 4, 0, 16);
      WAITV(1);
      if (lane == 0) __builtin_amdgcn_raw_buffer_store_b32(tag, rw, WS_F2 + (mi * 2 + 1) * 256 + (fj * 4 + hw) * 4, 0, 16);
      STAMP(12);
    } else {
      const int un = b >> 1, ua = un >= 7, uc = (b & 1) * 7 + (ua ? un - 7 : un);
      const u32x4 scs = mk_srd(ua ? wu : wg, b < 28 ? 1835008 : 0);
      const u32 vcs = (hw * 4 + t) * 3584 + uc * 256 + p16 * 16;
      u32x4 vv[16];
      f32x4 a0 = z4, a1 = z4;
#pragma unroll
      for (int half = 0; half < 2; ++half) {
#pragma unroll
        for (int k = 0; k < 16; ++k) ALD4S(vv[k], vcs, scs, (half * 16 + k) * 16 * 3584, "");
        if (half == 0) { tag = load_tag(); RAWBAR();   }
        WAITV(0);
#pragma unroll
        for (int k = 0; k < 16; ++k) {
          PIN(vv[k]);
          a0 += f32x4{bf_lo(vv[k][0]), bf_hi(vv[k][0]), bf_lo(vv[k][1]), bf_hi(vv[k][1])};
          a1 += f32x4{bf_lo(vv[k][2]), bf_hi(vv[k][2]), bf_lo(vv[k][3]), bf_hi(vv[k][3])};
        }
        PIN(a0); PIN(a1);
        if (half == 0) {
          const int q = 64 * g + 16 * hw + p16, ks = 8 * q + jp, km = (2048 - ks) & 2047, col = (t >> 1) * 256;
          const unsigned short* wa = (t & 1) ? wi : wr;
          const float va = bf_lo(wa[ks * DF_ + col]), vb = bf_lo(wa[km * DF_ + col]);
          const float wh = 0.5f * ((t & 1) ? va - vb : va + vb);
          const u32 wb = pk_bf16(wh, wh) & 0xffffu;
          auto s2 = __builtin_amdgcn_permlane16_swap(wb, wb, false, false);
          if ((t & 1) == 0) st_sc1_2(rw, WS_SF + jp * 4096 + (t >> 1) * 2048 + (64 * g + 4 * p16 + hw) * 8, u32x2{(u32)s2[0] | ((u32)s2[1] << 16), tag});
          STAMP(2);
        }
      }
      if (b < 28) {
#pragma unroll
        for (int k = 0; k < 4; ++k) { a0[k] = xrow_sum(a0[k]); a1[k] = xrow_sum(a1[k]); }
        const float s0 = t == 0 ? a0[0] : t == 1 ? a0[2] : t == 2 ? a1[0] : a1[2];
        const float s1 = t == 0 ? a0[1] : t == 1 ? a0[3] : t == 2 ? a1[1] : a1[3];
        st_sc1(rw, WS_CS + hw * (2 * F_ * 8) + (ua * 14 + uc) * 1024 + p16 * 64 + t * 16, u32x4{__builtin_bit_cast(u32, s0), tag, __builtin_bit_cast(u32, s1), tag});
      }
      STAMP(3);
      {
        const u32x4 sdp = mk_srd(wd, 1835008);
#pragma unroll
        for (int k = 0; k < 14; ++k) glds16(lw, G1_STAGE, b * 57344 + hw * 1024 + lane * 16, sdp, k * 4096);
      }
      RAWBAR(); RAWBAR(); RAWBAR();
      WAITV(0);
      RAWBAR();
    }
    RAWBAR();
    STAMP(13);
    wd_read(0, 3); wd_read(1, 4); wd_read(2, 5);
  } else if (is_gemm) helper(IC<1>{});
  else helper(IC<0>{});

  {
    const u32 foff = WS_F2 + rt * 256 + (kr * 7 + (lane < 7 ? lane : 0)) * 4;
#pragma nounroll
    for (int spin = 0; spin < SPIN_CAP; ++spin) {
      asm volatile("" ::: "memory");
      u32 fl = __builtin_amdgcn_raw_buffer_load_b32(rw, foff, 0, 16);
      if (all_ok(fl == tag)) break;
      __builtin_amdgcn_s_sleep(2);
    }
    __builtin_amdgcn_fence(__ATOMIC_ACQUIRE, "wavefront");
    asm volatile("" ::: "memory");
    STAMP(15);
#pragma unroll
    for (int tt = 0; tt < 3; ++tt)
#pragma unroll
      for (int ks = 0; ks < 7; ++ks) hf[tt][ks] = ld_sc1(rw, hs + (tt * 56 + ks) * 1024);
    SB();
    RAWBAR();
    if (w < 4) { wd_read(3, 6); wd_read(4, 7); wd_read(5, 8); }
    {
      LAS char* p0 = sm + 2 * G1_STAGE + (w < 4 ? 16384 : 0);
#pragma unroll
      for (int u = 0; u < 4; ++u) {
        LAS char* p = p0 + (wbase ^ (u << 5));
        u32x2 lo = tr_read(p), hi = tr_read(p + 512);
        wf[6][u] = u32x4{lo[0], lo[1], hi[0], hi[1]};
      }
    }
  }

  STAMP(16);
  const int oc = lane & 7, otok = (w & 1) * 8 + (lane >> 3);
  const int orow = rt * 64 + (w >> 1) * 16 + otok;
  const u32 roff = WS_R + orow * 2048 + (ct * 8 + oc) * 16;
  const u32 soff_ = WS_ST + ((orow & 255) * 8 + (orow >> 8)) * 8;
  u32x4 r0, r1; u32x2 sto;
#pragma unroll
  for (int tt = 0; tt < 4; ++tt) {
    f32x4 acc[4] = {z4, z4, z4, z4};
#pragma unroll
    for (int ks = 0; ks < 7; ++ks) {
#pragma unroll
      for (int u = 0; u < 4; ++u) acc[u] = mfma32_bf16(wf[ks][u], hf[tt][ks], acc[u]);
      if (tt == 0) { SB(); hf[3][ks] = ld_sc1(rw, hs + (3 * 56 + ks) * 1024); SB(); }
    }
    if (tt == 0) { BAR(); }
    if (tt == 2) { r0 = ld_sc1(rw, roff); r1 = ld_sc1(rw, roff + 1024); sto = ld_sc1_2(rw, soff_); }
#pragma unroll
    for (int u = 0; u < 4; ++u) *(LAS f32x4*)(sm + (tt * 4 + u) * 8192 + w * 1024 + t * 256 + ((p16 ^ (4 * u + (t & 2))) << 4)) = acc[u];
    SB();
  }
  STAMP(17);
  BAR();
  f32x4 sa = z4, sb = z4;
  {
    const int o0 = ((w >> 1) * 4 + (oc >> 1)) * 8192 + (oc & 1) * 512 + ((otok ^ (2 * oc)) << 4);
#pragma unroll
    for (int pw = 0; pw < 8; ++pw) { sa += *(LAS f32x4*)(sm + o0 + pw * 1024); sb += *(LAS f32x4*)(sm + o0 + 256 + pw * 1024); }
  }
  STAMP(22);
#define CHK2 all_ok(tags_ok(r0, tag) & tags_ok(r1, tag) & (sto[1] == tag))
  if (__builtin_expect(!CHK2, 0)) {
#pragma nounroll
    for (int spin = 0; spin < SPIN_CAP; ++spin) {
      asm volatile("" ::: "memory");
      r0 = ld_sc1(rw, roff); r1 = ld_sc1(rw, roff + 1024); sto = ld_sc1_2(rw, soff_);
      if (CHK2) break;
    }
  }
  {
    const float rs = __builtin_amdgcn_rcpf(bf_hi(sto[0])), rs2 = rs * rs;
    *(u32x4*)(out + (size_t)orow * 1024 + ct * 128 + oc * 16) =
        u32x4{pk_bf16(__builtin_fmaf(sa[0], rs2, bf_lo(r0[0])), __builtin_fmaf(sa[1], rs2, bf_hi(r0[0]))),
              pk_bf16(__builtin_fmaf(sa[2], rs2, bf_lo(r0[2])), __builtin_fmaf(sa[3], rs2, bf_hi(r0[2]))),
              pk_bf16(__builtin_fmaf(sb[0], rs2, bf_lo(r1[0])), __builtin_fmaf(sb[1], rs2, bf_hi(r1[0]))),
              pk_bf16(__builtin_fmaf(sb[2], rs2, bf_lo(r1[2])), __builtin_fmaf(sb[3], rs2, bf_hi(r1[2])))};
  }
  if (b == 0 && tid == 0) __builtin_amdgcn_raw_buffer_store_b32(tag, rw, WS_EPOCH, 0, 16);
  STAMP(18);
  STAMP_DUMP();
}

#if defined(__HIP_DEVICE_COMPILE__)
#pragma clang attribute pop
#endif

extern "C" void kernel_launch(void* const* d_in, const int* in_sizes, int n_in, void* d_out, int out_size, void* d_ws, size_t ws_size,
                              hipStream_t stream) {
  (void)hipFuncSetAttribute((const void*)fused_kernel, hipFuncAttributeMaxDynamicSharedMemorySize, LDS_BYTES);
  fused_kernel<<<256, 512, LDS_BYTES, stream>>>((const char*)d_in[0], (const unsigned short*)d_in[1], (const unsigned short*)d_in[2], (const char*)d_in[4],
                                                (const char*)d_in[5], (const char*)d_in[6], (char*)d_ws, (char*)d_out);
}
```

```cpp
#include <hip/hip_runtime.h>
#include <stdint.h>

#define LAS __attribute__((address_space(3)))
#define GAS __attribute__((address_space(1)))
#define DI __device__ __forceinline__
typedef unsigned u32;
typedef unsigned u32x2 __attribute__((ext_vector_type(2)));
typedef unsigned u32x4 __attribute__((ext_vector_type(4)));
typedef float f32x2 __attribute__((ext_vector_type(2)));
typedef float f32x4 __attribute__((ext_vector_type(4)));
typedef float f32x16 __attribute__((ext_vector_type(16)));
typedef short s16x4 __attribute__((ext_vector_type(4)));
typedef _Float16 h16x2 __attribute__((ext_vector_type(2)));
typedef _Float16 h16x4 __attribute__((ext_vector_type(4)));
typedef _Float16 h16x8 __attribute__((ext_vector_type(8)));
typedef __bf16 b16x2 __attribute__((ext_vector_type(2)));
typedef __bf16 b16x8 __attribute__((ext_vector_type(8)));
typedef __amdgpu_buffer_rsrc_t rsrc_t;

constexpr int F_ = 1792, DF_ = 257;
constexpr u32 WS_EPOCH = 0;
constexpr u32 WS_F2 = 4096;
constexpr u32 WS_ST = 16384;
constexpr u32 WS_CS = WS_ST + 16384;
constexpr u32 WS_U = 262144;
constexpr u32 WS_V = WS_U + 4194304;
constexpr u32 WS_R = WS_V + 4194304;
constexpr u32 WS_SF = 196608;
constexpr u32 WS_H = WS_R + 4194304;
constexpr u32 WS_END = WS_H + 7340032;
constexpr int LDS_BYTES = 163840;
constexpr int G1_STAGE = 49152;
constexpr int FS = 3 * G1_STAGE;
#define SPIN_CAP (1 << 17)
constexpr int CODE_LINES = 284;
#define HPRIO 2
#define S1PRE 2
#define POLLSLEEP 2
#define CS_DELAY 450
#define WARM 0
#define ROTM 1
constexpr float XS = 1.0f / 16;
constexpr float FSC = (2.0f / (2048.0f * 512.0f)) * 16.0f;
constexpr size_t WS_DBG = 64 << 20;
#define STAMP(k) do {} while (0)
#define STAMP_DECL
#define STAMP_DUMP() do {} while (0)

#if defined(__HIP_DEVICE_COMPILE__)
#pragma clang attribute push(__attribute__((target("no-packed-fp32-ops"))), apply_to = function)
#endif
DI u32 pk_f16(float a, float b) { h16x2 p = {(_Float16)a, (_Float16)b}; return __builtin_bit_cast(u32, p); }
DI u32 pk_bf16(float a, float b) { b16x2 p = {(__bf16)a, (__bf16)b}; return __builtin_bit_cast(u32, p); }
DI float bf_lo(u32 v) { return __builtin_bit_cast(float, v << 16); }
DI float bf_hi(u32 v) { return __builtin_bit_cast(float, v & 0xffff0000u); }
DI float rnd_bf(float a) { return bf_lo(pk_bf16(a, a)); }
DI float relu(float a) { int i = __builtin_bit_cast(int, a); return __builtin_bit_cast(float, i > 0 ? i : 0); }
DI float cosr(float rev) { return __builtin_amdgcn_cosf(rev); }
DI float sinr(float rev) { return __builtin_amdgcn_sinf(rev); }
DI f32x4 mfma16_f16(u32x2 a, u32x2 b, f32x4 c) { return __builtin_amdgcn_mfma_f32_16x16x16f16(__builtin_bit_cast(h16x4, a), __builtin_bit_cast(h16x4, b), c, 0, 0, 0); }
DI f32x4 mfma16_bf16(u32x2 a, u32x2 b, f32x4 c) { return __builtin_amdgcn_mfma_f32_16x16x16bf16_1k(__builtin_bit_cast(s16x4, a), __builtin_bit_cast(s16x4, b), c, 0, 0, 0); }
DI f32x4 mfma32_f16(u32x4 a, u32x4 b, f32x4 c) { return __builtin_amdgcn_mfma_f32_16x16x32_f16(__builtin_bit_cast(h16x8, a), __builtin_bit_cast(h16x8, b), c, 0, 0, 0); }
DI f32x4 mfma32_bf16(u32x4 a, u32x4 b, f32x4 c) { return __builtin_amdgcn_mfma_f32_16x16x32_bf16(__builtin_bit_cast(b16x8, a), __builtin_bit_cast(b16x8, b), c, 0, 0, 0); }
DI f32x16 mfma3232(u32x4 a, u32x4 b, f32x16 c) { return __builtin_amdgcn_mfma_f32_32x32x16_bf16(__builtin_bit_cast(b16x8, a), __builtin_bit_cast(b16x8, b), c, 0, 0, 0); }
DI u32x2 tr_read(LAS char* p) { return __builtin_bit_cast(u32x2, __builtin_amdgcn_ds_read_tr16_b64_v4i16((LAS s16x4*)p)); }
#define SB() __builtin_amdgcn_sched_barrier(0)
#define BAR() asm volatile("s_waitcnt lgkmcnt(0)\n\ts_barrier" ::: "memory")
#define WAITV(n) asm volatile("s_waitcnt vmcnt(%0)" :: "i"(n) : "memory")
#define PIN(x) asm volatile("" : "+v"(x))
DI u32x4 mk_srd(const void* p, u32 bytes) { uint64_t a = (uint64_t)p; return u32x4{(u32)a, (u32)(a >> 32), bytes, 0x00020000u}; }
DI void glds16(u32 ldsbase, int imm, u32 voff, u32x4 srd, u32 soff) {
  asm volatile("s_add_u32 m0, %0, %1\n\ts_nop 0\n\tbuffer_load_dwordx4 %2, %3, %4 offen lds" :: "s"(ldsbase), "i"(imm), "v"(voff), "s"(srd), "s"(soff) : "memory", "scc");
}
DI void glds4(u32 ldsaddr, u32 voff, u32x4 srd, u32 soff) {
  asm volatile("s_mov_b32 m0, %0\n\ts_nop 0\n\tbuffer_load_dword %1, %2, %3 offen lds" :: "s"(ldsaddr), "v"(voff), "s"(srd), "s"(soff) : "memory");
}
#define ALD4(dst, voff, srd, IMM, FL) asm volatile("buffer_load_dwordx4 %0, %1, %2, 0 offen offset:" #IMM FL : "=v"(dst) : "v"(voff), "s"(srd) : "memory")
#define ALD2(dst, voff, srd, IMM, FL) asm volatile("buffer_load_dwordx2 %0, %1, %2, 0 offen offset:" #IMM FL : "=v"(dst) : "v"(voff), "s"(srd) : "memory")
#define ALD1(dst, voff, srd, IMM, FL) asm volatile("buffer_load_dword %0, %1, %2, 0 offen offset:" #IMM FL : "=v"(dst) : "v"(voff), "s"(srd) : "memory")
DI u32x4 ld_sc1(rsrc_t r, u32 off) { return __builtin_amdgcn_raw_buffer_load_b128(r, off, 0, 16); }
DI u32x2 ld_sc1_2(rsrc_t r, u32 off) { return __builtin_amdgcn_raw_buffer_load_b64(r, off, 0, 16); }
DI void st_sc1(rsrc_t r, u32 off, u32x4 v) { __builtin_amdgcn_raw_buffer_store_b128(v, r, off, 0, 16); }
DI void st_sc1_2(rsrc_t r, u32 off, u32x2 v) { __builtin_amdgcn_raw_buffer_store_b64(v, r, off, 0, 16); }
DI bool all_ok(bool ok) { return __builtin_amdgcn_ballot_w64(!ok) == 0; }
DI bool tags_ok(u32x4 a, u32 tag) { return (a[1] == tag) & (a[3] == tag); }

template <int CTRL> DI float dpp_add(float v) {
  int o = __builtin_amdgcn_update_dpp(0, __builtin_bit_cast(int, v), CTRL, 0xf, 0xf, true);
  return v + __builtin_bit_cast(float, o);
}
DI float row_sum(float v) { v = dpp_add<0xB1>(v); v = dpp_add<0x4E>(v); v = dpp_add<0x141>(v); return dpp_add<0x140>(v); }
DI float xrow_sum(float v) {
  u32 u = __builtin_bit_cast(u32, v);
  auto a = __builtin_amdgcn_permlane16_swap(u, u, false, false);
  v = __builtin_bit_cast(float, (u32)a[0]) + __builtin_bit_cast(float, (u32)a[1]);
  u = __builtin_bit_cast(u32, v);
  auto b = __builtin_amdgcn_permlane32_swap(u, u, false, false);
  return __builtin_bit_cast(float, (u32)b[0]) + __builtin_bit_cast(float, (u32)b[1]);
}
template <bool NEG, bool BF> DI u32x4 cmul_pack(f32x4 re, f32x4 im, f32x4 c, f32x4 s) {
  f32x4 a = NEG ? re * c + im * s : re * c - im * s;
  f32x4 b = NEG ? im * c - re * s : im * c + re * s;
  u32x4 o;
#pragma unroll
  for (int r = 0; r < 4; ++r) o[r] = BF ? pk_bf16(a[r], b[r]) : pk_f16(a[r], b[r]);
  return o;
}
DI u32 rot16(u32 v) { return __builtin_amdgcn_alignbit(v, v, 16); }
template <bool NEG> DI void cmul_half(f32x4& a, f32x4& b, f32x4 re, f32x4 im, f32x4 c, f32x4 s) {
  a = NEG ? re * c + im * s : re * c - im * s;
  b = NEG ? im * c - re * s : im * c + re * s;
}
template <bool BF> DI u32x4 pack4(f32x4 a, f32x4 b) {
  u32x4 o;
#pragma unroll
  for (int r = 0; r < 4; ++r) o[r] = BF ? pk_bf16(a[r], b[r]) : pk_f16(a[r], b[r]);
  return o;
}
DI void glds_a(u32 ldsbase, int imm, u32 v0, u32 v1, u32x4 srd, u32 soff) {
  asm volatile("s_add_u32 m0, %0, %1\n\ts_nop 0\n\tbuffer_load_dwordx4 %2, %4, %5 offen lds\n\tbuffer_load_dwordx4 %3, %4, %5 offen offset:1024 lds"
               :: "s"(ldsbase), "i"(imm), "v"(v0), "v"(v1), "s"(srd), "s"(soff) : "memory", "scc");
}
DI void glds_b(u32 v2, u32 v3, u32x4 srd, u32 soff) {
  asm volatile("buffer_load_dwordx4 %0, %2, %3 offen offset:2048 lds\n\tbuffer_load_dwordx4 %1, %2, %3 offen offset:3072 lds"
               :: "v"(v2), "v"(v3), "s"(srd), "s"(soff) : "memory");
}
DI u32x4 cmulc_f16(f32x4 re, f32x4 im, u32x4 tw) {
  u32 z0 = pk_f16(re[0], im[0]), z1 = pk_f16(re[1], im[1]), z2 = pk_f16(re[2], im[2]), z3 = pk_f16(re[3], im[3]);
  u32 o0, o1, o2, o3;
  const u32 t0 = tw[0], t1 = tw[1], t2 = tw[2], t3 = tw[3];
#define PKM " op_sel_hi:[1,0]\n\t"
#define PKF " op_sel:[1,1,0] op_sel_hi:[0,1,1] neg_hi:[0,1,0]\n\t"
  asm("v_pk_mul_f16 %0, %4, %8" PKM "v_pk_mul_f16 %1, %5, %9" PKM "v_pk_mul_f16 %2, %6, %10" PKM "v_pk_mul_f16 %3, %7, %11" PKM
      "v_pk_fma_f16 %0, %4, %8, %0" PKF "v_pk_fma_f16 %1, %5, %9, %1" PKF "v_pk_fma_f16 %2, %6, %10, %2" PKF "v_pk_fma_f16 %3, %7, %11, %3" PKF "s_nop 1"
      : "=&v"(o0), "=&v"(o1), "=&v"(o2), "=&v"(o3) : "v"(z0), "v"(z1), "v"(z2), "v"(z3), "v"(t0), "v"(t1), "v"(t2), "v"(t3));
  return u32x4{o0, o1, o2, o3};
}
template <int N> struct IC { static constexpr int v = N; };
template <int A, int B, class Fn> DI void static_for(Fn&& f) { if constexpr (A < B) { f(IC<A>{}); static_for<A + 1, B>(f); } }

struct Frag { u32x4 bx[4], aw[2]; };
#define ALD4S(dst, voff, srd, SOFF, FL) asm volatile("buffer_load_dwordx4 %0, %1, %2, %3 offen" FL : "=v"(dst) : "v"(voff), "s"(srd), "s"(SOFF) : "memory")
#define ALD2S(dst, voff, srd, SOFF, FL) asm volatile("buffer_load_dwordx2 %0, %1, %2, %3 offen" FL : "=v"(dst) : "v"(voff), "s"(srd), "s"(SOFF) : "memory")
#define RAWBAR() do { asm volatile("s_barrier" ::: "memory"); } while (0)

typedef unsigned u32x16 __attribute__((ext_vector_type(16)));
__global__ __launch_bounds__(512, 2) void fused_kernel(const char* a_x, const unsigned short* a_wr, const unsigned short* a_wi, const char* a_wg,
                                                       const char* a_wu, const char* a_wd, char* a_ws, char* a_out) {
  extern __shared__ __attribute__((aligned(16))) char smem[];
  LAS char* sm = (LAS char*)smem;
  STAMP_DECL
  STAMP(0);
  u32x16 ka;
  asm volatile("s_load_dwordx16 %0, %1, 0x0" : "=s"(ka) : "s"(__builtin_amdgcn_kernarg_segment_ptr()) : "memory");
  const int b = blockIdx.x, tid = threadIdx.x, lane = tid & 63, w = __builtin_amdgcn_readfirstlane(tid >> 6);
  const int hw = w & 3, p16 = lane & 15, t = lane >> 4;
  const u32 lds0 = (u32)(uintptr_t)sm;
  {
    uint64_t pc; asm volatile("s_getpc_b64 %0" : "=s"(pc));
    asm volatile("s_cmp_gt_u32 %0, 7\n\ts_cbranch_scc1 1f\n\ts_mov_b32 m0, %1\n\ts_nop 0\n\tbuffer_load_dword %2, %3, 0 offen lds\n1:"
                 :: "s"(b), "s"(lds0), "v"(tid * 128), "s"(u32x4{(u32)pc, (u32)(pc >> 32), CODE_LINES * 128, 0x00020000u}) : "memory", "scc");
  }
  const bool is_gemm = b >= 32;
  const int xc = b & 7, r_ = (b - 32) >> 3;
  const int mi = (xc >> 1) * 4 + (r_ & 3), fj = (xc & 1) * 7 + (r_ >> 2);
  const int rot = ((r_ & 3) + (r_ >> 2)) & ROTM;
  const u32 lw = lds0 + hw * 4096;
  u32 vx[4], vg[4], vu[4];
#pragma unroll
  for (int j = 0; j < 4; ++j) {
    vx[j] = (mi * 128 + hw * 32 + j * 7 + (lane >> 3)) * 1024 + (((lane & 7) ^ ((4 * j + t) & 7)) << 4);
    vg[j] = (hw * 16 + j * 4 + t) * 3584 - j * 1024 + fj * 256 + ((p16 ^ (2 * t)) << 4);
    vu[j] = vg[j] ^ 128;
    PIN(vx[j]); PIN(vg[j]); PIN(vu[j]);
  }
  u32 vrow = (b + 256 * hw) * 1024 + lane * 16;
  asm volatile("s_cmp_lt_u32 %0, 4\n\ts_cbranch_scc1 1f\n\ts_setprio 2\n1:" :: "s"(w) : "scc");
  if (w < 4) vrow = 0x7ff00000u;
  const u32 lrow = w < 4 ? lw : lds0 + FS + hw * 1024;
  PIN(vrow);
  SB();
  asm volatile("s_waitcnt lgkmcnt(0)" : "+s"(ka) :: "memory");
#define KARG(i) (((uint64_t)ka[2 * (i) + 1] << 32) | ka[2 * (i)])
  const u32x4 sx = {ka[0], ka[1], 2097152u, 0x00020000u}, sg = {ka[6], ka[7], 1835008u, 0x00020000u}, su = {ka[8], ka[9], 1835008u, 0x00020000u};
  auto g1_issue = [&](int st, int k) {
    const int lb = (st % 3) * G1_STAGE + (k >> 1) * 16384, sk = st ^ rot;
    if (k == 0) glds_a(lw, lb, vx[0], vx[1], sx, sk * 128);
    if (k == 1) glds_b(vx[2], vx[3], sx, sk * 128);
    if (k == 2) glds_a(lw, lb, vg[0], vg[1], sg, sk * 64 * 3584);
    if (k == 3) glds_b(vg[2], vg[3], sg, sk * 64 * 3584);
    if (k == 4) glds_a(lw, lb, vu[0], vu[1], su, sk * 64 * 3584);
    if (k == 5) glds_b(vu[2], vu[3], su, sk * 64 * 3584);
  };
  glds16(lrow, 0, vrow, sx, 0);
  glds16(lrow + (w < 4 ? 0 : 4096), 0, vrow, sx, 1048576);
  if (w < 4 && is_gemm) {
#pragma unroll
    for (int k = 0; k < 6; ++k) g1_issue(0, k);
  }
  SB();
  STAMP(30);
  const f32x4 z4 = {0.f, 0.f, 0.f, 0.f};
  const GAS unsigned short* wr = (const GAS unsigned short*)KARG(1); const GAS unsigned short* wi = (const GAS unsigned short*)KARG(2);
  GAS char* ws = (GAS char*)KARG(6); GAS char* out = (GAS char*)KARG(7);
  auto load_tag = [&]() {
    u32 epoch;
    asm volatile("s_load_dword %0, %1, 0x0\n\ts_waitcnt lgkmcnt(0)" : "=s"(epoch) : "s"(ws) : "memory");
    return epoch * 0x9E3779B1u + 0x7F4A7C15u;
  };
  const rsrc_t rw = __builtin_amdgcn_make_buffer_rsrc((void*)ws, 0, WS_END, 0x00020000);
  const int jp = b & 7, g = b >> 3;
  const int rt = (b & 7) * 4 + ((b >> 3) & 3), ct = b >> 5;
  auto wslot = [&](int s) { return (s < 3 ? 2 * G1_STAGE : s < 6 ? -3 * 16384 : G1_STAGE - 6 * 16384) + s * 16384; };
  u32x4 wf[7][4];
  const int wbase = hw * 4096 + t * 1024 + (p16 >> 2) * 128 + (p16 & 3) * 8 + ((((p16 >> 3) | ((t & 1) << 1))) << 5);
  auto wd_read = [&](int ks, int s) {
#pragma unroll
    for (int u = 0; u < 4; ++u) {
      LAS char* p = sm + wslot(s) + (wbase ^ (u << 5));
      u32x2 lo = tr_read(p), hi = tr_read(p + 512);
      wf[ks][u] = u32x4{lo[0], lo[1], hi[0], hi[1]};
    }
  };
  u32 tag;
  auto ksof = [&](int i) { int k = i + 2 * (rt & 3); return k >= 7 ? k - 7 : k; };
  const u32 hs = WS_H + ((rt * 4) * 56 + w * 7) * 1024 + lane * 16;
  auto ld_h = [&](int j, int i) { return __builtin_amdgcn_raw_buffer_load_b128(rw, hs, (((j + ct) & 3) * 56 + ksof(i)) * 1024, 16); };
  u32x4 hf[4][7];
  const u32x4 sd_ = {ka[10], ka[11], 1835008u, 0x00020000u};
  u32 vwd[4];
  auto wd_setup = [&]() {
#pragma unroll
    for (int j = 0; j < 4; ++j) { vwd[j] = (j * 7 + (lane >> 3)) * 1024 + ct * 128 + (((lane & 7) ^ (((lane >> 4) & 1) << 1) ^ ((j & 1) << 2)) << 4); PIN(vwd[j]); }
  };
  auto wd_issue = [&](int kw, int i, int s) {
    const u32 so = kw * (224 * 1024) + ksof(i) * 32768;
    SB(); glds_a(lw, wslot(s), vwd[0], vwd[1], sd_, so); glds_b(vwd[2], vwd[3], sd_, so); SB();
  };
  auto wait_h_and_load = [&]() {
    const u32 foff = WS_F2 + rt * 256 + (w * 7 + (lane < 7 ? lane : 0)) * 4;
#pragma nounroll
    for (int spin = 0; spin < SPIN_CAP; ++spin) {
      asm volatile("" ::: "memory");
      u32 fl = __builtin_amdgcn_raw_buffer_load_b32(rw, foff, 0, 16);
      if (all_ok(fl == tag)) break;
      __builtin_amdgcn_s_sleep(POLLSLEEP);
    }
    __builtin_amdgcn_fence(__ATOMIC_ACQUIRE, "wavefront");
    asm volatile("" ::: "memory");
    STAMP(15);
#pragma unroll
    for (int tt = 0; tt < 3; ++tt)
#pragma unroll
      for (int i = 0; i < 7; ++i) hf[tt][i] = ld_h(tt, i);
    SB();
  };
  const float pf = (float)p16, tf = (float)t;

  auto helper = [&](auto G_) {
    constexpr bool G = decltype(G_)::v;
    const int ht = tid & 255;
    const u32x4 sw = {ka[12], ka[13], WS_END, 0x00020000u};
    const u32x4 sfr_ = {ka[2], ka[3], 2048 * DF_ * 2, 0x00020000u}, sfi_ = {ka[4], ka[5], 2048 * DF_ * 2, 0x00020000u};
    SB();
#define DMA(st, k) do { if (G) { SB(); g1_issue(st, k); SB(); } } while (0)
#define GWAIT(n) do { if (G) WAITV(n); } while (0)
#define WAITG(n) do { if (G) WAITV(n); else WAITV(0); } while (0)
#define GBAR() do { if (G) RAWBAR(); } while (0)
    auto hsync = [&](int off, int stride = 0) {
      asm volatile("s_waitcnt lgkmcnt(0)" ::: "memory");
      *(volatile LAS u32*)(sm + off + (lane & 3) * stride + hw * 4) = tag;
#pragma nounroll
      for (int spin = 0; spin < SPIN_CAP; ++spin) { if (all_ok(*(volatile LAS u32*)(sm + off + hw * stride + (lane & 3) * 4) == tag)) break; }
      asm volatile("" ::: "memory");
    };
#define XBAR(n) do { if (G) BAR(); else hsync(G1_STAGE + (n) * 16); } while (0)
    u32x4 b2re[2], b2im[2], tw[2], ac2re, ac2im, bc3re, bc3im, qa8, qa8i, bc1re, bc1im, ac4re, ac4im, t1, bi1re[2], bi1im[2];
    u32x2 b1re, b1im;
    f32x4 t2c, t2s, t4c[2], t4s[2];
    {
      const float d5 = pf * (1.0f / 512), a5 = 4.0f * tf * d5;
#pragma unroll
      for (int r = 0; r < 4; ++r) {
        const float th = a5 + r * d5, c = cosr(th * 32.0f), s = sinr(th * 32.0f);
        ac2re[r] = pk_f16(c, s); ac2im[r] = rot16(ac2re[r]) ^ 0x8000u;
        bc3im[r] = pk_bf16(s, c); bc3re[r] = rot16(bc3im[r]) ^ 0x80000000u;
#pragma unroll
        for (int hh = 0; hh < 2; ++hh) {
          const float tq = th + hh * (16.0f * d5);
          tw[hh][r] = pk_f16(cosr(tq), sinr(tq));
          b2re[hh][r] = pk_f16(cosr(tq * 16.0f), sinr(tq * 16.0f)); b2im[hh][r] = rot16(b2re[hh][r]) ^ 0x8000u;
        }
      }
      b1re = u32x2{__builtin_amdgcn_perm(ac2re[1], ac2re[0], 0x05040100u), __builtin_amdgcn_perm(ac2re[3], ac2re[2], 0x05040100u)};
      b1im = u32x2{__builtin_amdgcn_perm(ac2im[1], ac2im[0], 0x05040100u), __builtin_amdgcn_perm(ac2im[3], ac2im[2], 0x05040100u)};
      const int po = p16 & 1, j8 = p16 >> 1;
      const float j8f = (float)j8 * 0.125f;
      const float ra = 2.0f * tf * j8f, rb = ra + j8f;
      const float ca = cosr(ra), sa = sinr(ra), cb = cosr(rb), sb = sinr(rb);
      const u32 fa0 = pk_f16(ca, sa), fb0 = pk_f16(cb, sb);
      const u32 fa = po ? (rot16(fa0) ^ 0x8000u) : fa0, fb = po ? (rot16(fb0) ^ 0x8000u) : fb0;
      const u32 sel = (j8 & 3) == 0 ? 0x03020100u : (j8 < 4 ? 0x0c0c0100u : 0x01000c0cu);
      qa8 = u32x4{fa, fb, __builtin_amdgcn_perm(0u, fa, sel), __builtin_amdgcn_perm(0u, fb, sel)};
      const u32 ia = pk_bf16(sa, ca), ib = pk_bf16(sb, cb), pa = rot16(ia) ^ 0x80000000u, pb = rot16(ib) ^ 0x80000000u;
      const bool on = (po == 0) ? (t < 2) : (t >= 2);
      const u32 hc = pk_bf16(0.5f * ca, 0.f);
      qa8i = u32x4{po ? ia : pa, po ? ib : pb, (t & 1) == 0 ? (po ? hc << 16 : hc) : (on ? pa : 0u), on ? pb : 0u};
      PIN(ac2re); PIN(ac2im); PIN(bc3re); PIN(bc3im); PIN(tw[0]); PIN(tw[1]); PIN(b2re[0]); PIN(b2re[1]); PIN(b2im[0]); PIN(b2im[1]); PIN(b1re); PIN(b1im); PIN(qa8); PIN(qa8i);
    }
    SB();
    tag = load_tag();
    STAMP(21);
    LAS char* const img = sm + FS + hw * 1024;
    u32 stat[2];
    WAITV(0);
    STAMP(1);
    {
      float v[2][8], mean[2], var[2];
#pragma unroll
      for (int a = 0; a < 2; ++a) {
        const u32x4 xv = *(LAS u32x4*)(img + a * 4096 + lane * 16);
#pragma unroll
        for (int k = 0; k < 4; ++k) { v[a][2 * k] = bf_lo(xv[k]); v[a][2 * k + 1] = bf_hi(xv[k]); }
      }
#pragma unroll
      for (int a = 0; a < 2; ++a) mean[a] = xrow_sum(row_sum(((v[a][0] + v[a][1]) + (v[a][2] + v[a][3])) + ((v[a][4] + v[a][5]) + (v[a][6] + v[a][7])))) * (1.0f / 512);
#pragma unroll
      for (int a = 0; a < 2; ++a) {
        float q0 = 0.f, q1 = 0.f;
#pragma unroll
        for (int k = 0; k < 4; ++k) { float d = v[a][k] - mean[a], e = v[a][k + 4] - mean[a]; q0 += d * d; q1 += e * e; }
        var[a] = xrow_sum(row_sum(q0 + q1)) * (1.0f / 512);
      }
#pragma unroll
      for (int a = 0; a < 2; ++a) {
        stat[a] = pk_bf16(mean[a], __builtin_amdgcn_sqrtf(rnd_bf(rnd_bf(var[a]) + 1e-5f)));
        const float sc = __builtin_amdgcn_rcpf(bf_hi(stat[a])) * XS, of = -bf_lo(stat[a]) * sc;
        u32x4 xh;
#pragma unroll
        for (int k = 0; k < 4; ++k) xh[k] = pk_f16(__builtin_fmaf(v[a][2 * k], sc, of), __builtin_fmaf(v[a][2 * k + 1], sc, of));
        *(LAS u32x4*)(img + a * 4096 + ((lane ^ ((t & 1) << 1)) << 4)) = xh;
      }
      asm volatile("" ::: "memory");
      STAMP(23);
      u32x2 tra[2][2];
#pragma unroll
      for (int a = 0; a < 2; ++a)
#pragma unroll
        for (int hh = 0; hh < 2; ++hh) tra[a][hh] = tr_read(img + a * 4096 + (4 * t + (p16 >> 2)) * 64 + ((hh ^ (t & 1)) << 5) + (p16 & 3) * 8);
      f32x4 d1re[2][2], d1im[2][2], d2re[2], d2im[2];
#pragma unroll
      for (int a = 0; a < 2; ++a)
#pragma unroll
        for (int hh = 0; hh < 2; ++hh) { d1re[a][hh] = mfma16_f16(tra[a][hh], b1re, z4); d1im[a][hh] = mfma16_f16(tra[a][hh], b1im, z4); }
#pragma unroll
      for (int a = 0; a < 2; ++a)
#pragma unroll
        for (int hh = 0; hh < 2; ++hh) {
          u32x4 ta = cmulc_f16(d1re[a][hh], d1im[a][hh], tw[hh]);
          d2re[a] = mfma32_f16(ta, b2re[hh], hh ? d2re[a] : z4); d2im[a] = mfma32_f16(ta, b2im[hh], hh ? d2im[a] : z4);
        }
      float ny[2];
#pragma unroll
      for (int a = 0; a < 2; ++a) { f32x4 q = d1re[a][0] + d1re[a][1]; ny[a] = xrow_sum((q[0] - q[1]) + (q[2] - q[3])); }
      int e = 4 * p16 + t; e ^= (e >> 3) & 3;
#pragma unroll
      for (int a = 0; a < 2; ++a) {
        if (lane == 0) d2im[a][0] = ny[a];
        *(LAS u32x4*)(img + a * 4096 + e * 16) = pack4<false>(d2re[a], d2im[a]);
      }
    }
    STAMP(24);
    hsync(2 * G1_STAGE, 4096);
    {
      u32x2 e0[2], e1[2];
#pragma unroll
      for (int k = 0; k < 2; ++k) {
        int e = 16 * hw + 8 * k + (p16 >> 1); e ^= (e >> 3) & 3;
        e0[k] = *(LAS u32x2*)(sm + FS + (2 * t) * 1024 + e * 16 + (p16 & 1) * 8);
        e1[k] = *(LAS u32x2*)(sm + FS + (2 * t + 1) * 1024 + e * 16 + (p16 & 1) * 8);
      }
      f32x4 d8[4];
#pragma unroll
      for (int u = 0; u < 4; ++u) d8[u] = mfma16_f16(u32x2{qa8[0], qa8[1]}, u32x2{e0[u >> 1][u & 1], e1[u >> 1][u & 1]}, z4);
      f32x4 d8s = mfma16_f16(u32x2{qa8[2], qa8[3]}, u32x2{e0[0][0], e1[0][0]}, z4);
      if ((hw | p16) == 0) d8[0] = d8s;
      const u32 ub = WS_U + b * 16384 + (2 * t) * 2048 + hw * 512 + p16 * 16;
#pragma unroll
      for (int hf = 0; hf < 2; ++hf)
#pragma unroll
        for (int k = 0; k < 2; ++k)
          st_sc1(rw, ub + hf * 2048 + k * 256, u32x4{pk_f16(d8[2 * k][2 * hf], d8[2 * k][2 * hf + 1]), tag, pk_f16(d8[2 * k + 1][2 * hf], d8[2 * k + 1][2 * hf + 1]), tag});
      if (lane < 2) st_sc1_2(rw, WS_ST + b * 64 + hw * 8 + lane * 32, u32x2{lane ? stat[1] : stat[0], tag});
    }
    STAMP(19);
#pragma unroll
    for (int k = 0; k < 6; ++k) DMA(2, k);
    {
      const u32 vo = (((8 * (8 * g + 2 * hw + (lane >> 5)) + jp) * DF_ * 2 + 14) & ~15u) + (lane & 31) * 16;
      glds16(lds0 + hw * 2048, FS + 8192, vo, sfr_, 0); glds16(lds0 + hw * 2048, FS + 8192 + 1024, vo, sfi_, 0);
    }
    STAMP(25);
    GBAR();
    STAMP(2);
    {
      const float jf = (float)jp;
      const float d1 = (8.0f * pf + jf) * (1.0f / 2048), a1 = 4.0f * tf * d1, d2 = pf * (1.0f / 256), a2 = (32.0f * tf + jf) * (pf * (1.0f / 2048));
      DMA(3, 0); DMA(3, 1);
#pragma unroll
      for (int r = 0; r < 4; ++r) {
        DMA(3, 2 + r);
        const float n1 = a1 + r * d1, n2 = a2 + r * d2;
        bc1re[r] = pk_f16(cosr(n1 * 16.0f), sinr(n1 * 16.0f)); bc1im[r] = rot16(bc1re[r]) ^ 0x8000u;
        t1[r] = pk_f16(cosr(n1), sinr(n1));
        ac4im[r] = pk_bf16(sinr(n2 * 16.0f), cosr(n2 * 16.0f)); ac4re[r] = rot16(ac4im[r]) ^ 0x80000000u;
        t2c[r] = cosr(n2); t2s[r] = sinr(n2);
      }
      PIN(bc1re); PIN(bc1im); PIN(ac4re); PIN(ac4im); PIN(t1); PIN(t2c); PIN(t2s);
    }
    u32x4 fdr, fdi, ug[4];
    const u32 ffo = ((8 * ht + jp) * DF_ + 8 * g) * 2;
    const int uq = ht & 3;
    const int ui = (((ht >> 2) & 3) << 4) + (ht >> 4);
    const u32 uoff = WS_U + ui * 16384 + jp * 2048 + g * 64 + uq * 16;
    auto caddr = [&](int i) { return (LAS char*)(sm + FS + uq * 2048 + ((((i & 192) + 4 * (i & 15) + ((i >> 4) & 3)) * 4) ^ (uq << 5))); };
    if (!G) {
#pragma nounroll
      for (int spin = 0; spin < SPIN_CAP; ++spin) {
        asm volatile("" ::: "memory");
        u32x2 q = ld_sc1_2(rw, WS_ST + (b + 224) * 64 + hw * 8);
        if (all_ok(q[1] == tag)) break;
        __builtin_amdgcn_s_sleep(8);
      }
      __builtin_amdgcn_s_sleep(20);
    }
    SB();
    ALD4(fdr, ffo, sfr_, 0, "");
    STAMP(26);
    GWAIT(2 + 12 + 1 + WARM);
    GBAR();
    STAMP(3);
#pragma unroll
    for (int k = 0; k < 6; ++k) DMA(4, k);
#pragma unroll
    for (int k = 0; k < 4; ++k) ALD4S(ug[k], uoff, sw, k * 64 * 16384, " sc1");
    WAITG(1 + WARM + 12 + 4);
    XBAR(2);
    STAMP(4);
    ALD4(fdi, ffo, sfi_, 0, "");
#pragma unroll
    for (int k = 0; k < 6; ++k) DMA(5, k);
    STAMP(27);
    GWAIT(1 + 12);
    PIN(ug[0]); PIN(ug[1]); PIN(ug[2]); PIN(ug[3]);
#define CHK1 all_ok(tags_ok(ug[0], tag) & tags_ok(ug[1], tag) & tags_ok(ug[2], tag) & tags_ok(ug[3], tag))
    const bool uok = CHK1;
    {
      LAS char* cp = caddr(ui);
#pragma unroll
      for (int k = 0; k < 4; ++k) { *(LAS u32*)(cp + k * 256) = ug[k][0]; *(LAS u32*)(cp + k * 256 + 1024) = ug[k][2]; }
    }
    WAITG(12);
    PIN(fdr); PIN(fdi);
    {
      LAS char* fp = sm + FS + 8192 + ((ht & 192) + 4 * (ht & 15) + ((ht >> 4) & 3)) * 4;
#pragma unroll
      for (int k = 0; k < 4; ++k) {
        *(LAS u32*)(fp + (2 * k) * 1024) = __builtin_amdgcn_perm(fdi[k], fdr[k], 0x05040100u);
        *(LAS u32*)(fp + (2 * k + 1) * 1024) = __builtin_amdgcn_perm(fdi[k], fdr[k], 0x07060302u);
      }
    }
    if (__builtin_expect(!uok, 0)) {
#pragma nounroll
      for (int spin = 0; spin < SPIN_CAP; ++spin) {
        asm volatile("" ::: "memory");
#pragma unroll
        for (int k = 0; k < 4; ++k) ug[k] = ld_sc1(rw, uoff + k * 64 * 16384);
        if (CHK1) break;
      }
      LAS char* cp = caddr(ui);
#pragma unroll
      for (int k = 0; k < 4; ++k) { *(LAS u32*)(cp + k * 256) = ug[k][0]; *(LAS u32*)(cp + k * 256 + 1024) = ug[k][2]; }
    }
    u32x4 fsp[2];
    const bool special = !G && b < 8 && hw == 0;
    if (special) {
#pragma nounroll
      for (int spin = 0; spin < SPIN_CAP; ++spin) {
        asm volatile("" ::: "memory");
        bool ok = true;
#pragma unroll
        for (int c = 0; c < 2; ++c) {
          const u32x4 fa = ld_sc1(rw, WS_SF + jp * 4096 + c * 2048 + lane * 32), fb = ld_sc1(rw, WS_SF + jp * 4096 + c * 2048 + lane * 32 + 16);
          ok &= tags_ok(fa, tag) & tags_ok(fb, tag);
          fsp[c] = u32x4{fa[0], fa[2], fb[0], fb[2]};
        }
        if (all_ok(ok)) break;
      }
    }
    STAMP(28);
    XBAR(3);
    STAMP(5);
    LAS char* const colp = sm + FS + hw * 2048 + ((lane * 16) ^ (hw << 5));
    {
      u32x4 uin[2], fin[2];
#pragma unroll
      for (int a = 0; a < 2; ++a) { uin[a] = *(LAS u32x4*)(colp + a * 1024); fin[a] = *(LAS u32x4*)(sm + FS + 8192 + (2 * hw + a) * 1024 + lane * 16); }
      const bool two = special && (jp & 3) == 0;
      f32x4 dre[2], dim[2], keep;
      auto col_xform = [&](auto NA_) {
        constexpr int NA = decltype(NA_)::v;
        u32x4 ta[2];
        DMA(6, 0);
#pragma unroll
        for (int a = 0; a < NA; ++a) { dre[a] = mfma32_f16(uin[a], bc1re, z4); dim[a] = mfma32_f16(uin[a], bc1im, z4); }
        DMA(6, 1);
#pragma unroll
        for (int a = 0; a < NA; ++a) { ta[a] = cmulc_f16(dre[a], dim[a], t1); dre[a] = mfma32_f16(ac2re, ta[a], z4); dim[a] = mfma32_f16(ac2im, ta[a], z4); }
        DMA(6, 2);
#pragma unroll
        for (int a = 0; a < NA; ++a) {
          f32x4 fr_, fi_;
#pragma unroll
          for (int r = 0; r < 4; ++r) { fr_[r] = bf_lo(fin[a][r]); fi_[r] = bf_hi(fin[a][r]); }
          ta[a] = cmul_pack<false, true>(dre[a], dim[a], fr_, fi_); dre[a] = mfma32_bf16(ta[a], bc3re, z4); dim[a] = mfma32_bf16(ta[a], bc3im, z4);
          DMA(6, 3 + a);
        }
#pragma unroll
        for (int a = 0; a < NA; ++a) {
          ta[a] = cmul_pack<false, true>(dre[a], dim[a], t2c, t2s); dre[a] = mfma32_bf16(ac4re, ta[a], z4); dim[a] = mfma32_bf16(ac4im, ta[a], z4);
          if (a == 0) DMA(6, 5);
        }
      };
      if (!G) {
        if (special) {
          fin[0] = jp > 4 ? fsp[1] : fsp[0];
          if (two) {
            const u32x4 us = uin[0];
            uin[0] = us >> 16; fin[0] = fsp[1];
            col_xform(IC<1>{});
            keep = dre[0];
            uin[0] = us & 0xffffu; fin[0] = fsp[0];
          }
        }
      }
      col_xform(IC<2>{});
      if (!G) { if (two) dim[0] = keep; }
#pragma unroll
      for (int a = 0; a < 2; ++a) *(LAS u32x4*)(colp + a * 1024) = pack4<true>(dre[a], dim[a]);
      {
        const u32 vo = b * 7168 + hw * 2048 + lane * 16;
        glds_a(lds0 + hw * 2048, FS + 8192, vo, (hw < 3 ? vo : 0x7fffffffu), sd_, 0);
      }
#define NW 2
    }
    STAMP(29);
    GWAIT(12 + NW);
    XBAR(4);
    STAMP(6);
    {
      const int ps = ht >> 2;
      u32 vo[4][2];
#pragma unroll
      for (int k = 0; k < 4; ++k) { LAS char* cp = caddr((ps >> 5) + 2 * k + 8 * (ps & 31)); vo[k][0] = *(LAS u32*)cp; vo[k][1] = *(LAS u32*)(cp + 1024); }
#pragma unroll
      for (int k = 0; k < 6; ++k) DMA(7, k);
#pragma unroll
      for (int k = 0; k < 4; ++k) st_sc1(rw, WS_V + b * 16384 + k * 4096 + ht * 16, u32x4{vo[k][0], tag, vo[k][1], tag});
    }
    STAMP(20);
    GWAIT(NW + 12 + 4);
    XBAR(5);
    STAMP(7);
    wd_setup();
    u32x2 csv[4], stv;
    const u32 csoff = WS_CS + ((t & 1) * F_ + fj * 128 + hw * 32 + (lane >> 5) * 16 + ((p16 >> 2) & 1) * 8 + (p16 >> 3) * 4 + (p16 & 3)) * 8;
    const u32 stoff = WS_ST + (((mi & 1) * 128 + hw * 32 + (lane & 31)) * 8 + (mi >> 1)) * 8;
    if (G) {
      ALD2S(csv[0], csoff, sw, 0, " sc1"); ALD2S(csv[1], csoff, sw, 2 * F_ * 8, " sc1"); ALD2S(csv[2], csoff, sw, 4 * F_ * 8, " sc1"); ALD2S(csv[3], csoff, sw, 6 * F_ * 8, " sc1");
      ALD2(stv, stoff, sw, 0, " sc1");
    }
#pragma unroll
    for (int hh = 0; hh < 2; ++hh) {
      wd_issue(w, hh, hh);
      const float n2 = (float)(8 * (p16 >> 2) + (p16 & 3)) + 4.0f * hh;
#pragma unroll
      for (int m = 0; m < 4; ++m) {
        float rev = n2 * ((tf + 4.0f * m) * (1.0f / 32));
        bi1im[hh][m] = pk_bf16(sinr(rev), cosr(rev)); bi1re[hh][m] = rot16(bi1im[hh][m]) ^ 0x80000000u;
        float rev2 = n2 * ((4.0f * tf + m) * (1.0f / 512));
        t4c[hh][m] = cosr(rev2); t4s[hh][m] = sinr(rev2);
      }
      PIN(bi1re[hh]); PIN(bi1im[hh]); PIN(t4c[hh]); PIN(t4s[hh]);
    }
    wd_issue(w, 2, 2);
    if (G) {
      WAITV(12);
      PIN(csv[0]); PIN(csv[1]); PIN(csv[2]); PIN(csv[3]); PIN(stv);
#define CHK3 all_ok((stv[1] == tag) & (csv[0][1] == tag) & (csv[1][1] == tag) & (csv[2][1] == tag) & (csv[3][1] == tag))
      if (__builtin_expect(!CHK3, 0)) {
#pragma nounroll
        for (int spin = 0; spin < SPIN_CAP; ++spin) {
          asm volatile("" ::: "memory");
#pragma unroll
          for (int k = 0; k < 4; ++k) csv[k] = ld_sc1_2(rw, csoff + k * 2 * F_ * 8);
          stv = ld_sc1_2(rw, stoff);
          if (CHK3) break;
        }
      }
      const u32 c0 = csv[0][0], c1 = csv[1][0], c2 = csv[2][0], c3 = csv[3][0];
      const float cs = (__builtin_bit_cast(float, c0) + __builtin_bit_cast(float, c1)) + (__builtin_bit_cast(float, c2) + __builtin_bit_cast(float, c3));
      const float ch = rnd_bf(cs);
      LAS char* cp = sm + FS + hw * 2048 + lane * 4;
      *(LAS u32*)cp = pk_bf16(ch, cs - ch);
      *(LAS u32*)(cp + 256) = __builtin_amdgcn_perm(0u, stv[0], 0x01000100u) ^ 0x80008000u;
    }
    u32x4 xr[2], rv[2][2];
    const u32 voff = WS_V + ((b & 7) * 32 + (b >> 3)) * 64 + (p16 & 3) * 16 + ((8 * hw + (p16 >> 2)) * 8 + 2 * t) * 16384;
    if (!G) {
      WAITV(12);
#pragma nounroll
      for (int spin = 0; spin < SPIN_CAP; ++spin) {
        asm volatile("" ::: "memory");
        u32x2 q = ld_sc1_2(rw, WS_V + (b + 224) * 16384 + hw * 4096);
        if (all_ok(q[1] == tag)) break;
        __builtin_amdgcn_s_sleep(8);
      }
      __builtin_amdgcn_s_sleep(20);
    }
    XBAR(6);
    STAMP(8);
    wd_issue(hw, 0, 3); wd_issue(hw, 1, 4); wd_issue(hw, 2, 5);
    auto mwait = [&](int off) {
      if (G) {
#pragma nounroll
        for (int spin = 0; spin < SPIN_CAP; ++spin) { if (all_ok(*(volatile LAS u32*)(sm + off + (lane & 3) * 4) == tag)) break; __builtin_amdgcn_s_sleep(1); }
        asm volatile("" ::: "memory");
      }
    };
    WAITV(12);
    wd_read(0, 0); wd_read(1, 1); wd_read(2, 2);
    asm volatile("s_waitcnt lgkmcnt(0)" ::: "memory");
    STAMP(9);
    mwait(FS + 1056);
    {
      const u32 xo = (b + 256 * hw) * 1024 + (4 * p16 + t) * 16;
      ALD4S(rv[0][0], voff, sw, 0, " sc1"); ALD4S(rv[0][1], voff, sw, 16384, " sc1"); ALD4S(rv[1][0], voff, sw, 32 * 16384, " sc1"); ALD4S(rv[1][1], voff, sw, 33 * 16384, " sc1");
      ALD4S(xr[0], xo, sx, 0, ""); ALD4S(xr[1], xo, sx, 1048576, "");
    }
    wd_issue(w, 3, 0); wd_issue(w, 4, 1); wd_issue(w, 5, 2);
    WAITV(18);
    RAWBAR();
    WAITV(12);
    PIN(rv[0][0]); PIN(rv[0][1]); PIN(rv[1][0]); PIN(rv[1][1]); PIN(xr[0]); PIN(xr[1]);
#define CHK4 all_ok(tags_ok(rv[0][0], tag) & tags_ok(rv[0][1], tag) & tags_ok(rv[1][0], tag) & tags_ok(rv[1][1], tag))
    if (__builtin_expect(!CHK4, 0)) {
#pragma nounroll
      for (int spin = 0; spin < SPIN_CAP; ++spin) {
        asm volatile("" ::: "memory");
#pragma unroll
        for (int k = 0; k < 2; ++k) { rv[k][0] = ld_sc1(rw, voff + k * 32 * 16384); rv[k][1] = ld_sc1(rw, voff + k * 32 * 16384 + 16384); }
        if (CHK4) break;
        __builtin_amdgcn_s_sleep(4);
      }
    }
    STAMP(12);
    LAS char* const yb = sm + FS + 8192;
#pragma unroll
    for (int k = 0; k < 2; ++k) {
      f32x4 da = mfma16_bf16(u32x2{qa8i[0], qa8i[1]}, u32x2{rv[k][0][0], rv[k][1][0]}, z4), db = mfma16_bf16(u32x2{qa8i[0], qa8i[1]}, u32x2{rv[k][0][2], rv[k][1][2]}, z4);
      if (k == 0) { f32x4 ds = mfma16_bf16(u32x2{qa8i[2], qa8i[3]}, u32x2{rv[0][0][0], rv[0][1][0]}, z4); if ((hw | p16) == 0) da = ds; }
      *(LAS u32x2*)(yb + (2 * t) * 1024 + (32 * hw + 16 * k + p16) * 8) = u32x2{pk_bf16(da[0], da[1]), pk_bf16(db[0], db[1])};
      *(LAS u32x2*)(yb + (2 * t + 1) * 1024 + (32 * hw + 16 * k + p16) * 8) = u32x2{pk_bf16(da[2], da[3]), pk_bf16(db[2], db[3])};
    }
    hsync(FS + 1024);
    STAMP(13);
    u32x4 ro[2][2];
#pragma unroll
    for (int a = 0; a < 2; ++a) {
      u32x4 av;
#pragma unroll
      for (int m = 0; m < 4; ++m) av[m] = *(LAS u32*)(yb + (hw + 4 * a) * 1024 + (lane + 64 * m) * 4);
      const float nyq = bf_hi(*(LAS u32*)(yb + (hw + 4 * a) * 1024));
#pragma unroll
      for (int hh = 0; hh < 2; ++hh) {
        f32x4 gre = mfma32_bf16(av, bi1re[hh], z4), gim = mfma32_bf16(av, bi1im[hh], z4);
        u32x4 bb = cmul_pack<false, true>(gre, gim, t4c[hh], t4s[hh]);
        f32x4 d = mfma32_bf16(bb, bc3re, f32x4{nyq, -nyq, nyq, -nyq});
        ro[a][hh] = u32x4{pk_bf16(__builtin_fmaf(d[0], FSC, bf_lo(xr[a][2 * hh])), __builtin_fmaf(d[1], FSC, bf_hi(xr[a][2 * hh]))), tag,
                          pk_bf16(__builtin_fmaf(d[2], FSC, bf_lo(xr[a][2 * hh + 1])), __builtin_fmaf(d[3], FSC, bf_hi(xr[a][2 * hh + 1]))), tag};
        PIN(ro[a][hh]);
      }
    }
    STAMP(14);
    WAITV(8);
    wd_read(3, 0);
    asm volatile("s_waitcnt lgkmcnt(0)" ::: "memory");
    STAMP(10);
    wd_issue(w, 6, 0);
#pragma unroll
    for (int a = 0; a < 2; ++a)
#pragma unroll
      for (int hh = 0; hh < 2; ++hh) st_sc1(rw, WS_R + (b + 256 * hw + 1024 * a) * 2048 + hh * 1024 + (4 * p16 + t) * 16, ro[a][hh]);
    STAMP(11);
  };

  if (w < 4) {
    if (is_gemm) {
      tag = load_tag();
      const int h = lane >> 5;
      int xoff[4], woff[2];
#pragma unroll
      for (int ks = 0; ks < 4; ++ks) { int tok = lane & 31; xoff[ks] = tok * 128 + (((2 * ks + h) ^ ((tok >> 1) & 7)) << 4); PIN(xoff[ks]); }
      {
        int arr = t & 1, q = p16 >> 2, p = lane & 3;
#pragma unroll
        for (int m = 0; m < 2; ++m) {
          woff[m] = 16384 + arr * 16384 + (8 * h + q) * 256 + (((hw * 2 + m) ^ (q | (arr << 2))) << 5) + (p & 1) * 16 + (p >> 1) * 8;
          PIN(woff[m]);
        }
      }
      auto rdx = [&](Frag& f, int st, int ks, int n) { f.bx[n] = *(LAS u32x4*)(sm + (st % 3) * G1_STAGE + xoff[ks] + n * 4096); };
      auto rdw = [&](Frag& f, int st, int ks, int m) {
        LAS char* buf = sm + (st % 3) * G1_STAGE;
        u32x2 lo = tr_read(buf + woff[m] + ks * 4096), hi = tr_read(buf + woff[m] + ks * 4096 + 1024);
        f.aw[m] = u32x4{lo[0], lo[1], hi[0], hi[1]};
      };
      f32x16 acc[2][4];
#pragma unroll
      for (int m = 0; m < 2; ++m)
#pragma unroll
        for (int n = 0; n < 4; ++n)
#pragma unroll
          for (int k = 0; k < 16; ++k) acc[m][n][k] = 0.f;
      Frag fr[2];
      u32 qa[2], qb[4];
      SB();
      STAMP(21);
      g1_issue(1, 0); g1_issue(1, 1);
      WAITV(4);
      {
        LAS char* mb = sm + G1_STAGE + 32768 + 3072;
        *(volatile LAS u32*)(mb + (lane & 3) * 4096 + hw * 4) = tag;
#pragma nounroll
        for (int spin = 0; spin < SPIN_CAP; ++spin) { if (all_ok(*(volatile LAS u32*)(mb + hw * 4096 + (lane & 3) * 4) == tag)) break; }
        asm volatile("" ::: "memory");
      }
      STAMP(1);
      rdw(fr[0], 0, 0, 0); rdx(fr[0], 0, 0, 0); rdx(fr[0], 0, 0, 1); rdx(fr[0], 0, 0, 2); rdx(fr[0], 0, 0, 3); rdw(fr[0], 0, 0, 1);
      static_for<0, 30>([&](auto KS_) {
        constexpr int KS = decltype(KS_)::v, st = KS >> 2, ks = KS & 3;
        constexpr int nst = ks < 3 ? st : st + 1, nks = (ks + 1) & 3;
        Frag& f = fr[KS & 1]; Frag& g = fr[(KS + 1) & 1];
        SB();
        if constexpr (ks == 3) { if constexpr (st == 0) WAITV(0); BAR(); STAMP(2 + st); SB(); }
        acc[0][0] = mfma3232(f.aw[0], f.bx[0], acc[0][0]); SB(); rdw(g, nst, nks, 0); SB();
        acc[0][1] = mfma3232(f.aw[0], f.bx[1], acc[0][1]); SB(); rdx(g, nst, nks, 0); SB();
        if constexpr (KS == 0 && S1PRE != 6) { g1_issue(1, 2); SB(); }
        if constexpr (KS == 1 && S1PRE != 6) { g1_issue(1, 5); SB(); }
        acc[0][2] = mfma3232(f.aw[0], f.bx[2], acc[0][2]); SB(); rdx(g, nst, nks, 1); SB();
        acc[0][3] = mfma3232(f.aw[0], f.bx[3], acc[0][3]); SB(); rdx(g, nst, nks, 2); SB();
        if constexpr (KS == 0 && S1PRE != 6) { g1_issue(1, 3); SB(); }
        acc[1][3] = mfma3232(f.aw[1], f.bx[3], acc[1][3]); SB(); rdx(g, nst, nks, 3); SB();
        acc[1][2] = mfma3232(f.aw[1], f.bx[2], acc[1][2]); SB(); rdw(g, nst, nks, 1); SB();
        acc[1][1] = mfma3232(f.aw[1], f.bx[1], acc[1][1]); SB();
        if constexpr (KS == 0 && S1PRE != 6) { g1_issue(1, 4); SB(); }
        if constexpr (KS == 27) {
          LAS char* cp = sm + FS + (lane & 31) * 4;
          qa[0] = *(LAS u32*)(cp + hw * 2048); qa[1] = *(LAS u32*)(cp + hw * 2048 + 128);
#pragma unroll
          for (int n = 0; n < 4; ++n) qb[n] = *(LAS u32*)(cp + n * 2048 + 256);
          SB();
        }
        acc[1][0] = mfma3232(f.aw[1], f.bx[0], acc[1][0]); SB();
      });
      rdw(fr[1], 7, 3, 0); rdx(fr[1], 7, 3, 0); rdw(fr[1], 7, 3, 1); rdx(fr[1], 7, 3, 1); rdx(fr[1], 7, 3, 2); rdx(fr[1], 7, 3, 3);
      __builtin_amdgcn_s_setprio(3);
      STAMP(9);
      u32x4 ca[2] = {u32x4{h ? 0u : qa[0], 0u, 0u, 0u}, u32x4{h ? 0u : qa[1], 0u, 0u, 0u}}, cb = {0u, 0u, 0u, 0u};
      const u32 hoff = WS_H + ((mi * 8 + (t & 1)) * 56 + fj * 4 + hw) * 1024 + h * 256 + p16 * 16;
      float ev[8];
      auto epi_a = [&](int q) {
#pragma unroll
        for (int k = 0; k < 8; ++k) ev[k] = relu(acc[q & 1][q >> 1][k]);
      };
      auto epi_b = [&](int q) {
#pragma unroll
        for (int k = 0; k < 8; ++k) ev[k] *= acc[q & 1][q >> 1][8 + k];
      };
      auto epi_c = [&](int q) {
        __builtin_amdgcn_raw_buffer_store_b128(u32x4{pk_bf16(ev[0], ev[1]), pk_bf16(ev[2], ev[3]), pk_bf16(ev[4], ev[5]), pk_bf16(ev[6], ev[7])}, rw,
                                               hoff + (q & 1) * 512, (q >> 1) * (2 * 56 * 1024), 16);
      };
#pragma unroll
      for (int q = 0; q < 8; ++q) {
        const int m = q & 1, n = q >> 1;
        SB();
        if (m == 0) cb[0] = qb[n];
        acc[m][n] = mfma3232(fr[0].aw[m], fr[0].bx[n], acc[m][n]); SB();
        if (q >= 2) { epi_c(q - 2); SB(); }
        acc[m][n] = mfma3232(ca[m], cb, acc[m][n]); SB();
        if (q >= 1) { epi_a(q - 1); SB(); }
        acc[m][n] = mfma3232(fr[1].aw[m], fr[1].bx[n], acc[m][n]); SB();
        if (q >= 1) { epi_b(q - 1); SB(); }
      }
      epi_c(6); SB(); epi_a(7); epi_b(7); epi_c(7);
      *(volatile LAS u32*)(sm + FS + 1056 + hw * 4) = tag;
      STAMP(10);
      wd_setup();
      WAITV(4);
      if (lane == 0) __builtin_amdgcn_raw_buffer_store_b32(tag, rw, WS_F2 + (mi * 2) * 256 + (fj * 4 + hw) * 4, 0, 16);
      WAITV(1);
      if (lane == 0) __builtin_amdgcn_raw_buffer_store_b32(tag, rw, WS_F2 + (mi * 2 + 1) * 256 + (fj * 4 + hw) * 4, 0, 16);
      STAMP(12);
      __builtin_amdgcn_s_setprio(HPRIO);
    } else {
      wd_setup();
      const int un = b >> 1, ua = un >= 7, uc = (b & 1) * 7 + (ua ? un - 7 : un);
      const u32x4 scs = {ua ? ka[8] : ka[6], ua ? ka[9] : ka[7], b < 28 ? 1835008u : 0u, 0x00020000u};
      const u32 vcs = (hw * 4 + t) * 3584 + uc * 256 + p16 * 16;
      u32x4 vv[16];
      f32x4 a0 = z4, a1 = z4;
      u32 sfa, sfb;
      u32 sel_lo = 0x00003f80u, sel_hi = 0x3f800000u;
      PIN(sel_lo); PIN(sel_hi);
      const unsigned long long t_start = __builtin_amdgcn_s_memrealtime();
#pragma unroll
      for (int half = 0; half < 2; ++half) {
        if (half == 1) { while (__builtin_amdgcn_s_memrealtime() - t_start < CS_DELAY) __builtin_amdgcn_s_sleep(4); }
#pragma unroll
        for (int k = 0; k < 16; ++k) ALD4S(vv[k], vcs, scs, (half * 16 + k) * 16 * 3584, "");
        if (half == 0) {
          const int q = 64 * g + 16 * hw + p16, ks = 8 * q + jp, km = (2048 - ks) & 2047, col = (t >> 1) * 256;
          const GAS unsigned short* wa = (t & 1) ? wi : wr;
          asm volatile("global_load_ushort %0, %1, off" : "=v"(sfa) : "v"(wa + ks * DF_ + col) : "memory");
          asm volatile("global_load_ushort %0, %1, off" : "=v"(sfb) : "v"(wa + km * DF_ + col) : "memory");
          tag = load_tag();
        }
        STAMP(23 + 3 * half);
        static_for<0, 4>([&](auto Q_) {
          constexpr int Q = decltype(Q_)::v;
          if (half == 0) WAITV(14 - 4 * Q); else WAITV(12 - 4 * Q);
#pragma unroll
          for (int k = 4 * Q; k < 4 * Q + 4; ++k) {
            PIN(vv[k]);
#pragma unroll
            for (int e = 0; e < 2; ++e) {
              const u32 p = vv[k][e], q = vv[k][2 + e];
              a0[2 * e] = __builtin_amdgcn_fdot2_f32_bf16(__builtin_bit_cast(b16x2, p), __builtin_bit_cast(b16x2, sel_lo), a0[2 * e], false);
              a0[2 * e + 1] = __builtin_amdgcn_fdot2_f32_bf16(__builtin_bit_cast(b16x2, p), __builtin_bit_cast(b16x2, sel_hi), a0[2 * e + 1], false);
              a1[2 * e] = __builtin_amdgcn_fdot2_f32_bf16(__builtin_bit_cast(b16x2, q), __builtin_bit_cast(b16x2, sel_lo), a1[2 * e], false);
              a1[2 * e + 1] = __builtin_amdgcn_fdot2_f32_bf16(__builtin_bit_cast(b16x2, q), __builtin_bit_cast(b16x2, sel_hi), a1[2 * e + 1], false);
            }
          }
        });
        PIN(a0); PIN(a1);
        STAMP(24 + 3 * half);
        if (half == 0) {
          WAITV(0);
          PIN(sfa); PIN(sfb);
          const float wh = 0.5f * ((t & 1) ? bf_lo(sfa) - bf_lo(sfb) : bf_lo(sfa) + bf_lo(sfb));
          const u32 wb = pk_bf16(wh, wh) & 0xffffu;
          auto s2 = __builtin_amdgcn_permlane16_swap(wb, wb, false, false);
          if ((t & 1) == 0) st_sc1_2(rw, WS_SF + jp * 4096 + (t >> 1) * 2048 + (64 * g + 4 * p16 + hw) * 8, u32x2{(u32)s2[0] | ((u32)s2[1] << 16), tag});
          STAMP(25);
        }
      }
      if (b < 28) {
#pragma unroll
        for (int k = 0; k < 4; ++k) { a0[k] = xrow_sum(a0[k]); a1[k] = xrow_sum(a1[k]); }
        const float s0 = t == 0 ? a0[0] : t == 1 ? a0[2] : t == 2 ? a1[0] : a1[2];
        const float s1 = t == 0 ? a0[1] : t == 1 ? a0[3] : t == 2 ? a1[1] : a1[3];
        st_sc1(rw, WS_CS + hw * (2 * F_ * 8) + (ua * 14 + uc) * 1024 + p16 * 64 + t * 16, u32x4{__builtin_bit_cast(u32, s0), tag, __builtin_bit_cast(u32, s1), tag});
      }
      STAMP(3);
    }
    RAWBAR();
    wd_issue(w, 3, 6); wd_issue(w, 4, 7); wd_issue(w, 5, 8);
    wd_read(0, 3); wd_read(1, 4); wd_read(2, 5);
    asm volatile("s_waitcnt lgkmcnt(0)" ::: "memory");
    wd_issue(w, 6, 3);
    WAITV(12);
    wd_read(3, 6);
    STAMP(13);
  } else if (is_gemm) helper(IC<1>{});
  else helper(IC<0>{});

  wait_h_and_load();
  __builtin_amdgcn_s_setprio(0);
#pragma unroll
  for (int i = 4; i < 7; ++i) {
    LAS char* p0 = sm + (w < 4 ? wslot(i == 6 ? 3 : i + 3) : wslot(i == 6 ? 0 : i - 3));
#pragma unroll
    for (int u = 0; u < 4; ++u) {
      LAS char* p = p0 + (wbase ^ (u << 5));
      u32x2 lo = tr_read(p), hi = tr_read(p + 512);
      wf[i][u] = u32x4{lo[0], lo[1], hi[0], hi[1]};
    }
  }
  STAMP(16);
  const int oc = lane & 7, otok = (w & 1) * 8 + (lane >> 3);
  const int orow = rt * 64 + (((w >> 1) + ct) & 3) * 16 + otok;
  const u32 roff = WS_R + orow * 2048 + (ct * 8 + oc) * 16;
  const u32 soff_ = WS_ST + ((orow & 255) * 8 + (orow >> 8)) * 8;
  u32x4 r0, r1; u32x2 sto;
#pragma unroll
  for (int tt = 0; tt < 4; ++tt) {
    f32x4 acc[4] = {z4, z4, z4, z4};
#pragma unroll
    for (int ks = 0; ks < 7; ++ks) {
#pragma unroll
      for (int u = 0; u < 4; ++u) acc[u] = mfma32_bf16(wf[ks][u], hf[tt][ks], acc[u]);
      if (tt == 0) { SB(); hf[3][ks] = ld_h(3, ks); SB(); }
    }
    if (tt == 0 && w < 4) *(volatile LAS u32*)(sm + FS + 1040 + hw * 4) = tag;
    if (tt == 3 && w >= 4) {
#pragma nounroll
      for (int spin = 0; spin < SPIN_CAP; ++spin) { if (all_ok(*(volatile LAS u32*)(sm + FS + 1040 + hw * 4) == tag)) break; }
      asm volatile("" ::: "memory");
    }
    if (tt == 2) { r0 = ld_sc1(rw, roff); r1 = ld_sc1(rw, roff + 1024); sto = ld_sc1_2(rw, soff_); }
#pragma unroll
    for (int u = 0; u < 4; ++u) *(LAS f32x4*)(sm + (w < 4 ? wslot(3 + tt) : wslot(tt < 3 ? tt : 8)) + hw * 4096 + u * 1024 + t * 256 + ((p16 ^ (4 * u + (t & 2))) << 4)) = acc[u];
    SB();
  }
  STAMP(17);
  BAR();
  f32x4 sa = z4, sb = z4;
  {
    const int tt = w >> 1;
    const int o0 = (oc >> 1) * 1024 + (oc & 1) * 512 + ((otok ^ (2 * oc)) << 4);
    const int bm = (tt < 3 ? wslot(3) + tt * 16384 : wslot(6)) + o0, bh = (tt < 3 ? wslot(0) + tt * 16384 : wslot(8)) + o0;
#pragma unroll
    for (int pw = 0; pw < 4; ++pw) {
      sa += *(LAS f32x4*)(sm + bm + pw * 4096) + *(LAS f32x4*)(sm + bh + pw * 4096);
      sb += *(LAS f32x4*)(sm + bm + 256 + pw * 4096) + *(LAS f32x4*)(sm + bh + 256 + pw * 4096);
    }
  }
  STAMP(22);
#define CHK2 all_ok(tags_ok(r0, tag) & tags_ok(r1, tag) & (sto[1] == tag))
  if (__builtin_expect(!CHK2, 0)) {
#pragma nounroll
    for (int spin = 0; spin < SPIN_CAP; ++spin) {
      asm volatile("" ::: "memory");
      r0 = ld_sc1(rw, roff); r1 = ld_sc1(rw, roff + 1024); sto = ld_sc1_2(rw, soff_);
      if (CHK2) break;
    }
  }
  {
    const float rs = __builtin_amdgcn_rcpf(bf_hi(sto[0])), rs2 = rs * rs;
    *(GAS u32x4*)(out + (size_t)orow * 1024 + ct * 128 + oc * 16) =
        u32x4{pk_bf16(__builtin_fmaf(sa[0], rs2, bf_lo(r0[0])), __builtin_fmaf(sa[1], rs2, bf_hi(r0[0]))),
              pk_bf16(__builtin_fmaf(sa[2], rs2, bf_lo(r0[2])), __builtin_fmaf(sa[3], rs2, bf_hi(r0[2]))),
              pk_bf16(__builtin_fmaf(sb[0], rs2, bf_lo(r1[0])), __builtin_fmaf(sb[1], rs2, bf_hi(r1[0]))),
              pk_bf16(__builtin_fmaf(sb[2], rs2, bf_lo(r1[2])), __builtin_fmaf(sb[3], rs2, bf_hi(r1[2])))};
  }
  if (b == 0 && tid == 0) __builtin_amdgcn_raw_buffer_store_b32(tag, rw, WS_EPOCH, 0, 16);
  STAMP(18);
  STAMP_DUMP();
}

#if defined(__HIP_DEVICE_COMPILE__)
#pragma clang attribute pop
#endif

extern "C" void kernel_launch(void* const* d_in, const int* in_sizes, int n_in, void* d_out, int out_size, void* d_ws, size_t ws_size,
                              hipStream_t stream) {
  (void)hipFuncSetAttribute((const void*)fused_kernel, hipFuncAttributeMaxDynamicSharedMemorySize, LDS_BYTES);
  fused_kernel<<<256, 512, LDS_BYTES, stream>>>((const char*)d_in[0], (const unsigned short*)d_in[1], (const unsigned short*)d_in[2], (const char*)d_in[4],
                                                (const char*)d_in[5], (const char*)d_in[6], (char*)d_ws, (char*)d_out);
}
```

```cpp
#include <hip/hip_runtime.h>
#include <stdint.h>

#define LAS __attribute__((address_space(3)))
#define GAS __attribute__((address_space(1)))
#define DI __device__ __forceinline__
typedef unsigned u32;
typedef unsigned u32x2 __attribute__((ext_vector_type(2)));
typedef unsigned u32x4 __attribute__((ext_vector_type(4)));
typedef float f32x2 __attribute__((ext_vector_type(2)));
typedef float f32x4 __attribute__((ext_vector_type(4)));
typedef float f32x16 __attribute__((ext_vector_type(16)));
typedef short s16x4 __attribute__((ext_vector_type(4)));
typedef _Float16 h16x2 __attribute__((ext_vector_type(2)));
typedef _Float16 h16x4 __attribute__((ext_vector_type(4)));
typedef _Float16 h16x8 __attribute__((ext_vector_type(8)));
typedef __bf16 b16x2 __attribute__((ext_vector_type(2)));
typedef __bf16 b16x8 __attribute__((ext_vector_type(8)));
typedef __amdgpu_buffer_rsrc_t rsrc_t;

constexpr int F_ = 1792, DF_ = 257;
constexpr u32 WS_EPOCH = 0;
constexpr u32 WS_F2 = 4096;
constexpr u32 WS_ST = 16384;
constexpr u32 WS_CS = WS_ST + 16384;
constexpr u32 WS_U = 262144;
constexpr u32 WS_V = WS_U + 4194304;
constexpr u32 WS_R = WS_V + 4194304;
constexpr u32 WS_SF = 196608;
constexpr u32 WS_H = WS_R + 4194304;
constexpr u32 WS_END = WS_H + 7340032;
constexpr int LDS_BYTES = 163840;
constexpr int G1_STAGE = 49152;
constexpr int FS = 3 * G1_STAGE;
#define SPIN_CAP (1 << 17)
constexpr int CODE_LINES = 292;
#define HPRIO 2
#define S1PRE 2
#define POLLSLEEP 2
#define CS_DELAY 380
#define WARM 0
#define UPOS 0
#define TV 2
#define ROTM 1
constexpr float XS = 1.0f / 16;
constexpr float FSC = (2.0f / (2048.0f * 512.0f)) * 16.0f;
constexpr size_t WS_DBG = 64 << 20;
#define STAMP(k) do {} while (0)
#define STAMP_DECL
#define STAMP_DUMP() do {} while (0)

#if defined(__HIP_DEVICE_COMPILE__)
#pragma clang attribute push(__attribute__((target("no-packed-fp32-ops"))), apply_to = function)
#endif
DI u32 pk_f16(float a, float b) { h16x2 p = {(_Float16)a, (_Float16)b}; return __builtin_bit_cast(u32, p); }
DI u32 pk_bf16(float a, float b) { b16x2 p = {(__bf16)a, (__bf16)b}; return __builtin_bit_cast(u32, p); }
DI float bf_lo(u32 v) { return __builtin_bit_cast(float, v << 16); }
DI float bf_hi(u32 v) { return __builtin_bit_cast(float, v & 0xffff0000u); }
DI float rnd_bf(float a) { return bf_lo(pk_bf16(a, a)); }
DI float relu(float a) { int i = __builtin_bit_cast(int, a); return __builtin_bit_cast(float, i > 0 ? i : 0); }
DI float cosr(float rev) { return __builtin_amdgcn_cosf(rev); }
DI float sinr(float rev) { return __builtin_amdgcn_sinf(rev); }
DI f32x4 mfma16_f16(u32x2 a, u32x2 b, f32x4 c) { return __builtin_amdgcn_mfma_f32_16x16x16f16(__builtin_bit_cast(h16x4, a), __builtin_bit_cast(h16x4, b), c, 0, 0, 0); }
DI f32x4 mfma16_bf16(u32x2 a, u32x2 b, f32x4 c) { return __builtin_amdgcn_mfma_f32_16x16x16bf16_1k(__builtin_bit_cast(s16x4, a), __builtin_bit_cast(s16x4, b), c, 0, 0, 0); }
DI f32x4 mfma32_f16(u32x4 a, u32x4 b, f32x4 c) { return __builtin_amdgcn_mfma_f32_16x16x32_f16(__builtin_bit_cast(h16x8, a), __builtin_bit_cast(h16x8, b), c, 0, 0, 0); }
DI f32x4 mfma32_bf16(u32x4 a, u32x4 b, f32x4 c) { return __builtin_amdgcn_mfma_f32_16x16x32_bf16(__builtin_bit_cast(b16x8, a), __builtin_bit_cast(b16x8, b), c, 0, 0, 0); }
DI f32x16 mfma3232(u32x4 a, u32x4 b, f32x16 c) { return __builtin_amdgcn_mfma_f32_32x32x16_bf16(__builtin_bit_cast(b16x8, a), __builtin_bit_cast(b16x8, b), c, 0, 0, 0); }
DI u32x2 tr_read(LAS char* p) { return __builtin_bit_cast(u32x2, __builtin_amdgcn_ds_read_tr16_b64_v4i16((LAS s16x4*)p)); }
#define SB() __builtin_amdgcn_sched_barrier(0)
#define BAR() asm volatile("s_waitcnt lgkmcnt(0)\n\ts_barrier" ::: "memory")
#define WAITV(n) asm volatile("s_waitcnt vmcnt(%0)" :: "i"(n) : "memory")
#define PIN(x) asm volatile("" : "+v"(x))
DI u32x4 mk_srd(const void* p, u32 bytes) { uint64_t a = (uint64_t)p; return u32x4{(u32)a, (u32)(a >> 32), bytes, 0x00020000u}; }
DI void glds16(u32 ldsbase, int imm, u32 voff, u32x4 srd, u32 soff) {
  asm volatile("s_add_u32 m0, %0, %1\n\ts_nop 0\n\tbuffer_load_dwordx4 %2, %3, %4 offen lds" :: "s"(ldsbase), "i"(imm), "v"(voff), "s"(srd), "s"(soff) : "memory", "scc");
}
DI void glds4(u32 ldsaddr, u32 voff, u32x4 srd, u32 soff) {
  asm volatile("s_mov_b32 m0, %0\n\ts_nop 0\n\tbuffer_load_dword %1, %2, %3 offen lds" :: "s"(ldsaddr), "v"(voff), "s"(srd), "s"(soff) : "memory");
}
#define ALD4(dst, voff, srd, IMM, FL) asm volatile("buffer_load_dwordx4 %0, %1, %2, 0 offen offset:" #IMM FL : "=v"(dst) : "v"(voff), "s"(srd) : "memory")
#define ALD2(dst, voff, srd, IMM, FL) asm volatile("buffer_load_dwordx2 %0, %1, %2, 0 offen offset:" #IMM FL : "=v"(dst) : "v"(voff), "s"(srd) : "memory")
#define ALD1(dst, voff, srd, IMM, FL) asm volatile("buffer_load_dword %0, %1, %2, 0 offen offset:" #IMM FL : "=v"(dst) : "v"(voff), "s"(srd) : "memory")
DI u32x4 ld_sc1(rsrc_t r, u32 off) { return __builtin_amdgcn_raw_buffer_load_b128(r, off, 0, 16); }
DI u32x2 ld_sc1_2(rsrc_t r, u32 off) { return __builtin_amdgcn_raw_buffer_load_b64(r, off, 0, 16); }
DI void st_sc1(rsrc_t r, u32 off, u32x4 v) { __builtin_amdgcn_raw_buffer_store_b128(v, r, off, 0, 16); }
DI void st_sc1_2(rsrc_t r, u32 off, u32x2 v) { __builtin_amdgcn_raw_buffer_store_b64(v, r, off, 0, 16); }
DI bool all_ok(bool ok) { return __builtin_amdgcn_ballot_w64(!ok) == 0; }
DI bool tags_ok(u32x4 a, u32 tag) { return (a[1] == tag) & (a[3] == tag); }

template <int CTRL> DI float dpp_add(float v) {
  int o = __builtin_amdgcn_update_dpp(0, __builtin_bit_cast(int, v), CTRL, 0xf, 0xf, true);
  return v + __builtin_bit_cast(float, o);
}
DI float row_sum(float v) { v = dpp_add<0xB1>(v); v = dpp_add<0x4E>(v); v = dpp_add<0x141>(v); return dpp_add<0x140>(v); }
DI float xrow_sum(float v) {
  u32 u = __builtin_bit_cast(u32, v);
  auto a = __builtin_amdgcn_permlane16_swap(u, u, false, false);
  v = __builtin_bit_cast(float, (u32)a[0]) + __builtin_bit_cast(float, (u32)a[1]);
  u = __builtin_bit_cast(u32, v);
  auto b = __builtin_amdgcn_permlane32_swap(u, u, false, false);
  return __builtin_bit_cast(float, (u32)b[0]) + __builtin_bit_cast(float, (u32)b[1]);
}
template <bool NEG, bool BF> DI u32x4 cmul_pack(f32x4 re, f32x4 im, f32x4 c, f32x4 s) {
  f32x4 a = NEG ? re * c + im * s : re * c - im * s;
  f32x4 b = NEG ? im * c - re * s : im * c + re * s;
  u32x4 o;
#pragma unroll
  for (int r = 0; r < 4; ++r) o[r] = BF ? pk_bf16(a[r], b[r]) : pk_f16(a[r], b[r]);
  return o;
}
DI u32 rot16(u32 v) { return __builtin_amdgcn_alignbit(v, v, 16); }
template <bool NEG> DI void cmul_half(f32x4& a, f32x4& b, f32x4 re, f32x4 im, f32x4 c, f32x4 s) {
  a = NEG ? re * c + im * s : re * c - im * s;
  b = NEG ? im * c - re * s : im * c + re * s;
}
template <bool BF> DI u32x4 pack4(f32x4 a, f32x4 b) {
  u32x4 o;
#pragma unroll
  for (int r = 0; r < 4; ++r) o[r] = BF ? pk_bf16(a[r], b[r]) : pk_f16(a[r], b[r]);
  return o;
}
DI void glds_a(u32 ldsbase, int imm, u32 v0, u32 v1, u32x4 srd, u32 soff) {
  asm volatile("s_add_u32 m0, %0, %1\n\ts_nop 0\n\tbuffer_load_dwordx4 %2, %4, %5 offen lds\n\tbuffer_load_dwordx4 %3, %4, %5 offen offset:1024 lds"
               :: "s"(ldsbase), "i"(imm), "v"(v0), "v"(v1), "s"(srd), "s"(soff) : "memory", "scc");
}
DI void glds_b(u32 v2, u32 v3, u32x4 srd, u32 soff) {
  asm volatile("buffer_load_dwordx4 %0, %2, %3 offen offset:2048 lds\n\tbuffer_load_dwordx4 %1, %2, %3 offen offset:3072 lds"
               :: "v"(v2), "v"(v3), "s"(srd), "s"(soff) : "memory");
}
DI u32x4 cmulc_f16(f32x4 re, f32x4 im, u32x4 tw) {
  u32 z0 = pk_f16(re[0], im[0]), z1 = pk_f16(re[1], im[1]), z2 = pk_f16(re[2], im[2]), z3 = pk_f16(re[3], im[3]);
  u32 o0, o1, o2, o3;
  const u32 t0 = tw[0], t1 = tw[1], t2 = tw[2], t3 = tw[3];
#define PKM " op_sel_hi:[1,0]\n\t"
#define PKF " op_sel:[1,1,0] op_sel_hi:[0,1,1] neg_hi:[0,1,0]\n\t"
  asm("v_pk_mul_f16 %0, %4, %8" PKM "v_pk_mul_f16 %1, %5, %9" PKM "v_pk_mul_f16 %2, %6, %10" PKM "v_pk_mul_f16 %3, %7, %11" PKM
      "v_pk_fma_f16 %0, %4, %8, %0" PKF "v_pk_fma_f16 %1, %5, %9, %1" PKF "v_pk_fma_f16 %2, %6, %10, %2" PKF "v_pk_fma_f16 %3, %7, %11, %3" PKF "s_nop 1"
      : "=&v"(o0), "=&v"(o1), "=&v"(o2), "=&v"(o3) : "v"(z0), "v"(z1), "v"(z2), "v"(z3), "v"(t0), "v"(t1), "v"(t2), "v"(t3));
  return u32x4{o0, o1, o2, o3};
}
template <int N> struct IC { static constexpr int v = N; };
template <int A, int B, class Fn> DI void static_for(Fn&& f) { if constexpr (A < B) { f(IC<A>{}); static_for<A + 1, B>(f); } }

struct Frag { u32x4 bx[4], aw[2]; };
#define ALD4S(dst, voff, srd, SOFF, FL) asm volatile("buffer_load_dwordx4 %0, %1, %2, %3 offen" FL : "=v"(dst) : "v"(voff), "s"(srd), "s"(SOFF) : "memory")
#define ALD2S(dst, voff, srd, SOFF, FL) asm volatile("buffer_load_dwordx2 %0, %1, %2, %3 offen" FL : "=v"(dst) : "v"(voff), "s"(srd), "s"(SOFF) : "memory")
#define RAWBAR() do { asm volatile("s_barrier" ::: "memory"); } while (0)

typedef unsigned u32x16 __attribute__((ext_vector_type(16)));
__global__ __launch_bounds__(512, 2) void fused_kernel(const char* a_x, const unsigned short* a_wr, const unsigned short* a_wi, const char* a_wg,
                                                       const char* a_wu, const char* a_wd, char* a_ws, char* a_out) {
  extern __shared__ __attribute__((aligned(16))) char smem[];
  LAS char* sm = (LAS char*)smem;
  STAMP_DECL
  STAMP(0);
  u32x16 ka;
  asm volatile("s_load_dwordx16 %0, %1, 0x0" : "=s"(ka) : "s"(__builtin_amdgcn_kernarg_segment_ptr()) : "memory");
  const int b = blockIdx.x, tid = threadIdx.x, lane = tid & 63, w = __builtin_amdgcn_readfirstlane(tid >> 6);
  const int hw = w & 3, p16 = lane & 15, t = lane >> 4;
  const u32 lds0 = (u32)(uintptr_t)sm;
  {
    uint64_t pc; asm volatile("s_getpc_b64 %0" : "=s"(pc));
    asm volatile("s_cmp_gt_u32 %0, 7\n\ts_cbranch_scc1 1f\n\ts_mov_b32 m0, %1\n\ts_nop 0\n\tbuffer_load_dword %2, %3, 0 offen lds\n1:"
                 :: "s"(b), "s"(lds0), "v"(tid * 128), "s"(u32x4{(u32)pc, (u32)(pc >> 32), CODE_LINES * 128, 0x00020000u}) : "memory", "scc");
  }
  const bool is_gemm = b >= 32;
  const int xc = b & 7, r_ = (b - 32) >> 3;
  const int mi = (xc >> 1) * 4 + (r_ & 3), fj = (xc & 1) * 7 + (r_ >> 2);
  const int rot = ((r_ & 3) + (r_ >> 2)) & ROTM;
  const u32 lw = lds0 + hw * 4096;
  u32 vx[4], vg[4], vu[4];
#pragma unroll
  for (int j = 0; j < 4; ++j) {
    vx[j] = (mi * 128 + hw * 32 + j * 7 + (lane >> 3)) * 1024 + (((lane & 7) ^ ((4 * j + t) & 7)) << 4);
    vg[j] = (hw * 16 + j * 4 + t) * 3584 - j * 1024 + fj * 256 + ((p16 ^ (2 * t)) << 4);
    vu[j] = vg[j] ^ 128;
    PIN(vx[j]); PIN(vg[j]); PIN(vu[j]);
  }
  u32 vrow = (b + 256 * hw) * 1024 + lane * 16;
  asm volatile("s_cmp_lt_u32 %0, 4\n\ts_cbranch_scc1 1f\n\ts_setprio 2\n1:" :: "s"(w) : "scc");
  if (w < 4) vrow = 0x7ff00000u;
  const u32 lrow = w < 4 ? lw : lds0 + FS + hw * 1024;
  PIN(vrow);
  SB();
  asm volatile("s_waitcnt lgkmcnt(0)" : "+s"(ka) :: "memory");
#define KARG(i) (((uint64_t)ka[2 * (i) + 1] << 32) | ka[2 * (i)])
  const u32x4 sx = {ka[0], ka[1], 2097152u, 0x00020000u}, sg = {ka[6], ka[7], 1835008u, 0x00020000u}, su = {ka[8], ka[9], 1835008u, 0x00020000u};
  auto g1_issue = [&](int st, int k) {
    const int lb = (st % 3) * G1_STAGE + (k >> 1) * 16384, sk = st ^ rot;
    if (k == 0) glds_a(lw, lb, vx[0], vx[1], sx, sk * 128);
    if (k == 1) glds_b(vx[2], vx[3], sx, sk * 128);
    if (k == 2) glds_a(lw, lb, vg[0], vg[1], sg, sk * 64 * 3584);
    if (k == 3) glds_b(vg[2], vg[3], sg, sk * 64 * 3584);
    if (k == 4) glds_a(lw, lb, vu[0], vu[1], su, sk * 64 * 3584);
    if (k == 5) glds_b(vu[2], vu[3], su, sk * 64 * 3584);
  };
  glds16(lrow, 0, vrow, sx, 0);
  glds16(lrow + (w < 4 ? 0 : 4096), 0, vrow, sx, 1048576);
  if (w < 4 && is_gemm) {
#pragma unroll
    for (int k = 0; k < 6; ++k) g1_issue(0, k);
  }
  SB();
  STAMP(30);
  const f32x4 z4 = {0.f, 0.f, 0.f, 0.f};
  const GAS unsigned short* wr = (const GAS unsigned short*)KARG(1); const GAS unsigned short* wi = (const GAS unsigned short*)KARG(2);
  GAS char* ws = (GAS char*)KARG(6); GAS char* out = (GAS char*)KARG(7);
  auto load_tag = [&]() {
    u32 epoch;
    asm volatile("s_load_dword %0, %1, 0x0\n\ts_waitcnt lgkmcnt(0)" : "=s"(epoch) : "s"(ws) : "memory");
    return epoch * 0x9E3779B1u + 0x7F4A7C15u;
  };
  const rsrc_t rw = __builtin_amdgcn_make_buffer_rsrc((void*)ws, 0, WS_END, 0x00020000);
  const int jp = b & 7, g = b >> 3;
  const int rt = (b & 7) * 4 + ((b >> 3) & 3), ct = b >> 5;
  auto wslot = [&](int s) { return (s < 3 ? 2 * G1_STAGE : s < 6 ? -3 * 16384 : G1_STAGE - 6 * 16384) + s * 16384; };
  u32x4 wf[7][4];
  const int wbase = hw * 4096 + t * 1024 + (p16 >> 2) * 128 + (p16 & 3) * 8 + ((((p16 >> 3) | ((t & 1) << 1))) << 5);
  auto wd_read = [&](int ks, int s) {
#pragma unroll
    for (int u = 0; u < 4; ++u) {
      LAS char* p = sm + wslot(s) + (wbase ^ (u << 5));
      u32x2 lo = tr_read(p), hi = tr_read(p + 512);
      wf[ks][u] = u32x4{lo[0], lo[1], hi[0], hi[1]};
    }
  };
  u32 tag;
  auto ksof = [&](int i) { int k = i + 2 * (rt & 3); return k >= 7 ? k - 7 : k; };
  const u32 hs = WS_H + ((rt * 4) * 56 + w * 7) * 1024 + lane * 16;
  auto ld_h = [&](int j, int i) { return __builtin_amdgcn_raw_buffer_load_b128(rw, hs, (((j + ct) & 3) * 56 + ksof(i)) * 1024, 16); };
  u32x4 hf[4][7];
  const u32x4 sd_ = {ka[10], ka[11], 1835008u, 0x00020000u};
  u32 vwd[4];
  auto wd_setup = [&]() {
#pragma unroll
    for (int j = 0; j < 4; ++j) { vwd[j] = (j * 7 + (lane >> 3)) * 1024 + ct * 128 + (((lane & 7) ^ (((lane >> 4) & 1) << 1) ^ ((j & 1) << 2)) << 4); PIN(vwd[j]); }
  };
  auto wd_issue = [&](int kw, int i, int s) {
    const u32 so = kw * (224 * 1024) + ksof(i) * 32768;
    SB(); glds_a(lw, wslot(s), vwd[0], vwd[1], sd_, so); glds_b(vwd[2], vwd[3], sd_, so); SB();
  };
  auto wait_h_and_load = [&]() {
    const u32 foff = WS_F2 + rt * 256 + (w * 7 + (lane < 7 ? lane : 0)) * 4;
#pragma nounroll
    for (int spin = 0; spin < SPIN_CAP; ++spin) {
      asm volatile("" ::: "memory");
      u32 fl = __builtin_amdgcn_raw_buffer_load_b32(rw, foff, 0, 16);
      if (all_ok(fl == tag)) break;
      __builtin_amdgcn_s_sleep(POLLSLEEP);
    }
    __builtin_amdgcn_fence(__ATOMIC_ACQUIRE, "wavefront");
    asm volatile("" ::: "memory");
    STAMP(15);
#pragma unroll
    for (int tt = 0; tt < 3; ++tt)
#pragma unroll
      for (int i = 0; i < 7; ++i) hf[tt][i] = ld_h(tt, i);
    SB();
  };
  const float pf = (float)p16, tf = (float)t;

  auto helper = [&](auto G_) {
    constexpr bool G = decltype(G_)::v;
    const int ht = tid & 255;
    const u32x4 sw = {ka[12], ka[13], WS_END, 0x00020000u};
    const u32x4 sfr_ = {ka[2], ka[3], 2048 * DF_ * 2, 0x00020000u}, sfi_ = {ka[4], ka[5], 2048 * DF_ * 2, 0x00020000u};
    SB();
#define DMA(st, k) do { if (G) { SB(); g1_issue(st, k); SB(); } } while (0)
#define GWAIT(n) do { if (G) WAITV(n); } while (0)
#define WAITG(n) do { if (G) WAITV(n); else WAITV(0); } while (0)
#define GBAR() do { if (G) RAWBAR(); } while (0)
    auto hsync = [&](int off, int stride = 0) {
      asm volatile("s_waitcnt lgkmcnt(0)" ::: "memory");
      *(volatile LAS u32*)(sm + off + (lane & 3) * stride + hw * 4) = tag;
#pragma nounroll
      for (int spin = 0; spin < SPIN_CAP; ++spin) { if (all_ok(*(volatile LAS u32*)(sm + off + hw * stride + (lane & 3) * 4) == tag)) break; }
      asm volatile("" ::: "memory");
    };
#define XBAR(n) do { if (G) BAR(); else hsync(G1_STAGE + (n) * 16); } while (0)
    u32x4 b2re[2], b2im[2], tw[2], ac2re, ac2im, bc3re, bc3im, qa8, qa8i, bc1re, bc1im, ac4re, ac4im, t1, bi1re[2], bi1im[2];
    u32x2 b1re, b1im;
    f32x4 t2c, t2s, t4c[2], t4s[2];
    {
      const float d5 = pf * (1.0f / 512), a5 = 4.0f * tf * d5;
#pragma unroll
      for (int r = 0; r < 4; ++r) {
        const float th = a5 + r * d5, c = cosr(th * 32.0f), s = sinr(th * 32.0f);
        ac2re[r] = pk_f16(c, s); ac2im[r] = rot16(ac2re[r]) ^ 0x8000u;
        bc3im[r] = pk_bf16(s, c); bc3re[r] = rot16(bc3im[r]) ^ 0x80000000u;
#pragma unroll
        for (int hh = 0; hh < 2; ++hh) {
          const float tq = th + hh * (16.0f * d5);
          tw[hh][r] = pk_f16(cosr(tq), sinr(tq));
          b2re[hh][r] = pk_f16(cosr(tq * 16.0f), sinr(tq * 16.0f)); b2im[hh][r] = rot16(b2re[hh][r]) ^ 0x8000u;
        }
      }
      b1re = u32x2{__builtin_amdgcn_perm(ac2re[1], ac2re[0], 0x05040100u), __builtin_amdgcn_perm(ac2re[3], ac2re[2], 0x05040100u)};
      b1im = u32x2{__builtin_amdgcn_perm(ac2im[1], ac2im[0], 0x05040100u), __builtin_amdgcn_perm(ac2im[3], ac2im[2], 0x05040100u)};
      const int po = p16 & 1, j8 = p16 >> 1;
      const float j8f = (float)j8 * 0.125f;
      const float ra = 2.0f * tf * j8f, rb = ra + j8f;
      const float ca = cosr(ra), sa = sinr(ra), cb = cosr(rb), sb = sinr(rb);
      const u32 fa0 = pk_f16(ca, sa), fb0 = pk_f16(cb, sb);
      const u32 fa = po ? (rot16(fa0) ^ 0x8000u) : fa0, fb = po ? (rot16(fb0) ^ 0x8000u) : fb0;
      const u32 sel = (j8 & 3) == 0 ? 0x03020100u : (j8 < 4 ? 0x0c0c0100u : 0x01000c0cu);
      qa8 = u32x4{fa, fb, __builtin_amdgcn_perm(0u, fa, sel), __builtin_amdgcn_perm(0u, fb, sel)};
      const u32 ia = pk_bf16(sa, ca), ib = pk_bf16(sb, cb), pa = rot16(ia) ^ 0x80000000u, pb = rot16(ib) ^ 0x80000000u;
      const bool on = (po == 0) ? (t < 2) : (t >= 2);
      const u32 hc = pk_bf16(0.5f * ca, 0.f);
      qa8i = u32x4{po ? ia : pa, po ? ib : pb, (t & 1) == 0 ? (po ? hc << 16 : hc) : (on ? pa : 0u), on ? pb : 0u};
      PIN(ac2re); PIN(ac2im); PIN(bc3re); PIN(bc3im); PIN(tw[0]); PIN(tw[1]); PIN(b2re[0]); PIN(b2re[1]); PIN(b2im[0]); PIN(b2im[1]); PIN(b1re); PIN(b1im); PIN(qa8); PIN(qa8i);
    }
    SB();
    tag = load_tag();
    STAMP(21);
    LAS char* const img = sm + FS + hw * 1024;
    u32 stat[2];
    WAITV(0);
    STAMP(1);
    {
      const u32 vo = (8 * b + 2 * hw) * DF_ * 2 + (lane >> 5) * (DF_ * 2 + 2) + (lane & 31) * 16;
      glds16(lds0 + hw * 1024, FS + 8192, vo, sfr_, 0); glds16(lds0 + hw * 1024, FS + 12288, vo, sfi_, 0);
    }
    auto tpat = [&](int q, int u) { const u32 r = tag >> (1 + 7 * q + 3 * u); return (tag & 1u) | ((r & 3u) << 1) | ((r & 28u) << 14); };
    {
      float v[2][8], mean[2], var[2];
#pragma unroll
      for (int a = 0; a < 2; ++a) {
        const u32x4 xv = *(LAS u32x4*)(img + a * 4096 + lane * 16);
#pragma unroll
        for (int k = 0; k < 4; ++k) { v[a][2 * k] = bf_lo(xv[k]); v[a][2 * k + 1] = bf_hi(xv[k]); }
      }
#pragma unroll
      for (int a = 0; a < 2; ++a) mean[a] = xrow_sum(row_sum(((v[a][0] + v[a][1]) + (v[a][2] + v[a][3])) + ((v[a][4] + v[a][5]) + (v[a][6] + v[a][7])))) * (1.0f / 512);
#pragma unroll
      for (int a = 0; a < 2; ++a) {
        float q0 = 0.f, q1 = 0.f;
#pragma unroll
        for (int k = 0; k < 4; ++k) { float d = v[a][k] - mean[a], e = v[a][k + 4] - mean[a]; q0 += d * d; q1 += e * e; }
        var[a] = xrow_sum(row_sum(q0 + q1)) * (1.0f / 512);
      }
#pragma unroll
      for (int a = 0; a < 2; ++a) {
        stat[a] = pk_bf16(mean[a], __builtin_amdgcn_sqrtf(rnd_bf(rnd_bf(var[a]) + 1e-5f)));
        const float sc = __builtin_amdgcn_rcpf(bf_hi(stat[a])) * XS, of = -bf_lo(stat[a]) * sc;
        u32x4 xh;
#pragma unroll
        for (int k = 0; k < 4; ++k) xh[k] = pk_f16(__builtin_fmaf(v[a][2 * k], sc, of), __builtin_fmaf(v[a][2 * k + 1], sc, of));
        *(LAS u32x4*)(img + a * 4096 + ((lane ^ ((t & 1) << 1)) << 4)) = xh;
      }
      asm volatile("" ::: "memory");
      STAMP(23);
#pragma unroll
      for (int k = 0; k < 6; ++k) DMA(2, k);
      u32x2 tra[2][2];
#pragma unroll
      for (int a = 0; a < 2; ++a)
#pragma unroll
        for (int hh = 0; hh < 2; ++hh) tra[a][hh] = tr_read(img + a * 4096 + (4 * t + (p16 >> 2)) * 64 + ((hh ^ (t & 1)) << 5) + (p16 & 3) * 8);
      f32x4 d1re[2][2], d1im[2][2], d2re[2], d2im[2];
#pragma unroll
      for (int a = 0; a < 2; ++a)
#pragma unroll
        for (int hh = 0; hh < 2; ++hh) { d1re[a][hh] = mfma16_f16(tra[a][hh], b1re, z4); d1im[a][hh] = mfma16_f16(tra[a][hh], b1im, z4); }
#pragma unroll
      for (int a = 0; a < 2; ++a)
#pragma unroll
        for (int hh = 0; hh < 2; ++hh) {
          u32x4 ta = cmulc_f16(d1re[a][hh], d1im[a][hh], tw[hh]);
          d2re[a] = mfma32_f16(ta, b2re[hh], hh ? d2re[a] : z4); d2im[a] = mfma32_f16(ta, b2im[hh], hh ? d2im[a] : z4);
        }
      float ny[2];
#pragma unroll
      for (int a = 0; a < 2; ++a) { f32x4 q = d1re[a][0] + d1re[a][1]; ny[a] = xrow_sum((q[0] - q[1]) + (q[2] - q[3])); }
      int e = 4 * p16 + t; e ^= (e >> 3) & 3;
#pragma unroll
      for (int a = 0; a < 2; ++a) {
        if (lane == 0) d2im[a][0] = ny[a];
        *(LAS u32x4*)(img + a * 4096 + e * 16) = pack4<false>(d2re[a], d2im[a]);
      }
    }
    STAMP(24);
    WAITG(12);
    XBAR(0);
    STAMP(2);
#pragma unroll
    for (int k = 0; k < 6; ++k) DMA(3, k);
    u32x4 uo[2][2];
    {
      u32x2 e0[2], e1[2];
#pragma unroll
      for (int k = 0; k < 2; ++k) {
        int e = 16 * hw + 8 * k + (p16 >> 1); e ^= (e >> 3) & 3;
        e0[k] = *(LAS u32x2*)(sm + FS + (2 * t) * 1024 + e * 16 + (p16 & 1) * 8);
        e1[k] = *(LAS u32x2*)(sm + FS + (2 * t + 1) * 1024 + e * 16 + (p16 & 1) * 8);
      }
      u32 wv[2][2][2];
      {
        LAS char* fp = sm + FS + 8192 + t * 1024 + hw * 128 + p16 * 4;
#pragma unroll
        for (int hf = 0; hf < 2; ++hf)
#pragma unroll
          for (int k = 0; k < 2; ++k)
#pragma unroll
            for (int u = 0; u < 2; ++u) {
              LAS char* q = fp + hf * 510 + k * 64 + u * 2;
              wv[hf][k][u] = (u32)*(LAS unsigned short*)q | ((u32)*(LAS unsigned short*)(q + 4096) << 16);
            }
      }
      f32x4 d8[4];
#pragma unroll
      for (int u = 0; u < 4; ++u) d8[u] = mfma16_f16(u32x2{qa8[0], qa8[1]}, u32x2{e0[u >> 1][u & 1], e1[u >> 1][u & 1]}, z4);
      f32x4 d8s = mfma16_f16(u32x2{qa8[2], qa8[3]}, u32x2{e0[0][0], e1[0][0]}, z4);
      if ((hw | p16) == 0) d8[0] = d8s;
      const u32 p0 = tpat(p16 & 3, 0), p1 = tpat(p16 & 3, 1);
#pragma unroll
      for (int hf = 0; hf < 2; ++hf)
#pragma unroll
        for (int k = 0; k < 2; ++k) {
          uo[hf][k] = u32x4{(pk_f16(d8[2 * k][2 * hf], d8[2 * k][2 * hf + 1]) & 0xfff8fff8u) | p0, wv[hf][k][0],
                            (pk_f16(d8[2 * k + 1][2 * hf], d8[2 * k + 1][2 * hf + 1]) & 0xfff8fff8u) | p1, wv[hf][k][1]};
          PIN(uo[hf][k]);
        }
    }
    auto ustore = [&]() {
      const u32 ub = WS_U + b * 16384 + (2 * t) * 2048 + hw * 512 + p16 * 16;
#pragma unroll
      for (int hf = 0; hf < 2; ++hf)
#pragma unroll
        for (int k = 0; k < 2; ++k) st_sc1(rw, ub + hf * 2048 + k * 256, uo[hf][k]);
      if (lane < 2) st_sc1_2(rw, WS_ST + b * 64 + hw * 8 + lane * 32, u32x2{lane ? stat[1] : stat[0], tag});
      STAMP(19);
    };
    ustore();
    GWAIT(12 + 5);
    GBAR();
    STAMP(3);
    {
      const float jf = (float)jp;
      const float d1 = (8.0f * pf + jf) * (1.0f / 2048), a1 = 4.0f * tf * d1, d2 = pf * (1.0f / 256), a2 = (32.0f * tf + jf) * (pf * (1.0f / 2048));
      DMA(4, 0); DMA(4, 1);
#pragma unroll
      for (int r = 0; r < 4; ++r) {
        DMA(4, 2 + r);
        const float n1 = a1 + r * d1, n2 = a2 + r * d2;
        bc1re[r] = pk_f16(cosr(n1 * 16.0f), sinr(n1 * 16.0f)); bc1im[r] = rot16(bc1re[r]) ^ 0x8000u;
        t1[r] = pk_f16(cosr(n1), sinr(n1));
        ac4im[r] = pk_bf16(sinr(n2 * 16.0f), cosr(n2 * 16.0f)); ac4re[r] = rot16(ac4im[r]) ^ 0x80000000u;
        t2c[r] = cosr(n2); t2s[r] = sinr(n2);
      }
      PIN(bc1re); PIN(bc1im); PIN(ac4re); PIN(ac4im); PIN(t1); PIN(t2c); PIN(t2s);
    }
    u32x4 ug[4];
    const int uq = ht & 3;
    const int ui = (((ht >> 2) & 3) << 4) + (ht >> 4);
    const u32 uoff = WS_U + ui * 16384 + jp * 2048 + g * 64 + uq * 16;
    auto caddr = [&](int i) { return (LAS char*)(sm + FS + uq * 2048 + ((((i & 192) + 4 * (i & 15) + ((i >> 4) & 3)) * 4) ^ (uq << 5))); };
    if (!G) {
#pragma nounroll
      for (int spin = 0; spin < SPIN_CAP; ++spin) {
        asm volatile("" ::: "memory");
        u32x2 q = ld_sc1_2(rw, WS_ST + (b + 224) * 64 + hw * 8);
        if (all_ok(q[1] == tag)) break;
        __builtin_amdgcn_s_sleep(8);
      }
      __builtin_amdgcn_s_sleep(20);
    }
    STAMP(26);
#pragma unroll
    for (int k = 0; k < 4; ++k) ALD4S(ug[k], uoff, sw, k * 64 * 16384, " sc1");
    WAITG(5 + 12 + 4);
    XBAR(2);
    STAMP(4);
#pragma unroll
    for (int k = 0; k < 6; ++k) DMA(5, k);
    STAMP(27);
    GWAIT(12);
    PIN(ug[0]); PIN(ug[1]); PIN(ug[2]); PIN(ug[3]);
    const u32 c0 = tpat(uq, 0), c1 = tpat(uq, 1);
#define CHK1 all_ok(((((ug[0][0] ^ c0) | (ug[1][0] ^ c0) | (ug[2][0] ^ c0) | (ug[3][0] ^ c0) | (ug[0][2] ^ c1) | (ug[1][2] ^ c1) | (ug[2][2] ^ c1) | (ug[3][2] ^ c1)) & 0x00070007u) == 0))
    if (__builtin_expect(!CHK1, 0)) {
#pragma nounroll
      for (int spin = 0; spin < SPIN_CAP; ++spin) {
        asm volatile("" ::: "memory");
#pragma unroll
        for (int k = 0; k < 4; ++k) ug[k] = ld_sc1(rw, uoff + k * 64 * 16384);
        if (CHK1) break;
      }
    }
    {
      LAS char* cp = caddr(ui);
      const u32 x0 = c0 ^ 0x00040004u, x1 = c1 ^ 0x00040004u;
#pragma unroll
      for (int k = 0; k < 4; ++k) {
        *(LAS u32*)(cp + k * 256) = ug[k][0] ^ x0; *(LAS u32*)(cp + k * 256 + 1024) = ug[k][2] ^ x1;
        *(LAS u32*)(cp + k * 256 + 8192) = ug[k][1]; *(LAS u32*)(cp + k * 256 + 9216) = ug[k][3];
      }
    }
    u32x4 fsp[2];
    const bool special = !G && b < 8 && hw == 0;
    if (special) {
#pragma nounroll
      for (int spin = 0; spin < SPIN_CAP; ++spin) {
        asm volatile("" ::: "memory");
        bool ok = true;
#pragma unroll
        for (int c = 0; c < 2; ++c) {
          const u32x4 fa = ld_sc1(rw, WS_SF + jp * 4096 + c * 2048 + lane * 32), fb = ld_sc1(rw, WS_SF + jp * 4096 + c * 2048 + lane * 32 + 16);
          ok &= tags_ok(fa, tag) & tags_ok(fb, tag);
          fsp[c] = u32x4{fa[0], fa[2], fb[0], fb[2]};
        }
        if (all_ok(ok)) break;
      }
    }
    STAMP(28);
    XBAR(3);
    STAMP(5);
    LAS char* const colp = sm + FS + hw * 2048 + ((lane * 16) ^ (hw << 5));
    {
      u32x4 uin[2], fin[2];
#pragma unroll
      for (int a = 0; a < 2; ++a) { uin[a] = *(LAS u32x4*)(colp + a * 1024); fin[a] = *(LAS u32x4*)(colp + 8192 + a * 1024); }
      const bool two = special && (jp & 3) == 0;
      f32x4 dre[2], dim[2], keep;
      auto col_xform = [&](auto NA_) {
        constexpr int NA = decltype(NA_)::v;
        u32x4 ta[2];
        DMA(6, 0);
#pragma unroll
        for (int a = 0; a < NA; ++a) { dre[a] = mfma32_f16(uin[a], bc1re, z4); dim[a] = mfma32_f16(uin[a], bc1im, z4); }
        DMA(6, 1);
#pragma unroll
        for (int a = 0; a < NA; ++a) { ta[a] = cmulc_f16(dre[a], dim[a], t1); dre[a] = mfma32_f16(ac2re, ta[a], z4); dim[a] = mfma32_f16(ac2im, ta[a], z4); }
        DMA(6, 2);
#pragma unroll
        for (int a = 0; a < NA; ++a) {
          f32x4 fr_, fi_;
#pragma unroll
          for (int r = 0; r < 4; ++r) { fr_[r] = bf_lo(fin[a][r]); fi_[r] = bf_hi(fin[a][r]); }
          ta[a] = cmul_pack<false, true>(dre[a], dim[a], fr_, fi_); dre[a] = mfma32_bf16(ta[a], bc3re, z4); dim[a] = mfma32_bf16(ta[a], bc3im, z4);
          DMA(6, 3 + a);
        }
#pragma unroll
        for (int a = 0; a < NA; ++a) {
          ta[a] = cmul_pack<false, true>(dre[a], dim[a], t2c, t2s); dre[a] = mfma32_bf16(ac4re, ta[a], z4); dim[a] = mfma32_bf16(ac4im, ta[a], z4);
          if (a == 0) DMA(6, 5);
        }
      };
      if (!G) {
        if (special) {
          fin[0] = jp > 4 ? fsp[1] : fsp[0];
          if (two) {
            const u32x4 us = uin[0];
            uin[0] = us >> 16; fin[0] = fsp[1];
            col_xform(IC<1>{});
            keep = dre[0];
            uin[0] = us & 0xffffu; fin[0] = fsp[0];
          }
        }
      }
      col_xform(IC<2>{});
      if (!G) { if (two) dim[0] = keep; }
#pragma unroll
      for (int a = 0; a < 2; ++a) *(LAS u32x4*)(colp + a * 1024) = pack4<true>(dre[a], dim[a]);
      {
        const u32 vo = b * 7168 + hw * 2048 + lane * 16;
        glds_a(lds0 + hw * 2048, FS + 8192, vo, (hw < 3 ? vo : 0x7fffffffu), sd_, 0);
      }
#define NW 2
    }
    STAMP(29);
    GWAIT(12 + NW);
    XBAR(4);
    STAMP(6);
    {
      const int ps = ht >> 2;
      u32 vo[4][2];
#pragma unroll
      for (int k = 0; k < 4; ++k) { LAS char* cp = caddr((ps >> 5) + 2 * k + 8 * (ps & 31)); vo[k][0] = *(LAS u32*)cp; vo[k][1] = *(LAS u32*)(cp + 1024); }
#pragma unroll
      for (int k = 0; k < 6; ++k) DMA(7, k);
#pragma unroll
      for (int k = 0; k < 4; ++k) st_sc1(rw, WS_V + b * 16384 + k * 4096 + ht * 16, u32x4{vo[k][0], tag, vo[k][1], tag});
    }
    STAMP(20);
    GWAIT(NW + 12 + 4);
    XBAR(5);
    STAMP(7);
    wd_setup();
    u32x2 csv[4], stv;
    const u32 csoff = WS_CS + ((t & 1) * F_ + fj * 128 + hw * 32 + (lane >> 5) * 16 + ((p16 >> 2) & 1) * 8 + (p16 >> 3) * 4 + (p16 & 3)) * 8;
    const u32 stoff = WS_ST + (((mi & 1) * 128 + hw * 32 + (lane & 31)) * 8 + (mi >> 1)) * 8;
    if (G) {
      ALD2S(csv[0], csoff, sw, 0, " sc1"); ALD2S(csv[1], csoff, sw, 2 * F_ * 8, " sc1"); ALD2S(csv[2], csoff, sw, 4 * F_ * 8, " sc1"); ALD2S(csv[3], csoff, sw, 6 * F_ * 8, " sc1");
      ALD2(stv, stoff, sw, 0, " sc1");
    }
#pragma unroll
    for (int hh = 0; hh < 2; ++hh) {
      wd_issue(w, hh, hh);
      const float n2 = (float)(8 * (p16 >> 2) + (p16 & 3)) + 4.0f * hh;
#pragma unroll
      for (int m = 0; m < 4; ++m) {
        float rev = n2 * ((tf + 4.0f * m) * (1.0f / 32));
        bi1im[hh][m] = pk_bf16(sinr(rev), cosr(rev)); bi1re[hh][m] = rot16(bi1im[hh][m]) ^ 0x80000000u;
        float rev2 = n2 * ((4.0f * tf + m) * (1.0f / 512));
        t4c[hh][m] = cosr(rev2); t4s[hh][m] = sinr(rev2);
      }
      PIN(bi1re[hh]); PIN(bi1im[hh]); PIN(t4c[hh]); PIN(t4s[hh]);
    }
    wd_issue(w, 2, 2);
    if (G) {
      WAITV(12);
      PIN(csv[0]); PIN(csv[1]); PIN(csv[2]); PIN(csv[3]); PIN(stv);
#define CHK3 all_ok((stv[1] == tag) & (csv[0][1] == tag) & (csv[1][1] == tag) & (csv[2][1] == tag) & (csv[3][1] == tag))
      if (__builtin_expect(!CHK3, 0)) {
#pragma nounroll
        for (int spin = 0; spin < SPIN_CAP; ++spin) {
          asm volatile("" ::: "memory");
#pragma unroll
          for (int k = 0; k < 4; ++k) csv[k] = ld_sc1_2(rw, csoff + k * 2 * F_ * 8);
          stv = ld_sc1_2(rw, stoff);
          if (CHK3) break;
        }
      }
      const u32 s0 = csv[0][0], s1 = csv[1][0], s2 = csv[2][0], s3 = csv[3][0];
      const float cs = (__builtin_bit_cast(float, s0) + __builtin_bit_cast(float, s1)) + (__builtin_bit_cast(float, s2) + __builtin_bit_cast(float, s3));
      const float ch = rnd_bf(cs);
      LAS char* cp = sm + FS + hw * 2048 + lane * 4;
      *(LAS u32*)cp = pk_bf16(ch, cs - ch);
      *(LAS u32*)(cp + 256) = __builtin_amdgcn_perm(0u, stv[0], 0x01000100u) ^ 0x80008000u;
    }
    u32x4 xr[2], rv[2][2];
    const u32 voff = WS_V + ((b & 7) * 32 + (b >> 3)) * 64 + (p16 & 3) * 16 + ((8 * hw + (p16 >> 2)) * 8 + 2 * t) * 16384;
    if (!G) {
      WAITV(12);
#pragma nounroll
      for (int spin = 0; spin < SPIN_CAP; ++spin) {
        asm volatile("" ::: "memory");
        u32x2 q = ld_sc1_2(rw, WS_V + (b + 224) * 16384 + hw * 4096);
        if (all_ok(q[1] == tag)) break;
        __builtin_amdgcn_s_sleep(8);
      }
      __builtin_amdgcn_s_sleep(20);
    }
    XBAR(6);
    STAMP(8);
    {
      const u32 xo = (b + 256 * hw) * 1024 + (4 * p16 + t) * 16;
      ALD4S(rv[0][0], voff, sw, 0, " sc1"); ALD4S(rv[0][1], voff, sw, 16384, " sc1"); ALD4S(rv[1][0], voff, sw, 32 * 16384, " sc1"); ALD4S(rv[1][1], voff, sw, 33 * 16384, " sc1");
      ALD4S(xr[0], xo, sx, 0, ""); ALD4S(xr[1], xo, sx, 1048576, "");
    }
    if (TV == 1) { wd_issue(w, 3, 3); wd_issue(w, 4, 4); wd_issue(w, 5, 5); }
    auto mwait = [&](int off) {
      if (G) {
#pragma nounroll
        for (int spin = 0; spin < SPIN_CAP; ++spin) { if (all_ok(*(volatile LAS u32*)(sm + off + (lane & 3) * 4) == tag)) break; __builtin_amdgcn_s_sleep(1); }
        asm volatile("" ::: "memory");
      }
    };
    WAITV(6 + (TV == 1 ? 12 : 0));
    wd_read(0, 0); wd_read(1, 1); wd_read(2, 2);
    STAMP(9);
    WAITV(TV == 1 ? 12 : 0);
    PIN(rv[0][0]); PIN(rv[0][1]); PIN(rv[1][0]); PIN(rv[1][1]); PIN(xr[0]); PIN(xr[1]);
#define CHK4 all_ok(tags_ok(rv[0][0], tag) & tags_ok(rv[0][1], tag) & tags_ok(rv[1][0], tag) & tags_ok(rv[1][1], tag))
    if (__builtin_expect(!CHK4, 0)) {
#pragma nounroll
      for (int spin = 0; spin < SPIN_CAP; ++spin) {
        asm volatile("" ::: "memory");
#pragma unroll
        for (int k = 0; k < 2; ++k) { rv[k][0] = ld_sc1(rw, voff + k * 32 * 16384); rv[k][1] = ld_sc1(rw, voff + k * 32 * 16384 + 16384); }
        if (CHK4) break;
        __builtin_amdgcn_s_sleep(4);
      }
    }
    STAMP(12);
    LAS char* const yb = sm + FS + 8192;
#pragma unroll
    for (int k = 0; k < 2; ++k) {
      f32x4 da = mfma16_bf16(u32x2{qa8i[0], qa8i[1]}, u32x2{rv[k][0][0], rv[k][1][0]}, z4), db = mfma16_bf16(u32x2{qa8i[0], qa8i[1]}, u32x2{rv[k][0][2], rv[k][1][2]}, z4);
      if (k == 0) { f32x4 ds = mfma16_bf16(u32x2{qa8i[2], qa8i[3]}, u32x2{rv[0][0][0], rv[0][1][0]}, z4); if ((hw | p16) == 0) da = ds; }
      *(LAS u32x2*)(yb + (2 * t) * 1024 + (32 * hw + 16 * k + p16) * 8) = u32x2{pk_bf16(da[0], da[1]), pk_bf16(db[0], db[1])};
      *(LAS u32x2*)(yb + (2 * t + 1) * 1024 + (32 * hw + 16 * k + p16) * 8) = u32x2{pk_bf16(da[2], da[3]), pk_bf16(db[2], db[3])};
    }
    hsync(FS + 1024);
    STAMP(13);
    u32x4 ro[2][2];
#pragma unroll
    for (int a = 0; a < 2; ++a) {
      u32x4 av;
#pragma unroll
      for (int m = 0; m < 4; ++m) av[m] = *(LAS u32*)(yb + (hw + 4 * a) * 1024 + (lane + 64 * m) * 4);
      const float nyq = bf_hi(*(LAS u32*)(yb + (hw + 4 * a) * 1024));
#pragma unroll
      for (int hh = 0; hh < 2; ++hh) {
        f32x4 gre = mfma32_bf16(av, bi1re[hh], z4), gim = mfma32_bf16(av, bi1im[hh], z4);
        u32x4 bb = cmul_pack<false, true>(gre, gim, t4c[hh], t4s[hh]);
        f32x4 d = mfma32_bf16(bb, bc3re, f32x4{nyq, -nyq, nyq, -nyq});
        ro[a][hh] = u32x4{pk_bf16(__builtin_fmaf(d[0], FSC, bf_lo(xr[a][2 * hh])), __builtin_fmaf(d[1], FSC, bf_hi(xr[a][2 * hh]))), tag,
                          pk_bf16(__builtin_fmaf(d[2], FSC, bf_lo(xr[a][2 * hh + 1])), __builtin_fmaf(d[3], FSC, bf_hi(xr[a][2 * hh + 1]))), tag};
        PIN(ro[a][hh]);
      }
    }
    STAMP(14);
    mwait(FS + 1056);
    if (TV != 1) { wd_issue(w, 3, 3); wd_issue(w, 4, 4); wd_issue(w, 5, 5); }
    wd_issue(w, 6, 0);
#pragma unroll
    for (int a = 0; a < 2; ++a)
#pragma unroll
      for (int hh = 0; hh < 2; ++hh) st_sc1(rw, WS_R + (b + 256 * hw + 1024 * a) * 2048 + hh * 1024 + (4 * p16 + t) * 16, ro[a][hh]);
    STAMP(11);
  };

  if (w < 4) {
    if (is_gemm) {
      tag = load_tag();
      const int h = lane >> 5;
      int xoff[4], woff[2];
#pragma unroll
      for (int ks = 0; ks < 4; ++ks) { int tok = lane & 31; xoff[ks] = tok * 128 + (((2 * ks + h) ^ ((tok >> 1) & 7)) << 4); PIN(xoff[ks]); }
      {
        int arr = t & 1, q = p16 >> 2, p = lane & 3;
#pragma unroll
        for (int m = 0; m < 2; ++m) {
          woff[m] = 16384 + arr * 16384 + (8 * h + q) * 256 + (((hw * 2 + m) ^ (q | (arr << 2))) << 5) + (p & 1) * 16 + (p >> 1) * 8;
          PIN(woff[m]);
        }
      }
      auto rdx = [&](Frag& f, int st, int ks, int n) { f.bx[n] = *(LAS u32x4*)(sm + (st % 3) * G1_STAGE + xoff[ks] + n * 4096); };
      auto rdw = [&](Frag& f, int st, int ks, int m) {
        LAS char* buf = sm + (st % 3) * G1_STAGE;
        u32x2 lo = tr_read(buf + woff[m] + ks * 4096), hi = tr_read(buf + woff[m] + ks * 4096 + 1024);
        f.aw[m] = u32x4{lo[0], lo[1], hi[0], hi[1]};
      };
      f32x16 acc[2][4];
#pragma unroll
      for (int m = 0; m < 2; ++m)
#pragma unroll
        for (int n = 0; n < 4; ++n)
#pragma unroll
          for (int k = 0; k < 16; ++k) acc[m][n][k] = 0.f;
      Frag fr[2];
      u32 qa[2], qb[4];
      SB();
      STAMP(21);
      g1_issue(1, 0); g1_issue(1, 1);
      WAITV(4);
      {
        LAS char* mb = sm + G1_STAGE + 32768 + 3072;
        *(volatile LAS u32*)(mb + (lane & 3) * 4096 + hw * 4) = tag;
#pragma nounroll
        for (int spin = 0; spin < SPIN_CAP; ++spin) { if (all_ok(*(volatile LAS u32*)(mb + hw * 4096 + (lane & 3) * 4) == tag)) break; }
        asm volatile("" ::: "memory");
      }
      STAMP(1);
      rdw(fr[0], 0, 0, 0); rdx(fr[0], 0, 0, 0); rdx(fr[0], 0, 0, 1); rdx(fr[0], 0, 0, 2); rdx(fr[0], 0, 0, 3); rdw(fr[0], 0, 0, 1);
      static_for<0, 30>([&](auto KS_) {
        constexpr int KS = decltype(KS_)::v, st = KS >> 2, ks = KS & 3;
        constexpr int nst = ks < 3 ? st : st + 1, nks = (ks + 1) & 3;
        Frag& f = fr[KS & 1]; Frag& g = fr[(KS + 1) & 1];
        SB();
        if constexpr (ks == 3) { if constexpr (st == 0) WAITV(0); BAR(); STAMP(2 + st); SB(); }
        acc[0][0] = mfma3232(f.aw[0], f.bx[0], acc[0][0]); SB(); rdw(g, nst, nks, 0); SB();
        acc[0][1] = mfma3232(f.aw[0], f.bx[1], acc[0][1]); SB(); rdx(g, nst, nks, 0); SB();
        if constexpr (KS == 0 && S1PRE != 6) { g1_issue(1, 2); SB(); }
        if constexpr (KS == 1 && S1PRE != 6) { g1_issue(1, 5); SB(); }
        acc[0][2] = mfma3232(f.aw[0], f.bx[2], acc[0][2]); SB(); rdx(g, nst, nks, 1); SB();
        acc[0][3] = mfma3232(f.aw[0], f.bx[3], acc[0][3]); SB(); rdx(g, nst, nks, 2); SB();
        if constexpr (KS == 0 && S1PRE != 6) { g1_issue(1, 3); SB(); }
        acc[1][3] = mfma3232(f.aw[1], f.bx[3], acc[1][3]); SB(); rdx(g, nst, nks, 3); SB();
        acc[1][2] = mfma3232(f.aw[1], f.bx[2], acc[1][2]); SB(); rdw(g, nst, nks, 1); SB();
        acc[1][1] = mfma3232(f.aw[1], f.bx[1], acc[1][1]); SB();
        if constexpr (KS == 0 && S1PRE != 6) { g1_issue(1, 4); SB(); }
        if constexpr (KS == 27) {
          LAS char* cp = sm + FS + (lane & 31) * 4;
          qa[0] = *(LAS u32*)(cp + hw * 2048); qa[1] = *(LAS u32*)(cp + hw * 2048 + 128);
#pragma unroll
          for (int n = 0; n < 4; ++n) qb[n] = *(LAS u32*)(cp + n * 2048 + 256);
          SB();
        }
        acc[1][0] = mfma3232(f.aw[1], f.bx[0], acc[1][0]); SB();
      });
      rdw(fr[1], 7, 3, 0); rdx(fr[1], 7, 3, 0); rdw(fr[1], 7, 3, 1); rdx(fr[1], 7, 3, 1); rdx(fr[1], 7, 3, 2); rdx(fr[1], 7, 3, 3);
      __builtin_amdgcn_s_setprio(3);
      STAMP(9);
      u32x4 ca[2] = {u32x4{h ? 0u : qa[0], 0u, 0u, 0u}, u32x4{h ? 0u : qa[1], 0u, 0u, 0u}}, cb = {0u, 0u, 0u, 0u};
      const u32 hoff = WS_H + ((mi * 8 + (t & 1)) * 56 + fj * 4 + hw) * 1024 + h * 256 + p16 * 16;
      float ev[8];
      auto epi_a = [&](int q) {
#pragma unroll
        for (int k = 0; k < 8; ++k) ev[k] = relu(acc[q & 1][q >> 1][k]);
      };
      auto epi_b = [&](int q) {
#pragma unroll
        for (int k = 0; k < 8; ++k) ev[k] *= acc[q & 1][q >> 1][8 + k];
      };
      auto epi_c = [&](int q) {
        __builtin_amdgcn_raw_buffer_store_b128(u32x4{pk_bf16(ev[0], ev[1]), pk_bf16(ev[2], ev[3]), pk_bf16(ev[4], ev[5]), pk_bf16(ev[6], ev[7])}, rw,
                                               hoff + (q & 1) * 512, (q >> 1) * (2 * 56 * 1024), 16);
      };
#pragma unroll
      for (int q = 0; q < 8; ++q) {
        const int m = q & 1, n = q >> 1;
        SB();
        if (m == 0) cb[0] = qb[n];
        acc[m][n] = mfma3232(fr[0].aw[m], fr[0].bx[n], acc[m][n]); SB();
        if (q >= 2) { epi_c(q - 2); SB(); }
        acc[m][n] = mfma3232(ca[m], cb, acc[m][n]); SB();
        if (q >= 1) { epi_a(q - 1); SB(); }
        acc[m][n] = mfma3232(fr[1].aw[m], fr[1].bx[n], acc[m][n]); SB();
        if (q >= 1) { epi_b(q - 1); SB(); }
      }
      epi_c(6); SB(); epi_a(7); epi_b(7); epi_c(7);
      *(volatile LAS u32*)(sm + FS + 1056 + hw * 4) = tag;
      STAMP(10);
      wd_setup();
      WAITV(4);
      if (lane == 0) __builtin_amdgcn_raw_buffer_store_b32(tag, rw, WS_F2 + (mi * 2) * 256 + (fj * 4 + hw) * 4, 0, 16);
      WAITV(1);
      if (lane == 0) __builtin_amdgcn_raw_buffer_store_b32(tag, rw, WS_F2 + (mi * 2 + 1) * 256 + (fj * 4 + hw) * 4, 0, 16);
      STAMP(12);
      __builtin_amdgcn_s_setprio(HPRIO);
    } else {
      wd_setup();
      const int un = b >> 1, ua = un >= 7, uc = (b & 1) * 7 + (ua ? un - 7 : un);
      const u32x4 scs = {ua ? ka[8] : ka[6], ua ? ka[9] : ka[7], b < 28 ? 1835008u : 0u, 0x00020000u};
      const u32 vcs = (hw * 4 + t) * 3584 + uc * 256 + p16 * 16;
      u32x4 vv[16];
      f32x4 a0 = z4, a1 = z4;
      u32 sfa, sfb;
      u32 sel_lo = 0x00003f80u, sel_hi = 0x3f800000u;
      PIN(sel_lo); PIN(sel_hi);
      const unsigned long long t_start = __builtin_amdgcn_s_memrealtime();
#pragma unroll
      for (int half = 0; half < 2; ++half) {
        if (half == 1) { while (__builtin_amdgcn_s_memrealtime() - t_start < CS_DELAY) __builtin_amdgcn_s_sleep(4); }
#pragma unroll
        for (int k = 0; k < 16; ++k) ALD4S(vv[k], vcs, scs, (half * 16 + k) * 16 * 3584, "");
        if (half == 0) {
          const int q = 64 * g + 16 * hw + p16, ks = 8 * q + jp, km = (2048 - ks) & 2047, col = (t >> 1) * 256;
          const GAS unsigned short* wa = (t & 1) ? wi : wr;
          asm volatile("global_load_ushort %0, %1, off" : "=v"(sfa) : "v"(wa + ks * DF_ + col) : "memory");
          asm volatile("global_load_ushort %0, %1, off" : "=v"(sfb) : "v"(wa + km * DF_ + col) : "memory");
          tag = load_tag();
        }
        STAMP(23 + 3 * half);
        static_for<0, 4>([&](auto Q_) {
          constexpr int Q = decltype(Q_)::v;
          if (half == 0) WAITV(14 - 4 * Q); else WAITV(12 - 4 * Q);
#pragma unroll
          for (int k = 4 * Q; k < 4 * Q + 4; ++k) {
            PIN(vv[k]);
#pragma unroll
            for (int e = 0; e < 2; ++e) {
              const u32 p = vv[k][e], q = vv[k][2 + e];
              a0[2 * e] = __builtin_amdgcn_fdot2_f32_bf16(__builtin_bit_cast(b16x2, p), __builtin_bit_cast(b16x2, sel_lo), a0[2 * e], false);
              a0[2 * e + 1] = __builtin_amdgcn_fdot2_f32_bf16(__builtin_bit_cast(b16x2, p), __builtin_bit_cast(b16x2, sel_hi), a0[2 * e + 1], false);
              a1[2 * e] = __builtin_amdgcn_fdot2_f32_bf16(__builtin_bit_cast(b16x2, q), __builtin_bit_cast(b16x2, sel_lo), a1[2 * e], false);
              a1[2 * e + 1] = __builtin_amdgcn_fdot2_f32_bf16(__builtin_bit_cast(b16x2, q), __builtin_bit_cast(b16x2, sel_hi), a1[2 * e + 1], false);
            }
          }
        });
        PIN(a0); PIN(a1);
        STAMP(24 + 3 * half);
        if (half == 0) {
          WAITV(0);
          PIN(sfa); PIN(sfb);
          const float wh = 0.5f * ((t & 1) ? bf_lo(sfa) - bf_lo(sfb) : bf_lo(sfa) + bf_lo(sfb));
          const u32 wb = pk_bf16(wh, wh) & 0xffffu;
          auto s2 = __builtin_amdgcn_permlane16_swap(wb, wb, false, false);
          if ((t & 1) == 0) st_sc1_2(rw, WS_SF + jp * 4096 + (t >> 1) * 2048 + (64 * g + 4 * p16 + hw) * 8, u32x2{(u32)s2[0] | ((u32)s2[1] << 16), tag});
          STAMP(25);
        }
      }
      if (b < 28) {
#pragma unroll
        for (int k = 0; k < 4; ++k) { a0[k] = xrow_sum(a0[k]); a1[k] = xrow_sum(a1[k]); }
        const float s0 = t == 0 ? a0[0] : t == 1 ? a0[2] : t == 2 ? a1[0] : a1[2];
        const float s1 = t == 0 ? a0[1] : t == 1 ? a0[3] : t == 2 ? a1[1] : a1[3];
        st_sc1(rw, WS_CS + hw * (2 * F_ * 8) + (ua * 14 + uc) * 1024 + p16 * 64 + t * 16, u32x4{__builtin_bit_cast(u32, s0), tag, __builtin_bit_cast(u32, s1), tag});
      }
      STAMP(3);
    }
    {
      const int off = is_gemm ? FS + 1056 : FS + 1024;
#pragma nounroll
      for (int spin = 0; spin < SPIN_CAP; ++spin) { if (all_ok(*(volatile LAS u32*)(sm + off + (lane & 3) * 4) == tag)) break; __builtin_amdgcn_s_sleep(1); }
      asm volatile("" ::: "memory");
    }
    wd_issue(w, 0, 6); wd_issue(w, 1, 7); wd_issue(w, 2, 8);
    {
#pragma nounroll
      for (int spin = 0; spin < SPIN_CAP; ++spin) { if (all_ok(*(volatile LAS u32*)(sm + FS + 1024 + hw * 4) == tag)) break; __builtin_amdgcn_s_sleep(1); }
      asm volatile("" ::: "memory");
    }
    wd_issue(w, 6, 1);
    WAITV(4);
    wd_read(0, 6); wd_read(1, 7); wd_read(2, 8);
    asm volatile("s_waitcnt lgkmcnt(0)" ::: "memory");
    wd_issue(w, 3, 6); wd_issue(w, 4, 7); wd_issue(w, 5, 8);
    STAMP(13);
  } else if (is_gemm) helper(IC<1>{});
  else helper(IC<0>{});

  wait_h_and_load();
  __builtin_amdgcn_s_setprio(0);
#pragma unroll
  for (int i = 3; i < 7; ++i) {
    LAS char* p0 = sm + (w < 4 ? wslot(i == 6 ? 1 : i + 3) : wslot(i == 6 ? 0 : i));
#pragma unroll
    for (int u = 0; u < 4; ++u) {
      LAS char* p = p0 + (wbase ^ (u << 5));
      u32x2 lo = tr_read(p), hi = tr_read(p + 512);
      wf[i][u] = u32x4{lo[0], lo[1], hi[0], hi[1]};
    }
  }
  STAMP(16);
  const int oc = lane & 7, otok = (w & 1) * 8 + (lane >> 3);
  const int orow = rt * 64 + (((w >> 1) + ct) & 3) * 16 + otok;
  const u32 roff = WS_R + orow * 2048 + (ct * 8 + oc) * 16;
  const u32 soff_ = WS_ST + ((orow & 255) * 8 + (orow >> 8)) * 8;
  u32x4 r0, r1; u32x2 sto;
#pragma unroll
  for (int tt = 0; tt < 4; ++tt) {
    f32x4 acc[4] = {z4, z4, z4, z4};
#pragma unroll
    for (int ks = 0; ks < 7; ++ks) {
#pragma unroll
      for (int u = 0; u < 4; ++u) acc[u] = mfma32_bf16(wf[ks][u], hf[tt][ks], acc[u]);
      if (tt == 0) { SB(); hf[3][ks] = ld_h(3, ks); SB(); }
    }
    if (tt == 2) { r0 = ld_sc1(rw, roff); r1 = ld_sc1(rw, roff + 1024); sto = ld_sc1_2(rw, soff_); }
#pragma unroll
    for (int u = 0; u < 4; ++u) *(LAS f32x4*)(sm + (w < 4 ? wslot(tt < 3 ? 6 + tt : 1) : wslot(tt < 3 ? 3 + tt : 0)) + hw * 4096 + u * 1024 + t * 256 + ((p16 ^ (4 * u + (t & 2))) << 4)) = acc[u];
    SB();
  }
  STAMP(17);
  BAR();
  f32x4 sa = z4, sb = z4;
  {
    const int tt = w >> 1;
    const int o0 = (oc >> 1) * 1024 + (oc & 1) * 512 + ((otok ^ (2 * oc)) << 4);
    const int bm = (tt < 3 ? wslot(6) + tt * 16384 : wslot(1)) + o0, bh = (tt < 3 ? wslot(3) + tt * 16384 : wslot(0)) + o0;
#pragma unroll
    for (int pw = 0; pw < 4; ++pw) {
      sa += *(LAS f32x4*)(sm + bm + pw * 4096) + *(LAS f32x4*)(sm + bh + pw * 4096);
      sb += *(LAS f32x4*)(sm + bm + 256 + pw * 4096) + *(LAS f32x4*)(sm + bh + 256 + pw * 4096);
    }
  }
  STAMP(22);
#define CHK2 all_ok(tags_ok(r0, tag) & tags_ok(r1, tag) & (sto[1] == tag))
  if (__builtin_expect(!CHK2, 0)) {
#pragma nounroll
    for (int spin = 0; spin < SPIN_CAP; ++spin) {
      asm volatile("" ::: "memory");
      r0 = ld_sc1(rw, roff); r1 = ld_sc1(rw, roff + 1024); sto = ld_sc1_2(rw, soff_);
      if (CHK2) break;
    }
  }
  {
    const float rs = __builtin_amdgcn_rcpf(bf_hi(sto[0])), rs2 = rs * rs;
    *(GAS u32x4*)(out + (size_t)orow * 1024 + ct * 128 + oc * 16) =
        u32x4{pk_bf16(__builtin_fmaf(sa[0], rs2, bf_lo(r0[0])), __builtin_fmaf(sa[1], rs2, bf_hi(r0[0]))),
              pk_bf16(__builtin_fmaf(sa[2], rs2, bf_lo(r0[2])), __builtin_fmaf(sa[3], rs2, bf_hi(r0[2]))),
              pk_bf16(__builtin_fmaf(sb[0], rs2, bf_lo(r1[0])), __builtin_fmaf(sb[1], rs2, bf_hi(r1[0]))),
              pk_bf16(__builtin_fmaf(sb[2], rs2, bf_lo(r1[2])), __builtin_fmaf(sb[3], rs2, bf_hi(r1[2])))};
  }
  if (b == 0 && tid == 0) __builtin_amdgcn_raw_buffer_store_b32(tag, rw, WS_EPOCH, 0, 16);
  STAMP(18);
  STAMP_DUMP();
}

#if defined(__HIP_DEVICE_COMPILE__)
#pragma clang attribute pop
#endif

extern "C" void kernel_launch(void* const* d_in, const int* in_sizes, int n_in, void* d_out, int out_size, void* d_ws, size_t ws_size,
                              hipStream_t stream) {
  (void)hipFuncSetAttribute((const void*)fused_kernel, hipFuncAttributeMaxDynamicSharedMemorySize, LDS_BYTES);
  fused_kernel<<<256, 512, LDS_BYTES, stream>>>((const char*)d_in[0], (const unsigned short*)d_in[1], (const unsigned short*)d_in[2], (const char*)d_in[4],
                                                (const char*)d_in[5], (const char*)d_in[6], (char*)d_ws, (char*)d_out);
}
```

```cpp
#include <hip/hip_runtime.h>
#include <stdint.h>

#define LAS __attribute__((address_space(3)))
#define GAS __attribute__((address_space(1)))
#define DI __device__ __forceinline__
typedef unsigned u32;
typedef unsigned u32x2 __attribute__((ext_vector_type(2)));
typedef unsigned u32x4 __attribute__((ext_vector_type(4)));
typedef float f32x2 __attribute__((ext_vector_type(2)));
typedef float f32x4 __attribute__((ext_vector_type(4)));
typedef float f32x16 __attribute__((ext_vector_type(16)));
typedef short s16x4 __attribute__((ext_vector_type(4)));
typedef _Float16 h16x2 __attribute__((ext_vector_type(2)));
typedef _Float16 h16x4 __attribute__((ext_vector_type(4)));
typedef _Float16 h16x8 __attribute__((ext_vector_type(8)));
typedef __bf16 b16x2 __attribute__((ext_vector_type(2)));
typedef __bf16 b16x8 __attribute__((ext_vector_type(8)));
typedef __amdgpu_buffer_rsrc_t rsrc_t;

constexpr int F_ = 1792, DF_ = 257;
constexpr u32 WS_EPOCH = 0;
constexpr u32 WS_F2 = 4096;
constexpr u32 WS_ST = 16384;
constexpr u32 WS_CS = WS_ST + 16384;
constexpr u32 WS_U = 262144;
constexpr u32 WS_V = WS_U + 4194304;
constexpr u32 WS_R = WS_V + 4194304;
constexpr u32 WS_ST2 = 229376;
constexpr u32 WS_SF = 196608;
constexpr u32 WS_H = WS_R + 4194304;
constexpr u32 WS_END = WS_H + 7340032;
constexpr int LDS_BYTES = 163840;
constexpr int G1_STAGE = 49152;
constexpr int FS = 3 * G1_STAGE;
#define SPIN_CAP (1 << 17)
constexpr int CODE_LINES = 292;
#define HPRIO 2
#define S1PRE 2
#define POLLSLEEP 2
#define CS_DELAY 380
#define WARM 0
#define UPOS 0
#define TV 2
#define V_DELAY 130
#define HW_EARLY 1
#define ROTM 0
constexpr float XS = 1.0f / 16;
constexpr float FSC = (2.0f / (2048.0f * 512.0f)) * 16.0f;
constexpr size_t WS_DBG = 64 << 20;
#define STAMP(k) do {} while (0)
#define STAMP_DECL
#define STAMP_DUMP() do {} while (0)

#if defined(__HIP_DEVICE_COMPILE__)
#pragma clang attribute push(__attribute__((target("no-packed-fp32-ops"))), apply_to = function)
#endif
DI u32 pk_f16(float a, float b) { h16x2 p = {(_Float16)a, (_Float16)b}; return __builtin_bit_cast(u32, p); }
DI u32 pk_bf16(float a, float b) { b16x2 p = {(__bf16)a, (__bf16)b}; return __builtin_bit_cast(u32, p); }
DI float bf_lo(u32 v) { return __builtin_bit_cast(float, v << 16); }
DI float bf_hi(u32 v) { return __builtin_bit_cast(float, v & 0xffff0000u); }
DI float rnd_bf(float a) { return bf_lo(pk_bf16(a, a)); }
DI float relu(float a) { int i = __builtin_bit_cast(int, a); return __builtin_bit_cast(float, i > 0 ? i : 0); }
DI float cosr(float rev) { return __builtin_amdgcn_cosf(rev); }
DI float sinr(float rev) { return __builtin_amdgcn_sinf(rev); }
DI f32x4 mfma16_f16(u32x2 a, u32x2 b, f32x4 c) { return __builtin_amdgcn_mfma_f32_16x16x16f16(__builtin_bit_cast(h16x4, a), __builtin_bit_cast(h16x4, b), c, 0, 0, 0); }
DI f32x4 mfma16_bf16(u32x2 a, u32x2 b, f32x4 c) { return __builtin_amdgcn_mfma_f32_16x16x16bf16_1k(__builtin_bit_cast(s16x4, a), __builtin_bit_cast(s16x4, b), c, 0, 0, 0); }
DI f32x4 mfma32_f16(u32x4 a, u32x4 b, f32x4 c) { return __builtin_amdgcn_mfma_f32_16x16x32_f16(__builtin_bit_cast(h16x8, a), __builtin_bit_cast(h16x8, b), c, 0, 0, 0); }
DI f32x4 mfma32_bf16(u32x4 a, u32x4 b, f32x4 c) { return __builtin_amdgcn_mfma_f32_16x16x32_bf16(__builtin_bit_cast(b16x8, a), __builtin_bit_cast(b16x8, b), c, 0, 0, 0); }
DI f32x16 mfma3232(u32x4 a, u32x4 b, f32x16 c) { return __builtin_amdgcn_mfma_f32_32x32x16_bf16(__builtin_bit_cast(b16x8, a), __builtin_bit_cast(b16x8, b), c, 0, 0, 0); }
DI u32x2 tr_read(LAS char* p) { return __builtin_bit_cast(u32x2, __builtin_amdgcn_ds_read_tr16_b64_v4i16((LAS s16x4*)p)); }
#define SB() __builtin_amdgcn_sched_barrier(0)
#define BAR() asm volatile("s_waitcnt lgkmcnt(0)\n\ts_barrier" ::: "memory")
#define WAITV(n) asm volatile("s_waitcnt vmcnt(%0)" :: "i"(n) : "memory")
#define PIN(x) asm volatile("" : "+v"(x))
DI u32x4 mk_srd(const void* p, u32 bytes) { uint64_t a = (uint64_t)p; return u32x4{(u32)a, (u32)(a >> 32), bytes, 0x00020000u}; }
DI void glds16(u32 ldsbase, int imm, u32 voff, u32x4 srd, u32 soff) {
  asm volatile("s_add_u32 m0, %0, %1\n\ts_nop 0\n\tbuffer_load_dwordx4 %2, %3, %4 offen lds" :: "s"(ldsbase), "i"(imm), "v"(voff), "s"(srd), "s"(soff) : "memory", "scc");
}
DI void glds4(u32 ldsaddr, u32 voff, u32x4 srd, u32 soff) {
  asm volatile("s_mov_b32 m0, %0\n\ts_nop 0\n\tbuffer_load_dword %1, %2, %3 offen lds" :: "s"(ldsaddr), "v"(voff), "s"(srd), "s"(soff) : "memory");
}
#define ALD4(dst, voff, srd, IMM, FL) asm volatile("buffer_load_dwordx4 %0, %1, %2, 0 offen offset:" #IMM FL : "=v"(dst) : "v"(voff), "s"(srd) : "memory")
#define ALD2(dst, voff, srd, IMM, FL) asm volatile("buffer_load_dwordx2 %0, %1, %2, 0 offen offset:" #IMM FL : "=v"(dst) : "v"(voff), "s"(srd) : "memory")
#define ALD1(dst, voff, srd, IMM, FL) asm volatile("buffer_load_dword %0, %1, %2, 0 offen offset:" #IMM FL : "=v"(dst) : "v"(voff), "s"(srd) : "memory")
DI u32x4 ld_sc1(rsrc_t r, u32 off) { return __builtin_amdgcn_raw_buffer_load_b128(r, off, 0, 16); }
DI u32x2 ld_sc1_2(rsrc_t r, u32 off) { return __builtin_amdgcn_raw_buffer_load_b64(r, off, 0, 16); }
DI void st_sc1(rsrc_t r, u32 off, u32x4 v) { __builtin_amdgcn_raw_buffer_store_b128(v, r, off, 0, 16); }
DI void st_sc1_2(rsrc_t r, u32 off, u32x2 v) { __builtin_amdgcn_raw_buffer_store_b64(v, r, off, 0, 16); }
DI bool all_ok(bool ok) { return __builtin_amdgcn_ballot_w64(!ok) == 0; }
DI bool tags_ok(u32x4 a, u32 tag) { return (a[1] == tag) & (a[3] == tag); }

template <int CTRL> DI float dpp_add(float v) {
  int o = __builtin_amdgcn_update_dpp(0, __builtin_bit_cast(int, v), CTRL, 0xf, 0xf, true);
  return v + __builtin_bit_cast(float, o);
}
DI float row_sum(float v) { v = dpp_add<0xB1>(v); v = dpp_add<0x4E>(v); v = dpp_add<0x141>(v); return dpp_add<0x140>(v); }
DI float xrow_sum(float v) {
  u32 u = __builtin_bit_cast(u32, v);
  auto a = __builtin_amdgcn_permlane16_swap(u, u, false, false);
  v = __builtin_bit_cast(float, (u32)a[0]) + __builtin_bit_cast(float, (u32)a[1]);
  u = __builtin_bit_cast(u32, v);
  auto b = __builtin_amdgcn_permlane32_swap(u, u, false, false);
  return __builtin_bit_cast(float, (u32)b[0]) + __builtin_bit_cast(float, (u32)b[1]);
}
template <bool NEG, bool BF> DI u32x4 cmul_pack(f32x4 re, f32x4 im, f32x4 c, f32x4 s) {
  f32x4 a = NEG ? re * c + im * s : re * c - im * s;
  f32x4 b = NEG ? im * c - re * s : im * c + re * s;
  u32x4 o;
#pragma unroll
  for (int r = 0; r < 4; ++r) o[r] = BF ? pk_bf16(a[r], b[r]) : pk_f16(a[r], b[r]);
  return o;
}
DI u32 rot16(u32 v) { return __builtin_amdgcn_alignbit(v, v, 16); }
template <bool NEG> DI void cmul_half(f32x4& a, f32x4& b, f32x4 re, f32x4 im, f32x4 c, f32x4 s) {
  a = NEG ? re * c + im * s : re * c - im * s;
  b = NEG ? im * c - re * s : im * c + re * s;
}
template <bool BF> DI u32x4 pack4(f32x4 a, f32x4 b) {
  u32x4 o;
#pragma unroll
  for (int r = 0; r < 4; ++r) o[r] = BF ? pk_bf16(a[r], b[r]) : pk_f16(a[r], b[r]);
  return o;
}
DI void glds_a(u32 ldsbase, int imm, u32 v0, u32 v1, u32x4 srd, u32 soff) {
  asm volatile("s_add_u32 m0, %0, %1\n\ts_nop 0\n\tbuffer_load_dwordx4 %2, %4, %5 offen lds\n\tbuffer_load_dwordx4 %3, %4, %5 offen offset:1024 lds"
               :: "s"(ldsbase), "i"(imm), "v"(v0), "v"(v1), "s"(srd), "s"(soff) : "memory", "scc");
}
DI void glds_b(u32 v2, u32 v3, u32x4 srd, u32 soff) {
  asm volatile("buffer_load_dwordx4 %0, %2, %3 offen offset:2048 lds\n\tbuffer_load_dwordx4 %1, %2, %3 offen offset:3072 lds"
               :: "v"(v2), "v"(v3), "s"(srd), "s"(soff) : "memory");
}
DI u32x4 cmulc_f16(f32x4 re, f32x4 im, u32x4 tw) {
  u32 z0 = pk_f16(re[0], im[0]), z1 = pk_f16(re[1], im[1]), z2 = pk_f16(re[2], im[2]), z3 = pk_f16(re[3], im[3]);
  u32 o0, o1, o2, o3;
  const u32 t0 = tw[0], t1 = tw[1], t2 = tw[2], t3 = tw[3];
#define PKM " op_sel_hi:[1,0]\n\t"
#define PKF " op_sel:[1,1,0] op_sel_hi:[0,1,1] neg_hi:[0,1,0]\n\t"
  asm("v_pk_mul_f16 %0, %4, %8" PKM "v_pk_mul_f16 %1, %5, %9" PKM "v_pk_mul_f16 %2, %6, %10" PKM "v_pk_mul_f16 %3, %7, %11" PKM
      "v_pk_fma_f16 %0, %4, %8, %0" PKF "v_pk_fma_f16 %1, %5, %9, %1" PKF "v_pk_fma_f16 %2, %6, %10, %2" PKF "v_pk_fma_f16 %3, %7, %11, %3" PKF "s_nop 1"
      : "=&v"(o0), "=&v"(o1), "=&v"(o2), "=&v"(o3) : "v"(z0), "v"(z1), "v"(z2), "v"(z3), "v"(t0), "v"(t1), "v"(t2), "v"(t3));
  return u32x4{o0, o1, o2, o3};
}
template <int N> struct IC { static constexpr int v = N; };
template <int A, int B, class Fn> DI void static_for(Fn&& f) { if constexpr (A < B) { f(IC<A>{}); static_for<A + 1, B>(f); } }

struct Frag { u32x4 bx[4], aw[2]; };
#define ALD4S(dst, voff, srd, SOFF, FL) asm volatile("buffer_load_dwordx4 %0, %1, %2, %3 offen" FL : "=v"(dst) : "v"(voff), "s"(srd), "s"(SOFF) : "memory")
#define ALD2S(dst, voff, srd, SOFF, FL) asm volatile("buffer_load_dwordx2 %0, %1, %2, %3 offen" FL : "=v"(dst) : "v"(voff), "s"(srd), "s"(SOFF) : "memory")
#define RAWBAR() do { asm volatile("s_barrier" ::: "memory"); } while (0)

typedef unsigned u32x16 __attribute__((ext_vector_type(16)));
__global__ __launch_bounds__(512, 2) void fused_kernel(const char* a_x, const unsigned short* a_wr, const unsigned short* a_wi, const char* a_wg,
                                                       const char* a_wu, const char* a_wd, char* a_ws, char* a_out) {
  extern __shared__ __attribute__((aligned(16))) char smem[];
  LAS char* sm = (LAS char*)smem;
  STAMP_DECL
  STAMP(0);
  u32x16 ka;
  asm volatile("s_load_dwordx16 %0, %1, 0x0" : "=s"(ka) : "s"(__builtin_amdgcn_kernarg_segment_ptr()) : "memory");
  const int b = blockIdx.x, tid = threadIdx.x, lane = tid & 63, w = __builtin_amdgcn_readfirstlane(tid >> 6);
  const int hw = w & 3, p16 = lane & 15, t = lane >> 4;
  const u32 lds0 = (u32)(uintptr_t)sm;
  {
    uint64_t pc; asm volatile("s_getpc_b64 %0" : "=s"(pc));
    asm volatile("s_cmp_gt_u32 %0, 7\n\ts_cbranch_scc1 1f\n\ts_mov_b32 m0, %1\n\ts_nop 0\n\tbuffer_load_dword %2, %3, 0 offen lds\n1:"
                 :: "s"(b), "s"(lds0), "v"(tid * 128), "s"(u32x4{(u32)pc, (u32)(pc >> 32), CODE_LINES * 128, 0x00020000u}) : "memory", "scc");
  }
  const bool is_gemm = b >= 32;
  const int xc = b & 7, r_ = (b - 32) >> 3;
  const int mi = (xc >> 1) * 4 + (r_ & 3), fj = (xc & 1) * 7 + (r_ >> 2);
  const int rot = ((r_ & 3) + (r_ >> 2)) & ROTM;
  const u32 lw = lds0 + hw * 4096;
  u32 vx[4], vg[4], vu[4];
#pragma unroll
  for (int j = 0; j < 4; ++j) {
    vx[j] = (mi * 128 + hw * 32 + j * 7 + (lane >> 3)) * 1024 + (((lane & 7) ^ ((4 * j + t) & 7)) << 4);
    vg[j] = (hw * 16 + j * 4 + t) * 3584 - j * 1024 + fj * 256 + ((p16 ^ (2 * t)) << 4);
    vu[j] = vg[j] ^ 128;
    PIN(vx[j]); PIN(vg[j]); PIN(vu[j]);
  }
  u32 vrow = (b + 256 * hw) * 1024 + lane * 16;
  asm volatile("s_cmp_lt_u32 %0, 4\n\ts_cbranch_scc1 1f\n\ts_setprio 2\n1:" :: "s"(w) : "scc");
  if (w < 4) vrow = 0x7ff00000u;
  const u32 lrow = w < 4 ? lw : lds0 + FS + hw * 1024;
  PIN(vrow);
  SB();
  asm volatile("s_waitcnt lgkmcnt(0)" : "+s"(ka) :: "memory");
#define KARG(i) (((uint64_t)ka[2 * (i) + 1] << 32) | ka[2 * (i)])
  const u32x4 sx = {ka[0], ka[1], 2097152u, 0x00020000u}, sg = {ka[6], ka[7], 1835008u, 0x00020000u}, su = {ka[8], ka[9], 1835008u, 0x00020000u};
  auto g1_issue = [&](int st, int k) {
    const int lb = (st % 3) * G1_STAGE + (k >> 1) * 16384, sk = st ^ rot;
    if (k == 0) glds_a(lw, lb, vx[0], vx[1], sx, sk * 128);
    if (k == 1) glds_b(vx[2], vx[3], sx, sk * 128);
    if (k == 2) glds_a(lw, lb, vg[0], vg[1], sg, sk * 64 * 3584);
    if (k == 3) glds_b(vg[2], vg[3], sg, sk * 64 * 3584);
    if (k == 4) glds_a(lw, lb, vu[0], vu[1], su, sk * 64 * 3584);
    if (k == 5) glds_b(vu[2], vu[3], su, sk * 64 * 3584);
  };
  glds16(lrow, 0, vrow, sx, 0);
  glds16(lrow + (w < 4 ? 0 : 4096), 0, vrow, sx, 1048576);
  if (w < 4 && is_gemm) {
#pragma unroll
    for (int k = 0; k < 6; ++k) g1_issue(0, k);
  }
  SB();
  STAMP(30);
  const f32x4 z4 = {0.f, 0.f, 0.f, 0.f};
  const GAS unsigned short* wr = (const GAS unsigned short*)KARG(1); const GAS unsigned short* wi = (const GAS unsigned short*)KARG(2);
  GAS char* ws = (GAS char*)KARG(6); GAS char* out = (GAS char*)KARG(7);
  auto load_tag = [&]() {
    u32 epoch;
    asm volatile("s_load_dword %0, %1, 0x0\n\ts_waitcnt lgkmcnt(0)" : "=s"(epoch) : "s"(ws) : "memory");
    return epoch * 0x9E3779B1u + 0x7F4A7C15u;
  };
  const rsrc_t rw = __builtin_amdgcn_make_buffer_rsrc((void*)ws, 0, WS_END, 0x00020000);
  const int jp = b & 7, g = b >> 3;
  const int rt = (b & 7) * 4 + ((b >> 3) & 3), ct = b >> 5;
  auto wslot = [&](int s) { return (s < 3 ? 2 * G1_STAGE : s < 6 ? -3 * 16384 : G1_STAGE - 6 * 16384) + s * 16384; };
  u32x4 wf[7][4];
  const int wbase = hw * 4096 + t * 1024 + (p16 >> 2) * 128 + (p16 & 3) * 8 + ((((p16 >> 3) | ((t & 1) << 1))) << 5);
  auto wd_read = [&](int ks, int s) {
#pragma unroll
    for (int u = 0; u < 4; ++u) {
      LAS char* p = sm + wslot(s) + (wbase ^ (u << 5));
      u32x2 lo = tr_read(p), hi = tr_read(p + 512);
      wf[ks][u] = u32x4{lo[0], lo[1], hi[0], hi[1]};
    }
  };
  u32 tag;
  auto ksof = [&](int i) { int k = i + 2 * (rt & 3); return k >= 7 ? k - 7 : k; };
  const u32 hs = WS_H + ((rt * 4) * 56 + w * 7) * 1024 + lane * 16;
  auto ld_h = [&](int j, int i) { return __builtin_amdgcn_raw_buffer_load_b128(rw, hs, (((j + ct) & 3) * 56 + ksof(i)) * 1024, 16); };
  u32x4 hf[4][7];
  const u32x4 sd_ = {ka[10], ka[11], 1835008u, 0x00020000u};
  u32 vwd[4];
  auto wd_setup = [&]() {
#pragma unroll
    for (int j = 0; j < 4; ++j) { vwd[j] = (j * 7 + (lane >> 3)) * 1024 + ct * 128 + (((lane & 7) ^ (((lane >> 4) & 1) << 1) ^ ((j & 1) << 2)) << 4); PIN(vwd[j]); }
  };
  auto wd_issue = [&](int kw, int i, int s) {
    const u32 so = kw * (224 * 1024) + ksof(i) * 32768;
    SB(); glds_a(lw, wslot(s), vwd[0], vwd[1], sd_, so); glds_b(vwd[2], vwd[3], sd_, so); SB();
  };
  auto wait_h_and_load = [&]() {
    const u32 foff = WS_F2 + rt * 256 + (w * 7 + (lane < 7 ? lane : 0)) * 4;
#pragma nounroll
    for (int spin = 0; spin < SPIN_CAP; ++spin) {
      asm volatile("" ::: "memory");
      u32 fl = __builtin_amdgcn_raw_buffer_load_b32(rw, foff, 0, 16);
      if (all_ok(fl == tag)) break;
      __builtin_amdgcn_s_sleep(POLLSLEEP);
    }
    __builtin_amdgcn_fence(__ATOMIC_ACQUIRE, "wavefront");
    asm volatile("" ::: "memory");
    STAMP(15);
#pragma unroll
    for (int tt = 0; tt < 3; ++tt)
#pragma unroll
      for (int i = 0; i < 7; ++i) hf[tt][i] = ld_h(tt, i);
    SB();
  };
  const float pf = (float)p16, tf = (float)t;

  auto helper = [&](auto G_) {
    constexpr bool G = decltype(G_)::v;
    const int ht = tid & 255;
    const u32x4 sw = {ka[12], ka[13], WS_END, 0x00020000u};
    const u32x4 sfr_ = {ka[2], ka[3], 2048 * DF_ * 2, 0x00020000u}, sfi_ = {ka[4], ka[5], 2048 * DF_ * 2, 0x00020000u};
    SB();
#define DMA(st, k) do { if (G) { SB(); g1_issue(st, k); SB(); } } while (0)
#define GWAIT(n) do { if (G) WAITV(n); } while (0)
#define WAITG(n) do { if (G) WAITV(n); else WAITV(0); } while (0)
#define GBAR() do { if (G) RAWBAR(); } while (0)
    auto hsync = [&](int off, int stride = 0) {
      asm volatile("s_waitcnt lgkmcnt(0)" ::: "memory");
      *(volatile LAS u32*)(sm + off + (lane & 3) * stride + hw * 4) = tag;
#pragma nounroll
      for (int spin = 0; spin < SPIN_CAP; ++spin) { if (all_ok(*(volatile LAS u32*)(sm + off + hw * stride + (lane & 3) * 4) == tag)) break; }
      asm volatile("" ::: "memory");
    };
#define XBAR(n) do { if (G) BAR(); else hsync(G1_STAGE + (n) * 16); } while (0)
    u32x4 b2re[2], b2im[2], tw[2], ac2re, ac2im, bc3re, bc3im, qa8, qa8i, bc1re, bc1im, ac4re, ac4im, t1, bi1re[2], bi1im[2];
    u32x2 b1re, b1im;
    f32x4 t2c, t2s, t4c[2], t4s[2];
    {
      const float d5 = pf * (1.0f / 512), a5 = 4.0f * tf * d5;
#pragma unroll
      for (int r = 0; r < 4; ++r) {
        const float th = a5 + r * d5, c = cosr(th * 32.0f), s = sinr(th * 32.0f);
        ac2re[r] = pk_f16(c, s); ac2im[r] = rot16(ac2re[r]) ^ 0x8000u;
        bc3im[r] = pk_bf16(s, c); bc3re[r] = rot16(bc3im[r]) ^ 0x80000000u;
#pragma unroll
        for (int hh = 0; hh < 2; ++hh) {
          const float tq = th + hh * (16.0f * d5);
          tw[hh][r] = pk_f16(cosr(tq), sinr(tq));
          b2re[hh][r] = pk_f16(cosr(tq * 16.0f), sinr(tq * 16.0f)); b2im[hh][r] = rot16(b2re[hh][r]) ^ 0x8000u;
        }
      }
      b1re = u32x2{__builtin_amdgcn_perm(ac2re[1], ac2re[0], 0x05040100u), __builtin_amdgcn_perm(ac2re[3], ac2re[2], 0x05040100u)};
      b1im = u32x2{__builtin_amdgcn_perm(ac2im[1], ac2im[0], 0x05040100u), __builtin_amdgcn_perm(ac2im[3], ac2im[2], 0x05040100u)};
      const int po = p16 & 1, j8 = p16 >> 1;
      const float j8f = (float)j8 * 0.125f;
      const float ra = 2.0f * tf * j8f, rb = ra + j8f;
      const float ca = cosr(ra), sa = sinr(ra), cb = cosr(rb), sb = sinr(rb);
      const u32 fa0 = pk_f16(ca, sa), fb0 = pk_f16(cb, sb);
      const u32 fa = po ? (rot16(fa0) ^ 0x8000u) : fa0, fb = po ? (rot16(fb0) ^ 0x8000u) : fb0;
      const u32 sel = (j8 & 3) == 0 ? 0x03020100u : (j8 < 4 ? 0x0c0c0100u : 0x01000c0cu);
      qa8 = u32x4{fa, fb, __builtin_amdgcn_perm(0u, fa, sel), __builtin_amdgcn_perm(0u, fb, sel)};
      const u32 ia = pk_bf16(sa, ca), ib = pk_bf16(sb, cb), pa = rot16(ia) ^ 0x80000000u, pb = rot16(ib) ^ 0x80000000u;
      const bool on = (po == 0) ? (t < 2) : (t >= 2);
      const u32 hc = pk_bf16(0.5f * ca, 0.f);
      qa8i = u32x4{po ? ia : pa, po ? ib : pb, (t & 1) == 0 ? (po ? hc << 16 : hc) : (on ? pa : 0u), on ? pb : 0u};
      PIN(ac2re); PIN(ac2im); PIN(bc3re); PIN(bc3im); PIN(tw[0]); PIN(tw[1]); PIN(b2re[0]); PIN(b2re[1]); PIN(b2im[0]); PIN(b2im[1]); PIN(b1re); PIN(b1im); PIN(qa8); PIN(qa8i);
    }
    SB();
    tag = load_tag();
    STAMP(21);
    LAS char* const img = sm + FS + hw * 1024;
    u32 stat[2];
    u32x4 xr[2];
    WAITV(0);
    STAMP(1);
    {
      const u32 vo = (8 * b + 2 * hw) * DF_ * 2 + (lane >> 5) * (DF_ * 2 + 2) + (lane & 31) * 16;
      glds16(lds0 + hw * 1024, FS + 8192, vo, sfr_, 0); glds16(lds0 + hw * 1024, FS + 12288, vo, sfi_, 0);
    }
    auto tpat = [&](int q, int u) { const u32 r = tag >> (1 + 7 * q + 3 * u); return (tag & 1u) | ((r & 3u) << 1) | ((r & 28u) << 14); };
    {
      float v[2][8], mean[2], var[2];
#pragma unroll
      for (int a = 0; a < 2; ++a) {
        const u32x4 xv = *(LAS u32x4*)(img + a * 4096 + (4 * p16 + t) * 16);
        xr[a] = xv;
#pragma unroll
        for (int k = 0; k < 4; ++k) { v[a][2 * k] = bf_lo(xv[k]); v[a][2 * k + 1] = bf_hi(xv[k]); }
      }
#pragma unroll
      for (int a = 0; a < 2; ++a) mean[a] = xrow_sum(row_sum(((v[a][0] + v[a][1]) + (v[a][2] + v[a][3])) + ((v[a][4] + v[a][5]) + (v[a][6] + v[a][7])))) * (1.0f / 512);
#pragma unroll
      for (int a = 0; a < 2; ++a) {
        float q0 = 0.f, q1 = 0.f;
#pragma unroll
        for (int k = 0; k < 4; ++k) { float d = v[a][k] - mean[a], e = v[a][k + 4] - mean[a]; q0 += d * d; q1 += e * e; }
        var[a] = xrow_sum(row_sum(q0 + q1)) * (1.0f / 512);
      }
#pragma unroll
      for (int a = 0; a < 2; ++a) {
        stat[a] = pk_bf16(mean[a], __builtin_amdgcn_sqrtf(rnd_bf(rnd_bf(var[a]) + 1e-5f)));
        const float sc = __builtin_amdgcn_rcpf(bf_hi(stat[a])) * XS, of = -bf_lo(stat[a]) * sc;
        u32x4 xh;
#pragma unroll
        for (int k = 0; k < 4; ++k) xh[k] = pk_f16(__builtin_fmaf(v[a][2 * k], sc, of), __builtin_fmaf(v[a][2 * k + 1], sc, of));
        *(LAS u32x4*)(img + a * 4096 + (((4 * p16 + t) ^ ((p16 >> 1) & 2)) << 4)) = xh;
      }
      asm volatile("" ::: "memory");
      STAMP(23);
#pragma unroll
      for (int k = 0; k < 6; ++k) DMA(2, k);
      u32x2 tra[2][2];
#pragma unroll
      for (int a = 0; a < 2; ++a)
#pragma unroll
        for (int hh = 0; hh < 2; ++hh) tra[a][hh] = tr_read(img + a * 4096 + (4 * t + (p16 >> 2)) * 64 + ((hh ^ (t & 1)) << 5) + (p16 & 3) * 8);
      f32x4 d1re[2][2], d1im[2][2], d2re[2], d2im[2];
#pragma unroll
      for (int a = 0; a < 2; ++a)
#pragma unroll
        for (int hh = 0; hh < 2; ++hh) { d1re[a][hh] = mfma16_f16(tra[a][hh], b1re, z4); d1im[a][hh] = mfma16_f16(tra[a][hh], b1im, z4); }
#pragma unroll
      for (int a = 0; a < 2; ++a)
#pragma unroll
        for (int hh = 0; hh < 2; ++hh) {
          u32x4 ta = cmulc_f16(d1re[a][hh], d1im[a][hh], tw[hh]);
          d2re[a] = mfma32_f16(ta, b2re[hh], hh ? d2re[a] : z4); d2im[a] = mfma32_f16(ta, b2im[hh], hh ? d2im[a] : z4);
        }
      float ny[2];
#pragma unroll
      for (int a = 0; a < 2; ++a) { f32x4 q = d1re[a][0] + d1re[a][1]; ny[a] = xrow_sum((q[0] - q[1]) + (q[2] - q[3])); }
      int e = 4 * p16 + t; e ^= (e >> 3) & 3;
#pragma unroll
      for (int a = 0; a < 2; ++a) {
        if (lane == 0) d2im[a][0] = ny[a];
        *(LAS u32x4*)(img + a * 4096 + e * 16) = pack4<false>(d2re[a], d2im[a]);
      }
    }
    STAMP(24);
    WAITG(12);
    XBAR(0);
    STAMP(2);
#pragma unroll
    for (int k = 0; k < 6; ++k) DMA(3, k);
    u32x4 uo[2][2];
    {
      u32x2 e0[2], e1[2];
#pragma unroll
      for (int k = 0; k < 2; ++k) {
        int e = 16 * hw + 8 * k + (p16 >> 1); e ^= (e >> 3) & 3;
        e0[k] = *(LAS u32x2*)(sm + FS + (2 * t) * 1024 + e * 16 + (p16 & 1) * 8);
        e1[k] = *(LAS u32x2*)(sm + FS + (2 * t + 1) * 1024 + e * 16 + (p16 & 1) * 8);
      }
      u32 wv[2][2][2];
      {
        LAS char* fp = sm + FS + 8192 + t * 1024 + hw * 128 + p16 * 4;
#pragma unroll
        for (int hf = 0; hf < 2; ++hf)
#pragma unroll
          for (int k = 0; k < 2; ++k)
#pragma unroll
            for (int u = 0; u < 2; ++u) {
              LAS char* q = fp + hf * 510 + k * 64 + u * 2;
              wv[hf][k][u] = (u32)*(LAS unsigned short*)q | ((u32)*(LAS unsigned short*)(q + 4096) << 16);
            }
      }
      f32x4 d8[4];
#pragma unroll
      for (int u = 0; u < 4; ++u) d8[u] = mfma16_f16(u32x2{qa8[0], qa8[1]}, u32x2{e0[u >> 1][u & 1], e1[u >> 1][u & 1]}, z4);
      f32x4 d8s = mfma16_f16(u32x2{qa8[2], qa8[3]}, u32x2{e0[0][0], e1[0][0]}, z4);
      if ((hw | p16) == 0) d8[0] = d8s;
      const u32 p0 = tpat(p16 & 3, 0), p1 = tpat(p16 & 3, 1);
#pragma unroll
      for (int hf = 0; hf < 2; ++hf)
#pragma unroll
        for (int k = 0; k < 2; ++k) {
          uo[hf][k] = u32x4{(pk_f16(d8[2 * k][2 * hf], d8[2 * k][2 * hf + 1]) & 0xfff8fff8u) | p0, wv[hf][k][0],
                            (pk_f16(d8[2 * k + 1][2 * hf], d8[2 * k + 1][2 * hf + 1]) & 0xfff8fff8u) | p1, wv[hf][k][1]};
          PIN(uo[hf][k]);
        }
    }
    auto ustore = [&]() {
      const u32 ub = WS_U + b * 16384 + (2 * t) * 2048 + hw * 512 + p16 * 16;
#pragma unroll
      for (int hf = 0; hf < 2; ++hf)
#pragma unroll
        for (int k = 0; k < 2; ++k) st_sc1(rw, ub + hf * 2048 + k * 256, uo[hf][k]);
      if (lane < 2) st_sc1_2(rw, WS_ST + b * 64 + hw * 8 + lane * 32, u32x2{lane ? stat[1] : stat[0], tag});
      STAMP(19);
    };
    ustore();
    GWAIT(12 + 5);
    GBAR();
    STAMP(3);
    {
      const float jf = (float)jp;
      const float d1 = (8.0f * pf + jf) * (1.0f / 2048), a1 = 4.0f * tf * d1, d2 = pf * (1.0f / 256), a2 = (32.0f * tf + jf) * (pf * (1.0f / 2048));
      DMA(4, 0); DMA(4, 1);
#pragma unroll
      for (int r = 0; r < 4; ++r) {
        DMA(4, 2 + r);
        const float n1 = a1 + r * d1, n2 = a2 + r * d2;
        bc1re[r] = pk_f16(cosr(n1 * 16.0f), sinr(n1 * 16.0f)); bc1im[r] = rot16(bc1re[r]) ^ 0x8000u;
        t1[r] = pk_f16(cosr(n1), sinr(n1));
        ac4im[r] = pk_bf16(sinr(n2 * 16.0f), cosr(n2 * 16.0f)); ac4re[r] = rot16(ac4im[r]) ^ 0x80000000u;
        t2c[r] = cosr(n2); t2s[r] = sinr(n2);
      }
      PIN(bc1re); PIN(bc1im); PIN(ac4re); PIN(ac4im); PIN(t1); PIN(t2c); PIN(t2s);
    }
    u32x4 ug[4];
    const int uq = ht & 3;
    const int ui = (((ht >> 2) & 3) << 4) + (ht >> 4);
    const u32 uoff = WS_U + ui * 16384 + jp * 2048 + g * 64 + uq * 16;
    auto caddr = [&](int i) { return (LAS char*)(sm + FS + uq * 2048 + ((((i & 192) + 4 * (i & 15) + ((i >> 4) & 3)) * 4) ^ (uq << 5))); };
    if (!G) {
#pragma nounroll
      for (int spin = 0; spin < SPIN_CAP; ++spin) {
        asm volatile("" ::: "memory");
        u32x2 q = ld_sc1_2(rw, WS_ST + (b + 224) * 64 + hw * 8);
        if (all_ok(q[1] == tag)) break;
        __builtin_amdgcn_s_sleep(8);
      }
      __builtin_amdgcn_s_sleep(20);
    }
    STAMP(26);
#pragma unroll
    for (int k = 0; k < 4; ++k) ALD4S(ug[k], uoff, sw, k * 64 * 16384, " sc1");
    WAITG(5 + 12 + 4);
    XBAR(2);
    STAMP(4);
#pragma unroll
    for (int k = 0; k < 6; ++k) DMA(5, k);
    STAMP(27);
    GWAIT(12);
    PIN(ug[0]); PIN(ug[1]); PIN(ug[2]); PIN(ug[3]);
    const u32 c0 = tpat(uq, 0), c1 = tpat(uq, 1);
#define CHK1 all_ok(((((ug[0][0] ^ c0) | (ug[1][0] ^ c0) | (ug[2][0] ^ c0) | (ug[3][0] ^ c0) | (ug[0][2] ^ c1) | (ug[1][2] ^ c1) | (ug[2][2] ^ c1) | (ug[3][2] ^ c1)) & 0x00070007u) == 0))
    if (__builtin_expect(!CHK1, 0)) {
#pragma nounroll
      for (int spin = 0; spin < SPIN_CAP; ++spin) {
        asm volatile("" ::: "memory");
#pragma unroll
        for (int k = 0; k < 4; ++k) ug[k] = ld_sc1(rw, uoff + k * 64 * 16384);
        if (CHK1) break;
      }
    }
    {
      LAS char* cp = caddr(ui);
      const u32 x0 = c0 ^ 0x00040004u, x1 = c1 ^ 0x00040004u;
#pragma unroll
      for (int k = 0; k < 4; ++k) {
        *(LAS u32*)(cp + k * 256) = ug[k][0] ^ x0; *(LAS u32*)(cp + k * 256 + 1024) = ug[k][2] ^ x1;
        *(LAS u32*)(cp + k * 256 + 8192) = ug[k][1]; *(LAS u32*)(cp + k * 256 + 9216) = ug[k][3];
      }
    }
    u32x4 fsp[2];
    const bool special = !G && b < 8 && hw == 0;
    if (special) {
#pragma nounroll
      for (int spin = 0; spin < SPIN_CAP; ++spin) {
        asm volatile("" ::: "memory");
        bool ok = true;
#pragma unroll
        for (int c = 0; c < 2; ++c) {
          const u32x4 fa = ld_sc1(rw, WS_SF + jp * 4096 + c * 2048 + lane * 32), fb = ld_sc1(rw, WS_SF + jp * 4096 + c * 2048 + lane * 32 + 16);
          ok &= tags_ok(fa, tag) & tags_ok(fb, tag);
          fsp[c] = u32x4{fa[0], fa[2], fb[0], fb[2]};
        }
        if (all_ok(ok)) break;
      }
    }
    STAMP(28);
    XBAR(3);
    STAMP(5);
    LAS char* const colp = sm + FS + hw * 2048 + ((lane * 16) ^ (hw << 5));
    {
      u32x4 uin[2], fin[2];
#pragma unroll
      for (int a = 0; a < 2; ++a) { uin[a] = *(LAS u32x4*)(colp + a * 1024); fin[a] = *(LAS u32x4*)(colp + 8192 + a * 1024); }
      const bool two = special && (jp & 3) == 0;
      f32x4 dre[2], dim[2], keep;
      auto col_xform = [&](auto NA_) {
        constexpr int NA = decltype(NA_)::v;
        u32x4 ta[2];
        DMA(6, 0);
#pragma unroll
        for (int a = 0; a < NA; ++a) { dre[a] = mfma32_f16(uin[a], bc1re, z4); dim[a] = mfma32_f16(uin[a], bc1im, z4); }
        DMA(6, 1);
#pragma unroll
        for (int a = 0; a < NA; ++a) { ta[a] = cmulc_f16(dre[a], dim[a], t1); dre[a] = mfma32_f16(ac2re, ta[a], z4); dim[a] = mfma32_f16(ac2im, ta[a], z4); }
        DMA(6, 2);
#pragma unroll
        for (int a = 0; a < NA; ++a) {
          f32x4 fr_, fi_;
#pragma unroll
          for (int r = 0; r < 4; ++r) { fr_[r] = bf_lo(fin[a][r]); fi_[r] = bf_hi(fin[a][r]); }
          ta[a] = cmul_pack<false, true>(dre[a], dim[a], fr_, fi_); dre[a] = mfma32_bf16(ta[a], bc3re, z4); dim[a] = mfma32_bf16(ta[a], bc3im, z4);
          DMA(6, 3 + a);
        }
#pragma unroll
        for (int a = 0; a < NA; ++a) {
          ta[a] = cmul_pack<false, true>(dre[a], dim[a], t2c, t2s); dre[a] = mfma32_bf16(ac4re, ta[a], z4); dim[a] = mfma32_bf16(ac4im, ta[a], z4);
          if (a == 0) DMA(6, 5);
        }
      };
      if (!G) {
        if (special) {
          fin[0] = jp > 4 ? fsp[1] : fsp[0];
          if (two) {
            const u32x4 us = uin[0];
            uin[0] = us >> 16; fin[0] = fsp[1];
            col_xform(IC<1>{});
            keep = dre[0];
            uin[0] = us & 0xffffu; fin[0] = fsp[0];
          }
        }
      }
      col_xform(IC<2>{});
      if (!G) { if (two) dim[0] = keep; }
#pragma unroll
      for (int a = 0; a < 2; ++a) *(LAS u32x4*)(colp + a * 1024) = pack4<true>(dre[a], dim[a]);
      {
        const u32 vo = b * 7168 + hw * 2048 + lane * 16;
        glds_a(lds0 + hw * 2048, FS + 8192, vo, (hw < 3 ? vo : 0x7fffffffu), sd_, 0);
      }
#define NW 2
    }
    STAMP(29);
    GWAIT(12 + NW);
    XBAR(4);
    STAMP(6);
    {
      const int ps = ht >> 2;
      u32 vo[4][2];
#pragma unroll
      for (int k = 0; k < 4; ++k) { LAS char* cp = caddr((ps >> 5) + 2 * k + 8 * (ps & 31)); vo[k][0] = *(LAS u32*)cp; vo[k][1] = *(LAS u32*)(cp + 1024); }
#pragma unroll
      for (int k = 0; k < 6; ++k) DMA(7, k);
#pragma unroll
      for (int k = 0; k < 4; ++k) st_sc1(rw, WS_V + b * 16384 + k * 4096 + ht * 16, u32x4{vo[k][0], tag, vo[k][1], tag});
    }
    const unsigned long long t_v = __builtin_amdgcn_s_memrealtime();
    STAMP(20);
    GWAIT(NW + 12 + 4);
    XBAR(5);
    STAMP(7);
    wd_setup();
    u32x2 csv[4], stv;
    const u32 csoff = WS_CS + ((t & 1) * F_ + fj * 128 + hw * 32 + (lane >> 5) * 16 + ((p16 >> 2) & 1) * 8 + (p16 >> 3) * 4 + (p16 & 3)) * 8;
    const u32 stoff = WS_ST + (((mi & 1) * 128 + hw * 32 + (lane & 31)) * 8 + (mi >> 1)) * 8;
    if (G) {
      ALD2S(csv[0], csoff, sw, 0, " sc1"); ALD2S(csv[1], csoff, sw, 2 * F_ * 8, " sc1"); ALD2S(csv[2], csoff, sw, 4 * F_ * 8, " sc1"); ALD2S(csv[3], csoff, sw, 6 * F_ * 8, " sc1");
      ALD2(stv, stoff, sw, 0, " sc1");
    }
    if (G) { WAITV(4 + 5); *(volatile LAS u32*)(sm + FS + 1072 + hw * 4) = tag; }
#pragma unroll
    for (int hh = 0; hh < 2; ++hh) {
      wd_issue(w, hh, hh);
      const float n2 = (float)(8 * (p16 >> 2) + (p16 & 3)) + 4.0f * hh;
#pragma unroll
      for (int m = 0; m < 4; ++m) {
        float rev = n2 * ((tf + 4.0f * m) * (1.0f / 32));
        bi1im[hh][m] = pk_bf16(sinr(rev), cosr(rev)); bi1re[hh][m] = rot16(bi1im[hh][m]) ^ 0x80000000u;
        float rev2 = n2 * ((4.0f * tf + m) * (1.0f / 512));
        t4c[hh][m] = cosr(rev2); t4s[hh][m] = sinr(rev2);
      }
      PIN(bi1re[hh]); PIN(bi1im[hh]); PIN(t4c[hh]); PIN(t4s[hh]);
    }
    wd_issue(w, 2, 2);
    if (G) {
      WAITV(12);
      PIN(csv[0]); PIN(csv[1]); PIN(csv[2]); PIN(csv[3]); PIN(stv);
#define CHK3 all_ok((stv[1] == tag) & (csv[0][1] == tag) & (csv[1][1] == tag) & (csv[2][1] == tag) & (csv[3][1] == tag))
      if (__builtin_expect(!CHK3, 0)) {
#pragma nounroll
        for (int spin = 0; spin < SPIN_CAP; ++spin) {
          asm volatile("" ::: "memory");
#pragma unroll
          for (int k = 0; k < 4; ++k) csv[k] = ld_sc1_2(rw, csoff + k * 2 * F_ * 8);
          stv = ld_sc1_2(rw, stoff);
          if (CHK3) break;
        }
      }
      const u32 s0 = csv[0][0], s1 = csv[1][0], s2 = csv[2][0], s3 = csv[3][0];
      const float cs = (__builtin_bit_cast(float, s0) + __builtin_bit_cast(float, s1)) + (__builtin_bit_cast(float, s2) + __builtin_bit_cast(float, s3));
      const float ch = rnd_bf(cs);
      LAS char* cp = sm + FS + hw * 2048 + lane * 4;
      *(LAS u32*)cp = pk_bf16(ch, cs - ch);
      *(LAS u32*)(cp + 256) = __builtin_amdgcn_perm(0u, stv[0], 0x01000100u) ^ 0x80008000u;
    }
    u32x4 rv[2][2];
    const u32 voff = WS_V + ((b & 7) * 32 + (b >> 3)) * 64 + (p16 & 3) * 16 + ((8 * hw + (p16 >> 2)) * 8 + 2 * t) * 16384;
    if (!G) {
      WAITV(12);
#pragma nounroll
      for (int spin = 0; spin < SPIN_CAP; ++spin) {
        asm volatile("" ::: "memory");
        u32x2 q = ld_sc1_2(rw, WS_V + (b + 224) * 16384 + hw * 4096);
        if (all_ok(q[1] == tag)) break;
        __builtin_amdgcn_s_sleep(8);
      }
      __builtin_amdgcn_s_sleep(20);
    }
    hsync(G ? FS + 1088 : G1_STAGE + 96);
    if (G) { while (__builtin_amdgcn_s_memrealtime() - t_v < V_DELAY) __builtin_amdgcn_s_sleep(2); }
    STAMP(8);
    {
      ALD4S(rv[0][0], voff, sw, 0, " sc1"); ALD4S(rv[0][1], voff, sw, 16384, " sc1"); ALD4S(rv[1][0], voff, sw, 32 * 16384, " sc1"); ALD4S(rv[1][1], voff, sw, 33 * 16384, " sc1");
    }
    auto mwait = [&](int off) {
      if (G) {
#pragma nounroll
        for (int spin = 0; spin < SPIN_CAP; ++spin) { if (all_ok(*(volatile LAS u32*)(sm + off + (lane & 3) * 4) == tag)) break; __builtin_amdgcn_s_sleep(1); }
        asm volatile("" ::: "memory");
      }
    };
    WAITV(4);
    wd_read(0, 0); wd_read(1, 1); wd_read(2, 2);
    STAMP(9);
    WAITV(0);
    PIN(rv[0][0]); PIN(rv[0][1]); PIN(rv[1][0]); PIN(rv[1][1]);
#define CHK4 all_ok(tags_ok(rv[0][0], tag) & tags_ok(rv[0][1], tag) & tags_ok(rv[1][0], tag) & tags_ok(rv[1][1], tag))
    if (__builtin_expect(!CHK4, 0)) {
#pragma nounroll
      for (int spin = 0; spin < SPIN_CAP; ++spin) {
        asm volatile("" ::: "memory");
#pragma unroll
        for (int k = 0; k < 2; ++k) { rv[k][0] = ld_sc1(rw, voff + k * 32 * 16384); rv[k][1] = ld_sc1(rw, voff + k * 32 * 16384 + 16384); }
        if (CHK4) break;
        __builtin_amdgcn_s_sleep(4);
      }
    }
    STAMP(12);
    LAS char* const yb = sm + FS + 8192;
#pragma unroll
    for (int k = 0; k < 2; ++k) {
      f32x4 da = mfma16_bf16(u32x2{qa8i[0], qa8i[1]}, u32x2{rv[k][0][0], rv[k][1][0]}, z4), db = mfma16_bf16(u32x2{qa8i[0], qa8i[1]}, u32x2{rv[k][0][2], rv[k][1][2]}, z4);
      if (k == 0) { f32x4 ds = mfma16_bf16(u32x2{qa8i[2], qa8i[3]}, u32x2{rv[0][0][0], rv[0][1][0]}, z4); if ((hw | p16) == 0) da = ds; }
      *(LAS u32x2*)(yb + (2 * t) * 1024 + (32 * hw + 16 * k + p16) * 8) = u32x2{pk_bf16(da[0], da[1]), pk_bf16(db[0], db[1])};
      *(LAS u32x2*)(yb + (2 * t + 1) * 1024 + (32 * hw + 16 * k + p16) * 8) = u32x2{pk_bf16(da[2], da[3]), pk_bf16(db[2], db[3])};
    }
    asm volatile("s_waitcnt lgkmcnt(0)" ::: "memory");
    *(volatile LAS u32*)(sm + FS + 1024 + hw * 4) = tag;
    mwait(FS + 1056);
    wd_issue(w, 3, 3); wd_issue(w, 4, 4); wd_issue(w, 5, 5); wd_issue(w, 6, 0);
    hsync(FS + 1024);
    STAMP(13);
    u32x4 ro[2];
#pragma unroll
    for (int a = 0; a < 2; ++a) {
      u32x4 av;
#pragma unroll
      for (int m = 0; m < 4; ++m) av[m] = *(LAS u32*)(yb + (hw + 4 * a) * 1024 + (lane + 64 * m) * 4);
      const float nyq = bf_hi(*(LAS u32*)(yb + (hw + 4 * a) * 1024));
#pragma unroll
      for (int hh = 0; hh < 2; ++hh) {
        f32x4 gre = mfma32_bf16(av, bi1re[hh], z4), gim = mfma32_bf16(av, bi1im[hh], z4);
        u32x4 bb = cmul_pack<false, true>(gre, gim, t4c[hh], t4s[hh]);
        f32x4 d = mfma32_bf16(bb, bc3re, f32x4{nyq, -nyq, nyq, -nyq});
        ro[a][2 * hh] = pk_bf16(__builtin_fmaf(d[0], FSC, bf_lo(xr[a][2 * hh])), __builtin_fmaf(d[1], FSC, bf_hi(xr[a][2 * hh])));
        ro[a][2 * hh + 1] = pk_bf16(__builtin_fmaf(d[2], FSC, bf_lo(xr[a][2 * hh + 1])), __builtin_fmaf(d[3], FSC, bf_hi(xr[a][2 * hh + 1])));
      }
      PIN(ro[a]);
    }
    STAMP(14);
#pragma unroll
    for (int a = 0; a < 2; ++a) st_sc1(rw, WS_R + (b + 256 * hw + 1024 * a) * 1024 + (4 * p16 + t) * 16, ro[a]);
    WAITV(0);
    if (lane < 2) st_sc1_2(rw, WS_ST2 + b * 64 + hw * 8 + lane * 32, u32x2{lane ? stat[1] : stat[0], tag});
    STAMP(11);
  };

  if (w < 4) {
    if (is_gemm) {
      tag = load_tag();
      const int h = lane >> 5;
      int xoff[4], woff[2];
#pragma unroll
      for (int ks = 0; ks < 4; ++ks) { int tok = lane & 31; xoff[ks] = tok * 128 + (((2 * ks + h) ^ ((tok >> 1) & 7)) << 4); PIN(xoff[ks]); }
      {
        int arr = t & 1, q = p16 >> 2, p = lane & 3;
#pragma unroll
        for (int m = 0; m < 2; ++m) {
          woff[m] = 16384 + arr * 16384 + (8 * h + q) * 256 + (((hw * 2 + m) ^ (q | (arr << 2))) << 5) + (p & 1) * 16 + (p >> 1) * 8;
          PIN(woff[m]);
        }
      }
      auto rdx = [&](Frag& f, int st, int ks, int n) { f.bx[n] = *(LAS u32x4*)(sm + (st % 3) * G1_STAGE + xoff[ks] + n * 4096); };
      auto rdw = [&](Frag& f, int st, int ks, int m) {
        LAS char* buf = sm + (st % 3) * G1_STAGE;
        u32x2 lo = tr_read(buf + woff[m] + ks * 4096), hi = tr_read(buf + woff[m] + ks * 4096 + 1024);
        f.aw[m] = u32x4{lo[0], lo[1], hi[0], hi[1]};
      };
      f32x16 acc[2][4];
#pragma unroll
      for (int m = 0; m < 2; ++m)
#pragma unroll
        for (int n = 0; n < 4; ++n)
#pragma unroll
          for (int k = 0; k < 16; ++k) acc[m][n][k] = 0.f;
      Frag fr[2];
      u32 qa[2], qb[4], pv;
      auto twait = [&](int off) {
#pragma nounroll
        for (int spin = 0; spin < SPIN_CAP; ++spin) { if (all_ok(*(volatile LAS u32*)(sm + off + (lane & 3) * 4) == tag)) break; }
        asm volatile("" ::: "memory");
      };
      SB();
      STAMP(21);
      g1_issue(1, 0); g1_issue(1, 1);
      WAITV(4);
      {
        LAS char* mb = sm + G1_STAGE + 32768 + 3072;
        *(volatile LAS u32*)(mb + (lane & 3) * 4096 + hw * 4) = tag;
#pragma nounroll
        for (int spin = 0; spin < SPIN_CAP; ++spin) { if (all_ok(*(volatile LAS u32*)(mb + hw * 4096 + (lane & 3) * 4) == tag)) break; }
        asm volatile("" ::: "memory");
      }
      STAMP(1);
      rdw(fr[0], 0, 0, 0); rdx(fr[0], 0, 0, 0); rdx(fr[0], 0, 0, 1); rdx(fr[0], 0, 0, 2); rdx(fr[0], 0, 0, 3); rdw(fr[0], 0, 0, 1);
      static_for<0, 30>([&](auto KS_) {
        constexpr int KS = decltype(KS_)::v, st = KS >> 2, ks = KS & 3;
        constexpr int nst = ks < 3 ? st : st + 1, nks = (ks + 1) & 3;
        Frag& f = fr[KS & 1]; Frag& g = fr[(KS + 1) & 1];
        SB();
        if constexpr (ks == 3) {
          if constexpr (st == 0) WAITV(0);
          if constexpr (st < 6) BAR();
          else { if (__builtin_expect(!all_ok(pv == tag), 0)) twait(FS + 1072); asm volatile("" ::: "memory"); }
          STAMP(2 + st); SB();
        }
        acc[0][0] = mfma3232(f.aw[0], f.bx[0], acc[0][0]); SB(); rdw(g, nst, nks, 0); SB();
        acc[0][1] = mfma3232(f.aw[0], f.bx[1], acc[0][1]); SB(); rdx(g, nst, nks, 0); SB();
        if constexpr (KS == 0 && S1PRE != 6) { g1_issue(1, 2); SB(); }
        if constexpr (KS == 1 && S1PRE != 6) { g1_issue(1, 5); SB(); }
        acc[0][2] = mfma3232(f.aw[0], f.bx[2], acc[0][2]); SB(); rdx(g, nst, nks, 1); SB();
        acc[0][3] = mfma3232(f.aw[0], f.bx[3], acc[0][3]); SB(); rdx(g, nst, nks, 2); SB();
        if constexpr (KS == 0 && S1PRE != 6) { g1_issue(1, 3); SB(); }
        acc[1][3] = mfma3232(f.aw[1], f.bx[3], acc[1][3]); SB(); rdx(g, nst, nks, 3); SB();
        acc[1][2] = mfma3232(f.aw[1], f.bx[2], acc[1][2]); SB(); rdw(g, nst, nks, 1); SB();
        acc[1][1] = mfma3232(f.aw[1], f.bx[1], acc[1][1]); SB();
        if constexpr (KS == 0 && S1PRE != 6) { g1_issue(1, 4); SB(); }
        if constexpr (KS == 26) { pv = *(volatile LAS u32*)(sm + FS + 1072 + (lane & 3) * 4); SB(); }
        if constexpr (KS == 28) { pv = *(volatile LAS u32*)(sm + FS + 1088 + (lane & 3) * 4); SB(); }
        if constexpr (KS == 29) {
          if (__builtin_expect(!all_ok(pv == tag), 0)) twait(FS + 1088);
          asm volatile("" ::: "memory");
          LAS char* cp = sm + FS + (lane & 31) * 4;
          qa[0] = *(LAS u32*)(cp + hw * 2048); qa[1] = *(LAS u32*)(cp + hw * 2048 + 128);
#pragma unroll
          for (int n = 0; n < 4; ++n) qb[n] = *(LAS u32*)(cp + n * 2048 + 256);
          SB();
        }
        acc[1][0] = mfma3232(f.aw[1], f.bx[0], acc[1][0]); SB();
      });
      rdw(fr[1], 7, 3, 0); rdx(fr[1], 7, 3, 0); rdw(fr[1], 7, 3, 1); rdx(fr[1], 7, 3, 1); rdx(fr[1], 7, 3, 2); rdx(fr[1], 7, 3, 3);
      __builtin_amdgcn_s_setprio(3);
      STAMP(9);
      u32x4 ca[2] = {u32x4{h ? 0u : qa[0], 0u, 0u, 0u}, u32x4{h ? 0u : qa[1], 0u, 0u, 0u}}, cb = {0u, 0u, 0u, 0u};
      const u32 hoff = WS_H + ((mi * 8 + (t & 1)) * 56 + fj * 4 + hw) * 1024 + h * 256 + p16 * 16;
      float ev[8];
      auto epi_a = [&](int q) {
#pragma unroll
        for (int k = 0; k < 8; ++k) ev[k] = relu(acc[q & 1][q >> 1][k]);
      };
      auto epi_b = [&](int q) {
#pragma unroll
        for (int k = 0; k < 8; ++k) ev[k] *= acc[q & 1][q >> 1][8 + k];
      };
      auto epi_c = [&](int q) {
        __builtin_amdgcn_raw_buffer_store_b128(u32x4{pk_bf16(ev[0], ev[1]), pk_bf16(ev[2], ev[3]), pk_bf16(ev[4], ev[5]), pk_bf16(ev[6], ev[7])}, rw,
                                               hoff + (q & 1) * 512, (q >> 1) * (2 * 56 * 1024), 16);
      };
#pragma unroll
      for (int q = 0; q < 8; ++q) {
        const int m = q & 1, n = q >> 1;
        SB();
        if (m == 0) cb[0] = qb[n];
        acc[m][n] = mfma3232(fr[0].aw[m], fr[0].bx[n], acc[m][n]); SB();
        if (q >= 2) { epi_c(q - 2); SB(); }
        acc[m][n] = mfma3232(ca[m], cb, acc[m][n]); SB();
        if (q >= 1) { epi_a(q - 1); SB(); }
        acc[m][n] = mfma3232(fr[1].aw[m], fr[1].bx[n], acc[m][n]); SB();
        if (q >= 1) { epi_b(q - 1); SB(); }
      }
      epi_c(6); SB(); epi_a(7); epi_b(7); epi_c(7);
      *(volatile LAS u32*)(sm + FS + 1056 + hw * 4) = tag;
      STAMP(10);
      wd_setup();
      WAITV(4);
      if (lane == 0) __builtin_amdgcn_raw_buffer_store_b32(tag, rw, WS_F2 + (mi * 2) * 256 + (fj * 4 + hw) * 4, 0, 16);
      WAITV(1);
      if (lane == 0) __builtin_amdgcn_raw_buffer_store_b32(tag, rw, WS_F2 + (mi * 2 + 1) * 256 + (fj * 4 + hw) * 4, 0, 16);
      STAMP(12);
      __builtin_amdgcn_s_setprio(HPRIO);
    } else {
      wd_setup();
      const int un = b >> 1, ua = un >= 7, uc = (b & 1) * 7 + (ua ? un - 7 : un);
      const u32x4 scs = {ua ? ka[8] : ka[6], ua ? ka[9] : ka[7], b < 28 ? 1835008u : 0u, 0x00020000u};
      const u32 vcs = (hw * 4 + t) * 3584 + uc * 256 + p16 * 16;
      u32x4 vv[16];
      f32x4 a0 = z4, a1 = z4;
      u32 sfa, sfb;
      u32 sel_lo = 0x00003f80u, sel_hi = 0x3f800000u;
      PIN(sel_lo); PIN(sel_hi);
      const unsigned long long t_start = __builtin_amdgcn_s_memrealtime();
#pragma unroll
      for (int half = 0; half < 2; ++half) {
        if (half == 1) { while (__builtin_amdgcn_s_memrealtime() - t_start < CS_DELAY) __builtin_amdgcn_s_sleep(4); }
#pragma unroll
        for (int k = 0; k < 16; ++k) ALD4S(vv[k], vcs, scs, (half * 16 + k) * 16 * 3584, "");
        if (half == 0) {
          const int q = 64 * g + 16 * hw + p16, ks = 8 * q + jp, km = (2048 - ks) & 2047, col = (t >> 1) * 256;
          const GAS unsigned short* wa = (t & 1) ? wi : wr;
          asm volatile("global_load_ushort %0, %1, off" : "=v"(sfa) : "v"(wa + ks * DF_ + col) : "memory");
          asm volatile("global_load_ushort %0, %1, off" : "=v"(sfb) : "v"(wa + km * DF_ + col) : "memory");
          tag = load_tag();
        }
        STAMP(23 + 3 * half);
        static_for<0, 4>([&](auto Q_) {
          constexpr int Q = decltype(Q_)::v;
          if (half == 0) WAITV(14 - 4 * Q); else WAITV(12 - 4 * Q);
#pragma unroll
          for (int k = 4 * Q; k < 4 * Q + 4; ++k) {
            PIN(vv[k]);
#pragma unroll
            for (int e = 0; e < 2; ++e) {
              const u32 p = vv[k][e], q = vv[k][2 + e];
              a0[2 * e] = __builtin_amdgcn_fdot2_f32_bf16(__builtin_bit_cast(b16x2, p), __builtin_bit_cast(b16x2, sel_lo), a0[2 * e], false);
              a0[2 * e + 1] = __builtin_amdgcn_fdot2_f32_bf16(__builtin_bit_cast(b16x2, p), __builtin_bit_cast(b16x2, sel_hi), a0[2 * e + 1], false);
              a1[2 * e] = __builtin_amdgcn_fdot2_f32_bf16(__builtin_bit_cast(b16x2, q), __builtin_bit_cast(b16x2, sel_lo), a1[2 * e], false);
              a1[2 * e + 1] = __builtin_amdgcn_fdot2_f32_bf16(__builtin_bit_cast(b16x2, q), __builtin_bit_cast(b16x2, sel_hi), a1[2 * e + 1], false);
            }
          }
        });
        PIN(a0); PIN(a1);
        STAMP(24 + 3 * half);
        if (half == 0) {
          WAITV(0);
          PIN(sfa); PIN(sfb);
          const float wh = 0.5f * ((t & 1) ? bf_lo(sfa) - bf_lo(sfb) : bf_lo(sfa) + bf_lo(sfb));
          const u32 wb = pk_bf16(wh, wh) & 0xffffu;
          auto s2 = __builtin_amdgcn_permlane16_swap(wb, wb, false, false);
          if ((t & 1) == 0) st_sc1_2(rw, WS_SF + jp * 4096 + (t >> 1) * 2048 + (64 * g + 4 * p16 + hw) * 8, u32x2{(u32)s2[0] | ((u32)s2[1] << 16), tag});
          STAMP(25);
        }
      }
      if (b < 28) {
#pragma unroll
        for (int k = 0; k < 4; ++k) { a0[k] = xrow_sum(a0[k]); a1[k] = xrow_sum(a1[k]); }
        const float s0 = t == 0 ? a0[0] : t == 1 ? a0[2] : t == 2 ? a1[0] : a1[2];
        const float s1 = t == 0 ? a0[1] : t == 1 ? a0[3] : t == 2 ? a1[1] : a1[3];
        st_sc1(rw, WS_CS + hw * (2 * F_ * 8) + (ua * 14 + uc) * 1024 + p16 * 64 + t * 16, u32x4{__builtin_bit_cast(u32, s0), tag, __builtin_bit_cast(u32, s1), tag});
      }
      STAMP(3);
    }
    {
      const int off = is_gemm ? FS + 1056 : FS + 1024;
#pragma nounroll
      for (int spin = 0; spin < SPIN_CAP; ++spin) { if (all_ok(*(volatile LAS u32*)(sm + off + (lane & 3) * 4) == tag)) break; __builtin_amdgcn_s_sleep(1); }
      asm volatile("" ::: "memory");
    }
    wd_issue(w, 0, 6); wd_issue(w, 1, 7); wd_issue(w, 2, 8);
    {
#pragma nounroll
      for (int spin = 0; spin < SPIN_CAP; ++spin) { if (all_ok(*(volatile LAS u32*)(sm + FS + 1024 + hw * 4) == tag)) break; __builtin_amdgcn_s_sleep(1); }
      asm volatile("" ::: "memory");
    }
    wd_issue(w, 6, 1);
    WAITV(4);
    wd_read(0, 6); wd_read(1, 7); wd_read(2, 8);
    asm volatile("s_waitcnt lgkmcnt(0)" ::: "memory");
    wd_issue(w, 3, 6); wd_issue(w, 4, 7); wd_issue(w, 5, 8);
    STAMP(13);
  } else if (is_gemm) helper(IC<1>{});
  else helper(IC<0>{});

  wait_h_and_load();
  __builtin_amdgcn_s_setprio(0);
#pragma unroll
  for (int i = 3; i < 7; ++i) {
    LAS char* p0 = sm + (w < 4 ? wslot(i == 6 ? 1 : i + 3) : wslot(i == 6 ? 0 : i));
#pragma unroll
    for (int u = 0; u < 4; ++u) {
      LAS char* p = p0 + (wbase ^ (u << 5));
      u32x2 lo = tr_read(p), hi = tr_read(p + 512);
      wf[i][u] = u32x4{lo[0], lo[1], hi[0], hi[1]};
    }
  }
  STAMP(16);
  const int oc = lane & 7, otok = (w & 1) * 8 + (lane >> 3);
  const int orow = rt * 64 + (((w >> 1) + ct) & 3) * 16 + otok;
  const u32 roff = WS_R + orow * 1024 + (ct * 8 + oc) * 16;
  const u32 soff_ = WS_ST2 + ((orow & 255) * 8 + (orow >> 8)) * 8;
  u32x4 r0; u32x2 sto; bool rok;
#pragma unroll
  for (int tt = 0; tt < 4; ++tt) {
    f32x4 acc[4] = {z4, z4, z4, z4};
#pragma unroll
    for (int ks = 0; ks < 7; ++ks) {
#pragma unroll
      for (int u = 0; u < 4; ++u) acc[u] = mfma32_bf16(wf[ks][u], hf[tt][ks], acc[u]);
      if (tt == 0) { SB(); hf[3][ks] = ld_h(3, ks); SB(); }
    }
    if (tt == 1) sto = ld_sc1_2(rw, soff_);
    if (tt == 3) { asm volatile("" : "+v"(sto) :: "memory"); rok = all_ok(sto[1] == tag); r0 = ld_sc1(rw, roff); }
#pragma unroll
    for (int u = 0; u < 4; ++u) *(LAS f32x4*)(sm + (w < 4 ? wslot(tt < 3 ? 6 + tt : 1) : wslot(tt < 3 ? 3 + tt : 0)) + hw * 4096 + u * 1024 + t * 256 + ((p16 ^ (4 * u + (t & 2))) << 4)) = acc[u];
    SB();
  }
  STAMP(17);
  BAR();
  f32x4 sa = z4, sb = z4;
  {
    const int tt = w >> 1;
    const int o0 = (oc >> 1) * 1024 + (oc & 1) * 512 + ((otok ^ (2 * oc)) << 4);
    const int bm = (tt < 3 ? wslot(6) + tt * 16384 : wslot(1)) + o0, bh = (tt < 3 ? wslot(3) + tt * 16384 : wslot(0)) + o0;
#pragma unroll
    for (int pw = 0; pw < 4; ++pw) {
      sa += *(LAS f32x4*)(sm + bm + pw * 4096) + *(LAS f32x4*)(sm + bh + pw * 4096);
      sb += *(LAS f32x4*)(sm + bm + 256 + pw * 4096) + *(LAS f32x4*)(sm + bh + 256 + pw * 4096);
    }
  }
  STAMP(22);
  if (__builtin_expect(!rok, 0)) {
#pragma nounroll
    for (int spin = 0; spin < SPIN_CAP; ++spin) {
      asm volatile("" ::: "memory");
      sto = ld_sc1_2(rw, soff_);
      if (all_ok(sto[1] == tag)) break;
    }
    asm volatile("" ::: "memory");
    r0 = ld_sc1(rw, roff);
  }
  {
    const float rs = __builtin_amdgcn_rcpf(bf_hi(sto[0])), rs2 = rs * rs;
    *(GAS u32x4*)(out + (size_t)orow * 1024 + ct * 128 + oc * 16) =
        u32x4{pk_bf16(__builtin_fmaf(sa[0], rs2, bf_lo(r0[0])), __builtin_fmaf(sa[1], rs2, bf_hi(r0[0]))),
              pk_bf16(__builtin_fmaf(sa[2], rs2, bf_lo(r0[1])), __builtin_fmaf(sa[3], rs2, bf_hi(r0[1]))),
              pk_bf16(__builtin_fmaf(sb[0], rs2, bf_lo(r0[2])), __builtin_fmaf(sb[1], rs2, bf_hi(r0[2]))),
              pk_bf16(__builtin_fmaf(sb[2], rs2, bf_lo(r0[3])), __builtin_fmaf(sb[3], rs2, bf_hi(r0[3])))};
  }
  if (b == 0 && tid == 0) __builtin_amdgcn_raw_buffer_store_b32(tag, rw, WS_EPOCH, 0, 16);
  STAMP(18);
  STAMP_DUMP();
}

#if defined(__HIP_DEVICE_COMPILE__)
#pragma clang attribute pop
#endif

extern "C" void kernel_launch(void* const* d_in, const int* in_sizes, int n_in, void* d_out, int out_size, void* d_ws, size_t ws_size,
                              hipStream_t stream) {
  (void)hipFuncSetAttribute((const void*)fused_kernel, hipFuncAttributeMaxDynamicSharedMemorySize, LDS_BYTES);
  fused_kernel<<<256, 512, LDS_BYTES, stream>>>((const char*)d_in[0], (const unsigned short*)d_in[1], (const unsigned short*)d_in[2], (const char*)d_in[4],
                                                (const char*)d_in[5], (const char*)d_in[6], (char*)d_ws, (char*)d_out);
}
```

```cpp
#define PROBE 0
#define PROBE_WG 100
#define PROBE_W 0
#include <hip/hip_runtime.h>
#include <stdint.h>

#define LAS __attribute__((address_space(3)))
#define GAS __attribute__((address_space(1)))
#define DI __device__ __forceinline__
typedef unsigned u32;
typedef unsigned u32x2 __attribute__((ext_vector_type(2)));
typedef unsigned u32x4 __attribute__((ext_vector_type(4)));
typedef float f32x2 __attribute__((ext_vector_type(2)));
typedef float f32x4 __attribute__((ext_vector_type(4)));
typedef float f32x16 __attribute__((ext_vector_type(16)));
typedef short s16x4 __attribute__((ext_vector_type(4)));
typedef _Float16 h16x2 __attribute__((ext_vector_type(2)));
typedef _Float16 h16x4 __attribute__((ext_vector_type(4)));
typedef _Float16 h16x8 __attribute__((ext_vector_type(8)));
typedef __bf16 b16x2 __attribute__((ext_vector_type(2)));
typedef __bf16 b16x8 __attribute__((ext_vector_type(8)));
typedef __amdgpu_buffer_rsrc_t rsrc_t;

constexpr int F_ = 1792, DF_ = 257;
constexpr u32 WS_EPOCH = 0;
constexpr u32 WS_F2 = 4096;
constexpr u32 WS_ST = 16384;
constexpr u32 WS_CS = WS_ST + 16384;
constexpr u32 WS_U = 262144;
constexpr u32 WS_V = WS_U + 4194304;
constexpr u32 WS_R = WS_V + 4194304;
constexpr u32 WS_ST2 = 229376;
constexpr u32 WS_SF = 196608;
constexpr u32 WS_H = WS_R + 4194304;
constexpr u32 WS_END = WS_H + 7340032;
constexpr int LDS_BYTES = 163840;
constexpr int G1_STAGE = 49152;
constexpr int FS = 3 * G1_STAGE;
#define SPIN_CAP (1 << 17)
constexpr int CODE_LINES = 292;
#define HPRIO 2
#define S1PRE 2
#define POLLSLEEP 2
#define CS_DELAY 380
#define WARM 0
#define UPOS 0
#define TV 2
#define V_DELAY 130
#define HW_EARLY 1
#define ROTM 1
constexpr float XS = 1.0f / 16;
constexpr float FSC = (2.0f / (2048.0f * 512.0f)) * 16.0f;
constexpr size_t WS_DBG = 64 << 20;
#define STAMP(k) do { if ((k) == PROBE) { asm volatile("s_memrealtime %0\n\ts_waitcnt lgkmcnt(0)" : "=s"(pr_tk) :: "memory"); } } while (0)
#define STAMP_DECL unsigned long long pr_tk = 0;
#define STAMP_DUMP() do { if (blockIdx.x == PROBE_WG && (threadIdx.x >> 6) == PROBE_W) { while (__builtin_amdgcn_s_memrealtime() < pr_tk + 4000) __builtin_amdgcn_s_sleep(1); } } while (0)

#if defined(__HIP_DEVICE_COMPILE__)
#pragma clang attribute push(__attribute__((target("no-packed-fp32-ops"))), apply_to = function)
#endif
DI u32 pk_f16(float a, float b) { h16x2 p = {(_Float16)a, (_Float16)b}; return __builtin_bit_cast(u32, p); }
DI u32 pk_bf16(float a, float b) { b16x2 p = {(__bf16)a, (__bf16)b}; return __builtin_bit_cast(u32, p); }
DI float bf_lo(u32 v) { return __builtin_bit_cast(float, v << 16); }
DI float bf_hi(u32 v) { return __builtin_bit_cast(float, v & 0xffff0000u); }
DI float rnd_bf(float a) { return bf_lo(pk_bf16(a, a)); }
DI float relu(float a) { int i = __builtin_bit_cast(int, a); return __builtin_bit_cast(float, i > 0 ? i : 0); }
DI float cosr(float rev) { return __builtin_amdgcn_cosf(rev); }
DI float sinr(float rev) { return __builtin_amdgcn_sinf(rev); }
DI f32x4 mfma16_f16(u32x2 a, u32x2 b, f32x4 c) { return __builtin_amdgcn_mfma_f32_16x16x16f16(__builtin_bit_cast(h16x4, a), __builtin_bit_cast(h16x4, b), c, 0, 0, 0); }
DI f32x4 mfma16_bf16(u32x2 a, u32x2 b, f32x4 c) { return __builtin_amdgcn_mfma_f32_16x16x16bf16_1k(__builtin_bit_cast(s16x4, a), __builtin_bit_cast(s16x4, b), c, 0, 0, 0); }
DI f32x4 mfma32_f16(u32x4 a, u32x4 b, f32x4 c) { return __builtin_amdgcn_mfma_f32_16x16x32_f16(__builtin_bit_cast(h16x8, a), __builtin_bit_cast(h16x8, b), c, 0, 0, 0); }
DI f32x4 mfma32_bf16(u32x4 a, u32x4 b, f32x4 c) { return __builtin_amdgcn_mfma_f32_16x16x32_bf16(__builtin_bit_cast(b16x8, a), __builtin_bit_cast(b16x8, b), c, 0, 0, 0); }
DI f32x16 mfma3232(u32x4 a, u32x4 b, f32x16 c) { return __builtin_amdgcn_mfma_f32_32x32x16_bf16(__builtin_bit_cast(b16x8, a), __builtin_bit_cast(b16x8, b), c, 0, 0, 0); }
DI u32x2 tr_read(LAS char* p) { return __builtin_bit_cast(u32x2, __builtin_amdgcn_ds_read_tr16_b64_v4i16((LAS s16x4*)p)); }
#define SB() __builtin_amdgcn_sched_barrier(0)
#define BAR() asm volatile("s_waitcnt lgkmcnt(0)\n\ts_barrier" ::: "memory")
#define WAITV(n) asm volatile("s_waitcnt vmcnt(%0)" :: "i"(n) : "memory")
#define PIN(x) asm volatile("" : "+v"(x))
DI u32x4 mk_srd(const void* p, u32 bytes) { uint64_t a = (uint64_t)p; return u32x4{(u32)a, (u32)(a >> 32), bytes, 0x00020000u}; }
DI void glds16(u32 ldsbase, int imm, u32 voff, u32x4 srd, u32 soff) {
  asm volatile("s_add_u32 m0, %0, %1\n\ts_nop 0\n\tbuffer_load_dwordx4 %2, %3, %4 offen lds" :: "s"(ldsbase), "i"(imm), "v"(voff), "s"(srd), "s"(soff) : "memory", "scc");
}
DI void glds4(u32 ldsaddr, u32 voff, u32x4 srd, u32 soff) {
  asm volatile("s_mov_b32 m0, %0\n\ts_nop 0\n\tbuffer_load_dword %1, %2, %3 offen lds" :: "s"(ldsaddr), "v"(voff), "s"(srd), "s"(soff) : "memory");
}
#define ALD4(dst, voff, srd, IMM, FL) asm volatile("buffer_load_dwordx4 %0, %1, %2, 0 offen offset:" #IMM FL : "=v"(dst) : "v"(voff), "s"(srd) : "memory")
#define ALD2(dst, voff, srd, IMM, FL) asm volatile("buffer_load_dwordx2 %0, %1, %2, 0 offen offset:" #IMM FL : "=v"(dst) : "v"(voff), "s"(srd) : "memory")
#define ALD1(dst, voff, srd, IMM, FL) asm volatile("buffer_load_dword %0, %1, %2, 0 offen offset:" #IMM FL : "=v"(dst) : "v"(voff), "s"(srd) : "memory")
DI u32x4 ld_sc1(rsrc_t r, u32 off) { return __builtin_amdgcn_raw_buffer_load_b128(r, off, 0, 16); }
DI u32x2 ld_sc1_2(rsrc_t r, u32 off) { return __builtin_amdgcn_raw_buffer_load_b64(r, off, 0, 16); }
DI void st_sc1(rsrc_t r, u32 off, u32x4 v) { __builtin_amdgcn_raw_buffer_store_b128(v, r, off, 0, 16); }
DI void st_sc1_2(rsrc_t r, u32 off, u32x2 v) { __builtin_amdgcn_raw_buffer_store_b64(v, r, off, 0, 16); }
DI bool all_ok(bool ok) { return __builtin_amdgcn_ballot_w64(!ok) == 0; }
DI bool tags_ok(u32x4 a, u32 tag) { return (a[1] == tag) & (a[3] == tag); }

template <int CTRL> DI float dpp_add(float v) {
  int o = __builtin_amdgcn_update_dpp(0, __builtin_bit_cast(int, v), CTRL, 0xf, 0xf, true);
  return v + __builtin_bit_cast(float, o);
}
DI float row_sum(float v) { v = dpp_add<0xB1>(v); v = dpp_add<0x4E>(v); v = dpp_add<0x141>(v); return dpp_add<0x140>(v); }
DI float xrow_sum(float v) {
  u32 u = __builtin_bit_cast(u32, v);
  auto a = __builtin_amdgcn_permlane16_swap(u, u, false, false);
  v = __builtin_bit_cast(float, (u32)a[0]) + __builtin_bit_cast(float, (u32)a[1]);
  u = __builtin_bit_cast(u32, v);
  auto b = __builtin_amdgcn_permlane32_swap(u, u, false, false);
  return __builtin_bit_cast(float, (u32)b[0]) + __builtin_bit_cast(float, (u32)b[1]);
}
template <bool NEG, bool BF> DI u32x4 cmul_pack(f32x4 re, f32x4 im, f32x4 c, f32x4 s) {
  f32x4 a = NEG ? re * c + im * s : re * c - im * s;
  f32x4 b = NEG ? im * c - re * s : im * c + re * s;
  u32x4 o;
#pragma unroll
  for (int r = 0; r < 4; ++r) o[r] = BF ? pk_bf16(a[r], b[r]) : pk_f16(a[r], b[r]);
  return o;
}
DI u32 rot16(u32 v) { return __builtin_amdgcn_alignbit(v, v, 16); }
template <bool NEG> DI void cmul_half(f32x4& a, f32x4& b, f32x4 re, f32x4 im, f32x4 c, f32x4 s) {
  a = NEG ? re * c + im * s : re * c - im * s;
  b = NEG ? im * c - re * s : im * c + re * s;
}
template <bool BF> DI u32x4 pack4(f32x4 a, f32x4 b) {
  u32x4 o;
#pragma unroll
  for (int r = 0; r < 4; ++r) o[r] = BF ? pk_bf16(a[r], b[r]) : pk_f16(a[r], b[r]);
  return o;
}
DI void glds_a(u32 ldsbase, int imm, u32 v0, u32 v1, u32x4 srd, u32 soff) {
  asm volatile("s_add_u32 m0, %0, %1\n\ts_nop 0\n\tbuffer_load_dwordx4 %2, %4, %5 offen lds\n\tbuffer_load_dwordx4 %3, %4, %5 offen offset:1024 lds"
               :: "s"(ldsbase), "i"(imm), "v"(v0), "v"(v1), "s"(srd), "s"(soff) : "memory", "scc");
}
DI void glds_b(u32 v2, u32 v3, u32x4 srd, u32 soff) {
  asm volatile("buffer_load_dwordx4 %0, %2, %3 offen offset:2048 lds\n\tbuffer_load_dwordx4 %1, %2, %3 offen offset:3072 lds"
               :: "v"(v2), "v"(v3), "s"(srd), "s"(soff) : "memory");
}
DI u32x4 cmulc_f16(f32x4 re, f32x4 im, u32x4 tw) {
  u32 z0 = pk_f16(re[0], im[0]), z1 = pk_f16(re[1], im[1]), z2 = pk_f16(re[2], im[2]), z3 = pk_f16(re[3], im[3]);
  u32 o0, o1, o2, o3;
  const u32 t0 = tw[0], t1 = tw[1], t2 = tw[2], t3 = tw[3];
#define PKM " op_sel_hi:[1,0]\n\t"
#define PKF " op_sel:[1,1,0] op_sel_hi:[0,1,1] neg_hi:[0,1,0]\n\t"
  asm("v_pk_mul_f16 %0, %4, %8" PKM "v_pk_mul_f16 %1, %5, %9" PKM "v_pk_mul_f16 %2, %6, %10" PKM "v_pk_mul_f16 %3, %7, %11" PKM
      "v_pk_fma_f16 %0, %4, %8, %0" PKF "v_pk_fma_f16 %1, %5, %9, %1" PKF "v_pk_fma_f16 %2, %6, %10, %2" PKF "v_pk_fma_f16 %3, %7, %11, %3" PKF "s_nop 1"
      : "=&v"(o0), "=&v"(o1), "=&v"(o2), "=&v"(o3) : "v"(z0), "v"(z1), "v"(z2), "v"(z3), "v"(t0), "v"(t1), "v"(t2), "v"(t3));
  return u32x4{o0, o1, o2, o3};
}
template <int N> struct IC { static constexpr int v = N; };
template <int A, int B, class Fn> DI void static_for(Fn&& f) { if constexpr (A < B) { f(IC<A>{}); static_for<A + 1, B>(f); } }

struct Frag { u32x4 bx[4], aw[2]; };
#define ALD4S(dst, voff, srd, SOFF, FL) asm volatile("buffer_load_dwordx4 %0, %1, %2, %3 offen" FL : "=v"(dst) : "v"(voff), "s"(srd), "s"(SOFF) : "memory")
#define ALD2S(dst, voff, srd, SOFF, FL) asm volatile("buffer_load_dwordx2 %0, %1, %2, %3 offen" FL : "=v"(dst) : "v"(voff), "s"(srd), "s"(SOFF) : "memory")
#define RAWBAR() do { asm volatile("s_barrier" ::: "memory"); } while (0)

typedef unsigned u32x16 __attribute__((ext_vector_type(16)));
__global__ __launch_bounds__(512, 2) void fused_kernel(const char* a_x, const unsigned short* a_wr, const unsigned short* a_wi, const char* a_wg,
                                                       const char* a_wu, const char* a_wd, char* a_ws, char* a_out) {
  extern __shared__ __attribute__((aligned(16))) char smem[];
  LAS char* sm = (LAS char*)smem;
  STAMP_DECL
  STAMP(0);
  u32x16 ka;
  asm volatile("s_load_dwordx16 %0, %1, 0x0" : "=s"(ka) : "s"(__builtin_amdgcn_kernarg_segment_ptr()) : "memory");
  const int b = blockIdx.x, tid = threadIdx.x, lane = tid & 63, w = __builtin_amdgcn_readfirstlane(tid >> 6);
  const int hw = w & 3, p16 = lane & 15, t = lane >> 4;
  const u32 lds0 = (u32)(uintptr_t)sm;
  {
    uint64_t pc; asm volatile("s_getpc_b64 %0" : "=s"(pc));
    asm volatile("s_cmp_gt_u32 %0, 7\n\ts_cbranch_scc1 1f\n\ts_mov_b32 m0, %1\n\ts_nop 0\n\tbuffer_load_dword %2, %3, 0 offen lds\n1:"
                 :: "s"(b), "s"(lds0), "v"(tid * 128), "s"(u32x4{(u32)pc, (u32)(pc >> 32), CODE_LINES * 128, 0x00020000u}) : "memory", "scc");
  }
  const bool is_gemm = b >= 32;
  const int xc = b & 7, r_ = (b - 32) >> 3;
  const int mi = (xc >> 1) * 4 + (r_ & 3), fj = (xc & 1) * 7 + (r_ >> 2);
  const int rot = ((r_ & 3) + (r_ >> 2)) & ROTM;
  const u32 lw = lds0 + hw * 4096;
  u32 vx[4], vg[4], vu[4];
#pragma unroll
  for (int j = 0; j < 4; ++j) {
    vx[j] = (mi * 128 + hw * 32 + j * 7 + (lane >> 3)) * 1024 + (((lane & 7) ^ ((4 * j + t) & 7)) << 4);
    vg[j] = (hw * 16 + j * 4 + t) * 3584 - j * 1024 + fj * 256 + ((p16 ^ (2 * t)) << 4);
    vu[j] = vg[j] ^ 128;
    PIN(vx[j]); PIN(vg[j]); PIN(vu[j]);
  }
  u32 vrow = (b + 256 * hw) * 1024 + lane * 16;
  asm volatile("s_cmp_lt_u32 %0, 4\n\ts_cbranch_scc1 1f\n\ts_setprio 2\n1:" :: "s"(w) : "scc");
  if (w < 4) vrow = 0x7ff00000u;
  const u32 lrow = w < 4 ? lw : lds0 + FS + hw * 1024;
  PIN(vrow);
  SB();
  asm volatile("s_waitcnt lgkmcnt(0)" : "+s"(ka) :: "memory");
#define KARG(i) (((uint64_t)ka[2 * (i) + 1] << 32) | ka[2 * (i)])
  const u32x4 sx = {ka[0], ka[1], 2097152u, 0x00020000u}, sg = {ka[6], ka[7], 1835008u, 0x00020000u}, su = {ka[8], ka[9], 1835008u, 0x00020000u};
  auto g1_issue = [&](int st, int k) {
    const int lb = (st % 3) * G1_STAGE + (k >> 1) * 16384, sk = st ^ rot;
    if (k == 0) glds_a(lw, lb, vx[0], vx[1], sx, sk * 128);
    if (k == 1) glds_b(vx[2], vx[3], sx, sk * 128);
    if (k == 2) glds_a(lw, lb, vg[0], vg[1], sg, sk * 64 * 3584);
    if (k == 3) glds_b(vg[2], vg[3], sg, sk * 64 * 3584);
    if (k == 4) glds_a(lw, lb, vu[0], vu[1], su, sk * 64 * 3584);
    if (k == 5) glds_b(vu[2], vu[3], su, sk * 64 * 3584);
  };
  glds16(lrow, 0, vrow, sx, 0);
  glds16(lrow + (w < 4 ? 0 : 4096), 0, vrow, sx, 1048576);
  if (w < 4 && is_gemm) {
#pragma unroll
    for (int k = 0; k < 6; ++k) g1_issue(0, k);
  }
  SB();
  STAMP(30);
  const f32x4 z4 = {0.f, 0.f, 0.f, 0.f};
  const GAS unsigned short* wr = (const GAS unsigned short*)KARG(1); const GAS unsigned short* wi = (const GAS unsigned short*)KARG(2);
  GAS char* ws = (GAS char*)KARG(6); GAS char* out = (GAS char*)KARG(7);
  auto load_tag = [&]() {
    u32 epoch;
    asm volatile("s_load_dword %0, %1, 0x0\n\ts_waitcnt lgkmcnt(0)" : "=s"(epoch) : "s"(ws) : "memory");
    return epoch * 0x9E3779B1u + 0x7F4A7C15u;
  };
  const rsrc_t rw = __builtin_amdgcn_make_buffer_rsrc((void*)ws, 0, WS_END, 0x00020000);
  const int jp = b & 7, g = b >> 3;
  const int rt = (b & 7) * 4 + ((b >> 3) & 3), ct = b >> 5;
  auto wslot = [&](int s) { return (s < 3 ? 2 * G1_STAGE : s < 6 ? -3 * 16384 : G1_STAGE - 6 * 16384) + s * 16384; };
  u32x4 wf[7][4];
  const int wbase = hw * 4096 + t * 1024 + (p16 >> 2) * 128 + (p16 & 3) * 8 + ((((p16 >> 3) | ((t & 1) << 1))) << 5);
  auto wd_read = [&](int ks, int s) {
#pragma unroll
    for (int u = 0; u < 4; ++u) {
      LAS char* p = sm + wslot(s) + (wbase ^ (u << 5));
      u32x2 lo = tr_read(p), hi = tr_read(p + 512);
      wf[ks][u] = u32x4{lo[0], lo[1], hi[0], hi[1]};
    }
  };
  u32 tag;
  auto ksof = [&](int i) { int k = i + 2 * (rt & 3); return k >= 7 ? k - 7 : k; };
  const u32 hs = WS_H + ((rt * 4) * 56 + w * 7) * 1024 + lane * 16;
  auto ld_h = [&](int j, int i) { return __builtin_amdgcn_raw_buffer_load_b128(rw, hs, (((j + ct) & 3) * 56 + ksof(i)) * 1024, 16); };
  u32x4 hf[4][7];
  const u32x4 sd_ = {ka[10], ka[11], 1835008u, 0x00020000u};
  u32 vwd[4];
  auto wd_setup = [&]() {
#pragma unroll
    for (int j = 0; j < 4; ++j) { vwd[j] = (j * 7 + (lane >> 3)) * 1024 + ct * 128 + (((lane & 7) ^ (((lane >> 4) & 1) << 1) ^ ((j & 1) << 2)) << 4); PIN(vwd[j]); }
  };
  auto wd_issue = [&](int kw, int i, int s) {
    const u32 so = kw * (224 * 1024) + ksof(i) * 32768;
    SB(); glds_a(lw, wslot(s), vwd[0], vwd[1], sd_, so); glds_b(vwd[2], vwd[3], sd_, so); SB();
  };
  auto wait_h_and_load = [&]() {
    const u32 foff = WS_F2 + rt * 256 + (w * 7 + (lane < 7 ? lane : 0)) * 4;
#pragma nounroll
    for (int spin = 0; spin < SPIN_CAP; ++spin) {
      asm volatile("" ::: "memory");
      u32 fl = __builtin_amdgcn_raw_buffer_load_b32(rw, foff, 0, 16);
      if (all_ok(fl == tag)) break;
      __builtin_amdgcn_s_sleep(POLLSLEEP);
    }
    __builtin_amdgcn_fence(__ATOMIC_ACQUIRE, "wavefront");
    asm volatile("" ::: "memory");
    STAMP(15);
#pragma unroll
    for (int tt = 0; tt < 3; ++tt)
#pragma unroll
      for (int i = 0; i < 7; ++i) hf[tt][i] = ld_h(tt, i);
    SB();
  };
  const float pf = (float)p16, tf = (float)t;

  auto helper = [&](auto G_) {
    constexpr bool G = decltype(G_)::v;
    const int ht = tid & 255;
    const u32x4 sw = {ka[12], ka[13], WS_END, 0x00020000u};
    const u32x4 sfr_ = {ka[2], ka[3], 2048 * DF_ * 2, 0x00020000u}, sfi_ = {ka[4], ka[5], 2048 * DF_ * 2, 0x00020000u};
    SB();
#define DMA(st, k) do { if (G) { SB(); g1_issue(st, k); SB(); } } while (0)
#define GWAIT(n) do { if (G) WAITV(n); } while (0)
#define WAITG(n) do { if (G) WAITV(n); else WAITV(0); } while (0)
#define GBAR() do { if (G) RAWBAR(); } while (0)
    auto hsync = [&](int off, int stride = 0) {
      asm volatile("s_waitcnt lgkmcnt(0)" ::: "memory");
      *(volatile LAS u32*)(sm + off + (lane & 3) * stride + hw * 4) = tag;
#pragma nounroll
      for (int spin = 0; spin < SPIN_CAP; ++spin) { if (all_ok(*(volatile LAS u32*)(sm + off + hw * stride + (lane & 3) * 4) == tag)) break; }
      asm volatile("" ::: "memory");
    };
#define XBAR(n) do { if (G) BAR(); else hsync(G1_STAGE + (n) * 16); } while (0)
    u32x4 b2re[2], b2im[2], tw[2], ac2re, ac2im, bc3re, bc3im, qa8, qa8i, bc1re, bc1im, ac4re, ac4im, t1, bi1re[2], bi1im[2];
    u32x2 b1re, b1im;
    f32x4 t2c, t2s, t4c[2], t4s[2];
    {
      const float d5 = pf * (1.0f / 512), a5 = 4.0f * tf * d5;
#pragma unroll
      for (int r = 0; r < 4; ++r) {
        const float th = a5 + r * d5, c = cosr(th * 32.0f), s = sinr(th * 32.0f);
        ac2re[r] = pk_f16(c, s); ac2im[r] = rot16(ac2re[r]) ^ 0x8000u;
        bc3im[r] = pk_bf16(s, c); bc3re[r] = rot16(bc3im[r]) ^ 0x80000000u;
#pragma unroll
        for (int hh = 0; hh < 2; ++hh) {
          const float tq = th + hh * (16.0f * d5);
          tw[hh][r] = pk_f16(cosr(tq), sinr(tq));
          b2re[hh][r] = pk_f16(cosr(tq * 16.0f), sinr(tq * 16.0f)); b2im[hh][r] = rot16(b2re[hh][r]) ^ 0x8000u;
        }
      }
      b1re = u32x2{__builtin_amdgcn_perm(ac2re[1], ac2re[0], 0x05040100u), __builtin_amdgcn_perm(ac2re[3], ac2re[2], 0x05040100u)};
      b1im = u32x2{__builtin_amdgcn_perm(ac2im[1], ac2im[0], 0x05040100u), __builtin_amdgcn_perm(ac2im[3], ac2im[2], 0x05040100u)};
      const int po = p16 & 1, j8 = p16 >> 1;
      const float j8f = (float)j8 * 0.125f;
      const float ra = 2.0f * tf * j8f, rb = ra + j8f;
      const float ca = cosr(ra), sa = sinr(ra), cb = cosr(rb), sb = sinr(rb);
      const u32 fa0 = pk_f16(ca, sa), fb0 = pk_f16(cb, sb);
      const u32 fa = po ? (rot16(fa0) ^ 0x8000u) : fa0, fb = po ? (rot16(fb0) ^ 0x8000u) : fb0;
      const u32 sel = (j8 & 3) == 0 ? 0x03020100u : (j8 < 4 ? 0x0c0c0100u : 0x01000c0cu);
      qa8 = u32x4{fa, fb, __builtin_amdgcn_perm(0u, fa, sel), __builtin_amdgcn_perm(0u, fb, sel)};
      const u32 ia = pk_bf16(sa, ca), ib = pk_bf16(sb, cb), pa = rot16(ia) ^ 0x80000000u, pb = rot16(ib) ^ 0x80000000u;
      const bool on = (po == 0) ? (t < 2) : (t >= 2);
      const u32 hc = pk_bf16(0.5f * ca, 0.f);
      qa8i = u32x4{po ? ia : pa, po ? ib : pb, (t & 1) == 0 ? (po ? hc << 16 : hc) : (on ? pa : 0u), on ? pb : 0u};
      PIN(ac2re); PIN(ac2im); PIN(bc3re); PIN(bc3im); PIN(tw[0]); PIN(tw[1]); PIN(b2re[0]); PIN(b2re[1]); PIN(b2im[0]); PIN(b2im[1]); PIN(b1re); PIN(b1im); PIN(qa8); PIN(qa8i);
    }
    SB();
    tag = load_tag();
    STAMP(21);
    LAS char* const img = sm + FS + hw * 1024;
    u32 stat[2];
    u32x4 xr[2];
    WAITV(0);
    STAMP(1);
    {
      const u32 vo = (8 * b + 2 * hw) * DF_ * 2 + (lane >> 5) * (DF_ * 2 + 2) + (lane & 31) * 16;
      glds16(lds0 + hw * 1024, FS + 8192, vo, sfr_, 0); glds16(lds0 + hw * 1024, FS + 12288, vo, sfi_, 0);
    }
    auto tpat = [&](int q, int u) { const u32 r = tag >> (1 + 7 * q + 3 * u); return (tag & 1u) | ((r & 3u) << 1) | ((r & 28u) << 14); };
    {
      float v[2][8], mean[2], var[2];
#pragma unroll
      for (int a = 0; a < 2; ++a) {
        const u32x4 xv = *(LAS u32x4*)(img + a * 4096 + (4 * p16 + t) * 16);
        xr[a] = xv;
#pragma unroll
        for (int k = 0; k < 4; ++k) { v[a][2 * k] = bf_lo(xv[k]); v[a][2 * k + 1] = bf_hi(xv[k]); }
      }
#pragma unroll
      for (int a = 0; a < 2; ++a) mean[a] = xrow_sum(row_sum(((v[a][0] + v[a][1]) + (v[a][2] + v[a][3])) + ((v[a][4] + v[a][5]) + (v[a][6] + v[a][7])))) * (1.0f / 512);
#pragma unroll
      for (int a = 0; a < 2; ++a) {
        float q0 = 0.f, q1 = 0.f;
#pragma unroll
        for (int k = 0; k < 4; ++k) { float d = v[a][k] - mean[a], e = v[a][k + 4] - mean[a]; q0 += d * d; q1 += e * e; }
        var[a] = xrow_sum(row_sum(q0 + q1)) * (1.0f / 512);
      }
#pragma unroll
      for (int a = 0; a < 2; ++a) {
        stat[a] = pk_bf16(mean[a], __builtin_amdgcn_sqrtf(rnd_bf(rnd_bf(var[a]) + 1e-5f)));
        const float sc = __builtin_amdgcn_rcpf(bf_hi(stat[a])) * XS, of = -bf_lo(stat[a]) * sc;
        u32x4 xh;
#pragma unroll
        for (int k = 0; k < 4; ++k) xh[k] = pk_f16(__builtin_fmaf(v[a][2 * k], sc, of), __builtin_fmaf(v[a][2 * k + 1], sc, of));
        *(LAS u32x4*)(img + a * 4096 + (((4 * p16 + t) ^ ((p16 >> 1) & 2)) << 4)) = xh;
      }
      asm volatile("" ::: "memory");
      STAMP(23);
#pragma unroll
      for (int k = 0; k < 6; ++k) DMA(2, k);
      u32x2 tra[2][2];
#pragma unroll
      for (int a = 0; a < 2; ++a)
#pragma unroll
        for (int hh = 0; hh < 2; ++hh) tra[a][hh] = tr_read(img + a * 4096 + (4 * t + (p16 >> 2)) * 64 + ((hh ^ (t & 1)) << 5) + (p16 & 3) * 8);
      f32x4 d1re[2][2], d1im[2][2], d2re[2], d2im[2];
#pragma unroll
      for (int a = 0; a < 2; ++a)
#pragma unroll
        for (int hh = 0; hh < 2; ++hh) { d1re[a][hh] = mfma16_f16(tra[a][hh], b1re, z4); d1im[a][hh] = mfma16_f16(tra[a][hh], b1im, z4); }
#pragma unroll
      for (int a = 0; a < 2; ++a)
#pragma unroll
        for (int hh = 0; hh < 2; ++hh) {
          u32x4 ta = cmulc_f16(d1re[a][hh], d1im[a][hh], tw[hh]);
          d2re[a] = mfma32_f16(ta, b2re[hh], hh ? d2re[a] : z4); d2im[a] = mfma32_f16(ta, b2im[hh], hh ? d2im[a] : z4);
        }
      float ny[2];
#pragma unroll
      for (int a = 0; a < 2; ++a) { f32x4 q = d1re[a][0] + d1re[a][1]; ny[a] = xrow_sum((q[0] - q[1]) + (q[2] - q[3])); }
      int e = 4 * p16 + t; e ^= (e >> 3) & 3;
#pragma unroll
      for (int a = 0; a < 2; ++a) {
        if (lane == 0) d2im[a][0] = ny[a];
        *(LAS u32x4*)(img + a * 4096 + e * 16) = pack4<false>(d2re[a], d2im[a]);
      }
    }
    STAMP(24);
    WAITG(12);
    XBAR(0);
    STAMP(2);
#pragma unroll
    for (int k = 0; k < 6; ++k) DMA(3, k);
    u32x4 uo[2][2];
    {
      u32x2 e0[2], e1[2];
#pragma unroll
      for (int k = 0; k < 2; ++k) {
        int e = 16 * hw + 8 * k + (p16 >> 1); e ^= (e >> 3) & 3;
        e0[k] = *(LAS u32x2*)(sm + FS + (2 * t) * 1024 + e * 16 + (p16 & 1) * 8);
        e1[k] = *(LAS u32x2*)(sm + FS + (2 * t + 1) * 1024 + e * 16 + (p16 & 1) * 8);
      }
      u32 wv[2][2][2];
      {
        LAS char* fp = sm + FS + 8192 + t * 1024 + hw * 128 + p16 * 4;
#pragma unroll
        for (int hf = 0; hf < 2; ++hf)
#pragma unroll
          for (int k = 0; k < 2; ++k)
#pragma unroll
            for (int u = 0; u < 2; ++u) {
              LAS char* q = fp + hf * 510 + k * 64 + u * 2;
              wv[hf][k][u] = (u32)*(LAS unsigned short*)q | ((u32)*(LAS unsigned short*)(q + 4096) << 16);
            }
      }
      f32x4 d8[4];
#pragma unroll
      for (int u = 0; u < 4; ++u) d8[u] = mfma16_f16(u32x2{qa8[0], qa8[1]}, u32x2{e0[u >> 1][u & 1], e1[u >> 1][u & 1]}, z4);
      f32x4 d8s = mfma16_f16(u32x2{qa8[2], qa8[3]}, u32x2{e0[0][0], e1[0][0]}, z4);
      if ((hw | p16) == 0) d8[0] = d8s;
      const u32 p0 = tpat(p16 & 3, 0), p1 = tpat(p16 & 3, 1);
#pragma unroll
      for (int hf = 0; hf < 2; ++hf)
#pragma unroll
        for (int k = 0; k < 2; ++k) {
          uo[hf][k] = u32x4{(pk_f16(d8[2 * k][2 * hf], d8[2 * k][2 * hf + 1]) & 0xfff8fff8u) | p0, wv[hf][k][0],
                            (pk_f16(d8[2 * k + 1][2 * hf], d8[2 * k + 1][2 * hf + 1]) & 0xfff8fff8u) | p1, wv[hf][k][1]};
          PIN(uo[hf][k]);
        }
    }
    auto ustore = [&]() {
      const u32 ub = WS_U + b * 16384 + (2 * t) * 2048 + hw * 512 + p16 * 16;
#pragma unroll
      for (int hf = 0; hf < 2; ++hf)
#pragma unroll
        for (int k = 0; k < 2; ++k) st_sc1(rw, ub + hf * 2048 + k * 256, uo[hf][k]);
      if (lane < 2) st_sc1_2(rw, WS_ST + b * 64 + hw * 8 + lane * 32, u32x2{lane ? stat[1] : stat[0], tag});
      STAMP(19);
    };
    ustore();
    GWAIT(12 + 5);
    GBAR();
    STAMP(3);
    {
      const float jf = (float)jp;
      const float d1 = (8.0f * pf + jf) * (1.0f / 2048), a1 = 4.0f * tf * d1, d2 = pf * (1.0f / 256), a2 = (32.0f * tf + jf) * (pf * (1.0f / 2048));
      DMA(4, 0); DMA(4, 1);
#pragma unroll
      for (int r = 0; r < 4; ++r) {
        DMA(4, 2 + r);
        const float n1 = a1 + r * d1, n2 = a2 + r * d2;
        bc1re[r] = pk_f16(cosr(n1 * 16.0f), sinr(n1 * 16.0f)); bc1im[r] = rot16(bc1re[r]) ^ 0x8000u;
        t1[r] = pk_f16(cosr(n1), sinr(n1));
        ac4im[r] = pk_bf16(sinr(n2 * 16.0f), cosr(n2 * 16.0f)); ac4re[r] = rot16(ac4im[r]) ^ 0x80000000u;
        t2c[r] = cosr(n2); t2s[r] = sinr(n2);
      }
      PIN(bc1re); PIN(bc1im); PIN(ac4re); PIN(ac4im); PIN(t1); PIN(t2c); PIN(t2s);
    }
    u32x4 ug[4];
    const int uq = ht & 3;
    const int ui = (((ht >> 2) & 3) << 4) + (ht >> 4);
    const u32 uoff = WS_U + ui * 16384 + jp * 2048 + g * 64 + uq * 16;
    auto caddr = [&](int i) { return (LAS char*)(sm + FS + uq * 2048 + ((((i & 192) + 4 * (i & 15) + ((i >> 4) & 3)) * 4) ^ (uq << 5))); };
    if (!G) {
#pragma nounroll
      for (int spin = 0; spin < SPIN_CAP; ++spin) {
        asm volatile("" ::: "memory");
        u32x2 q = ld_sc1_2(rw, WS_ST + (b + 224) * 64 + hw * 8);
        if (all_ok(q[1] == tag)) break;
        __builtin_amdgcn_s_sleep(8);
      }
      __builtin_amdgcn_s_sleep(20);
    }
    STAMP(26);
#pragma unroll
    for (int k = 0; k < 4; ++k) ALD4S(ug[k], uoff, sw, k * 64 * 16384, " sc1");
    WAITG(5 + 12 + 4);
    XBAR(2);
    STAMP(4);
#pragma unroll
    for (int k = 0; k < 6; ++k) DMA(5, k);
    STAMP(27);
    GWAIT(12);
    PIN(ug[0]); PIN(ug[1]); PIN(ug[2]); PIN(ug[3]);
    const u32 c0 = tpat(uq, 0), c1 = tpat(uq, 1);
#define CHK1 all_ok(((((ug[0][0] ^ c0) | (ug[1][0] ^ c0) | (ug[2][0] ^ c0) | (ug[3][0] ^ c0) | (ug[0][2] ^ c1) | (ug[1][2] ^ c1) | (ug[2][2] ^ c1) | (ug[3][2] ^ c1)) & 0x00070007u) == 0))
    if (__builtin_expect(!CHK1, 0)) {
#pragma nounroll
      for (int spin = 0; spin < SPIN_CAP; ++spin) {
        asm volatile("" ::: "memory");
#pragma unroll
        for (int k = 0; k < 4; ++k) ug[k] = ld_sc1(rw, uoff + k * 64 * 16384);
        if (CHK1) break;
      }
    }
    {
      LAS char* cp = caddr(ui);
      const u32 x0 = c0 ^ 0x00040004u, x1 = c1 ^ 0x00040004u;
#pragma unroll
      for (int k = 0; k < 4; ++k) {
        *(LAS u32*)(cp + k * 256) = ug[k][0] ^ x0; *(LAS u32*)(cp + k * 256 + 1024) = ug[k][2] ^ x1;
        *(LAS u32*)(cp + k * 256 + 8192) = ug[k][1]; *(LAS u32*)(cp + k * 256 + 9216) = ug[k][3];
      }
    }
    u32x4 fsp[2];
    const bool special = !G && b < 8 && hw == 0;
    if (special) {
#pragma nounroll
      for (int spin = 0; spin < SPIN_CAP; ++spin) {
        asm volatile("" ::: "memory");
        bool ok = true;
#pragma unroll
        for (int c = 0; c < 2; ++c) {
          const u32x4 fa = ld_sc1(rw, WS_SF + jp * 4096 + c * 2048 + lane * 32), fb = ld_sc1(rw, WS_SF + jp * 4096 + c * 2048 + lane * 32 + 16);
          ok &= tags_ok(fa, tag) & tags_ok(fb, tag);
          fsp[c] = u32x4{fa[0], fa[2], fb[0], fb[2]};
        }
        if (all_ok(ok)) break;
      }
    }
    STAMP(28);
    XBAR(3);
    STAMP(5);
    LAS char* const colp = sm + FS + hw * 2048 + ((lane * 16) ^ (hw << 5));
    {
      u32x4 uin[2], fin[2];
#pragma unroll
      for (int a = 0; a < 2; ++a) { uin[a] = *(LAS u32x4*)(colp + a * 1024); fin[a] = *(LAS u32x4*)(colp + 8192 + a * 1024); }
      const bool two = special && (jp & 3) == 0;
      f32x4 dre[2], dim[2], keep;
      auto col_xform = [&](auto NA_) {
        constexpr int NA = decltype(NA_)::v;
        u32x4 ta[2];
        DMA(6, 0);
#pragma unroll
        for (int a = 0; a < NA; ++a) { dre[a] = mfma32_f16(uin[a], bc1re, z4); dim[a] = mfma32_f16(uin[a], bc1im, z4); }
        DMA(6, 1);
#pragma unroll
        for (int a = 0; a < NA; ++a) { ta[a] = cmulc_f16(dre[a], dim[a], t1); dre[a] = mfma32_f16(ac2re, ta[a], z4); dim[a] = mfma32_f16(ac2im, ta[a], z4); }
        DMA(6, 2);
#pragma unroll
        for (int a = 0; a < NA; ++a) {
          f32x4 fr_, fi_;
#pragma unroll
          for (int r = 0; r < 4; ++r) { fr_[r] = bf_lo(fin[a][r]); fi_[r] = bf_hi(fin[a][r]); }
          ta[a] = cmul_pack<false, true>(dre[a], dim[a], fr_, fi_); dre[a] = mfma32_bf16(ta[a], bc3re, z4); dim[a] = mfma32_bf16(ta[a], bc3im, z4);
          DMA(6, 3 + a);
        }
#pragma unroll
        for (int a = 0; a < NA; ++a) {
          ta[a] = cmul_pack<false, true>(dre[a], dim[a], t2c, t2s); dre[a] = mfma32_bf16(ac4re, ta[a], z4); dim[a] = mfma32_bf16(ac4im, ta[a], z4);
          if (a == 0) DMA(6, 5);
        }
      };
      if (!G) {
        if (special) {
          fin[0] = jp > 4 ? fsp[1] : fsp[0];
          if (two) {
            const u32x4 us = uin[0];
            uin[0] = us >> 16; fin[0] = fsp[1];
            col_xform(IC<1>{});
            keep = dre[0];
            uin[0] = us & 0xffffu; fin[0] = fsp[0];
          }
        }
      }
      col_xform(IC<2>{});
      if (!G) { if (two) dim[0] = keep; }
#pragma unroll
      for (int a = 0; a < 2; ++a) *(LAS u32x4*)(colp + a * 1024) = pack4<true>(dre[a], dim[a]);
      {
        const u32 vo = b * 7168 + hw * 2048 + lane * 16;
        glds_a(lds0 + hw * 2048, FS + 8192, vo, (hw < 3 ? vo : 0x7fffffffu), sd_, 0);
      }
#define NW 2
    }
    STAMP(29);
    GWAIT(12 + NW);
    XBAR(4);
    STAMP(6);
    {
      const int ps = ht >> 2;
      u32 vo[4][2];
#pragma unroll
      for (int k = 0; k < 4; ++k) { LAS char* cp = caddr((ps >> 5) + 2 * k + 8 * (ps & 31)); vo[k][0] = *(LAS u32*)cp; vo[k][1] = *(LAS u32*)(cp + 1024); }
#pragma unroll
      for (int k = 0; k < 6; ++k) DMA(7, k);
#pragma unroll
      for (int k = 0; k < 4; ++k) st_sc1(rw, WS_V + b * 16384 + k * 4096 + ht * 16, u32x4{vo[k][0], tag, vo[k][1], tag});
    }
    const unsigned long long t_v = __builtin_amdgcn_s_memrealtime();
    STAMP(20);
    GWAIT(NW + 12 + 4);
    XBAR(5);
    STAMP(7);
    wd_setup();
    u32x2 csv[4], stv;
    const u32 csoff = WS_CS + ((t & 1) * F_ + fj * 128 + hw * 32 + (lane >> 5) * 16 + ((p16 >> 2) & 1) * 8 + (p16 >> 3) * 4 + (p16 & 3)) * 8;
    const u32 stoff = WS_ST + (((mi & 1) * 128 + hw * 32 + (lane & 31)) * 8 + (mi >> 1)) * 8;
    if (G) {
      ALD2S(csv[0], csoff, sw, 0, " sc1"); ALD2S(csv[1], csoff, sw, 2 * F_ * 8, " sc1"); ALD2S(csv[2], csoff, sw, 4 * F_ * 8, " sc1"); ALD2S(csv[3], csoff, sw, 6 * F_ * 8, " sc1");
      ALD2(stv, stoff, sw, 0, " sc1");
    }
    if (G) { WAITV(4 + 5); *(volatile LAS u32*)(sm + FS + 1072 + hw * 4) = tag; }
#pragma unroll
    for (int hh = 0; hh < 2; ++hh) {
      wd_issue(w, hh, hh);
      const float n2 = (float)(8 * (p16 >> 2) + (p16 & 3)) + 4.0f * hh;
#pragma unroll
      for (int m = 0; m < 4; ++m) {
        float rev = n2 * ((tf + 4.0f * m) * (1.0f / 32));
        bi1im[hh][m] = pk_bf16(sinr(rev), cosr(rev)); bi1re[hh][m] = rot16(bi1im[hh][m]) ^ 0x80000000u;
        float rev2 = n2 * ((4.0f * tf + m) * (1.0f / 512));
        t4c[hh][m] = cosr(rev2); t4s[hh][m] = sinr(rev2);
      }
      PIN(bi1re[hh]); PIN(bi1im[hh]); PIN(t4c[hh]); PIN(t4s[hh]);
    }
    wd_issue(w, 2, 2);
    if (G) {
      WAITV(12);
      PIN(csv[0]); PIN(csv[1]); PIN(csv[2]); PIN(csv[3]); PIN(stv);
#define CHK3 all_ok((stv[1] == tag) & (csv[0][1] == tag) & (csv[1][1] == tag) & (csv[2][1] == tag) & (csv[3][1] == tag))
      if (__builtin_expect(!CHK3, 0)) {
#pragma nounroll
        for (int spin = 0; spin < SPIN_CAP; ++spin) {
          asm volatile("" ::: "memory");
#pragma unroll
          for (int k = 0; k < 4; ++k) csv[k] = ld_sc1_2(rw, csoff + k * 2 * F_ * 8);
          stv = ld_sc1_2(rw, stoff);
          if (CHK3) break;
        }
      }
      const u32 s0 = csv[0][0], s1 = csv[1][0], s2 = csv[2][0], s3 = csv[3][0];
      const float cs = (__builtin_bit_cast(float, s0) + __builtin_bit_cast(float, s1)) + (__builtin_bit_cast(float, s2) + __builtin_bit_cast(float, s3));
      const float ch = rnd_bf(cs);
      LAS char* cp = sm + FS + hw * 2048 + lane * 4;
      *(LAS u32*)cp = pk_bf16(ch, cs - ch);
      *(LAS u32*)(cp + 256) = __builtin_amdgcn_perm(0u, stv[0], 0x01000100u) ^ 0x80008000u;
    }
    u32x4 rv[2][2];
    const u32 voff = WS_V + ((b & 7) * 32 + (b >> 3)) * 64 + (p16 & 3) * 16 + ((8 * hw + (p16 >> 2)) * 8 + 2 * t) * 16384;
    if (!G) {
      WAITV(12);
#pragma nounroll
      for (int spin = 0; spin < SPIN_CAP; ++spin) {
        asm volatile("" ::: "memory");
        u32x2 q = ld_sc1_2(rw, WS_V + (b + 224) * 16384 + hw * 4096);
        if (all_ok(q[1] == tag)) break;
        __builtin_amdgcn_s_sleep(8);
      }
      __builtin_amdgcn_s_sleep(20);
    }
    hsync(G ? FS + 1088 : G1_STAGE + 96);
    if (G) { while (__builtin_amdgcn_s_memrealtime() - t_v < V_DELAY) __builtin_amdgcn_s_sleep(2); }
    STAMP(8);
    {
      ALD4S(rv[0][0], voff, sw, 0, " sc1"); ALD4S(rv[0][1], voff, sw, 16384, " sc1"); ALD4S(rv[1][0], voff, sw, 32 * 16384, " sc1"); ALD4S(rv[1][1], voff, sw, 33 * 16384, " sc1");
    }
    auto mwait = [&](int off) {
      if (G) {
#pragma nounroll
        for (int spin = 0; spin < SPIN_CAP; ++spin) { if (all_ok(*(volatile LAS u32*)(sm + off + (lane & 3) * 4) == tag)) break; __builtin_amdgcn_s_sleep(1); }
        asm volatile("" ::: "memory");
      }
    };
    WAITV(4);
    wd_read(0, 0); wd_read(1, 1); wd_read(2, 2);
    STAMP(9);
    WAITV(0);
    PIN(rv[0][0]); PIN(rv[0][1]); PIN(rv[1][0]); PIN(rv[1][1]);
#define CHK4 all_ok(tags_ok(rv[0][0], tag) & tags_ok(rv[0][1], tag) & tags_ok(rv[1][0], tag) & tags_ok(rv[1][1], tag))
    if (__builtin_expect(!CHK4, 0)) {
#pragma nounroll
      for (int spin = 0; spin < SPIN_CAP; ++spin) {
        asm volatile("" ::: "memory");
#pragma unroll
        for (int k = 0; k < 2; ++k) { rv[k][0] = ld_sc1(rw, voff + k * 32 * 16384); rv[k][1] = ld_sc1(rw, voff + k * 32 * 16384 + 16384); }
        if (CHK4) break;
        __builtin_amdgcn_s_sleep(4);
      }
    }
    STAMP(12);
    LAS char* const yb = sm + FS + 8192;
#pragma unroll
    for (int k = 0; k < 2; ++k) {
      f32x4 da = mfma16_bf16(u32x2{qa8i[0], qa8i[1]}, u32x2{rv[k][0][0], rv[k][1][0]}, z4), db = mfma16_bf16(u32x2{qa8i[0], qa8i[1]}, u32x2{rv[k][0][2], rv[k][1][2]}, z4);
      if (k == 0) { f32x4 ds = mfma16_bf16(u32x2{qa8i[2], qa8i[3]}, u32x2{rv[0][0][0], rv[0][1][0]}, z4); if ((hw | p16) == 0) da = ds; }
      *(LAS u32x2*)(yb + (2 * t) * 1024 + (32 * hw + 16 * k + p16) * 8) = u32x2{pk_bf16(da[0], da[1]), pk_bf16(db[0], db[1])};
      *(LAS u32x2*)(yb + (2 * t + 1) * 1024 + (32 * hw + 16 * k + p16) * 8) = u32x2{pk_bf16(da[2], da[3]), pk_bf16(db[2], db[3])};
    }
    asm volatile("s_waitcnt lgkmcnt(0)" ::: "memory");
    *(volatile LAS u32*)(sm + FS + 1024 + hw * 4) = tag;
    mwait(FS + 1056);
    wd_issue(w, 3, 3); wd_issue(w, 4, 4); wd_issue(w, 5, 5); wd_issue(w, 6, 0);
    hsync(FS + 1024);
    STAMP(13);
    u32x4 ro[2];
#pragma unroll
    for (int a = 0; a < 2; ++a) {
      u32x4 av;
#pragma unroll
      for (int m = 0; m < 4; ++m) av[m] = *(LAS u32*)(yb + (hw + 4 * a) * 1024 + (lane + 64 * m) * 4);
      const float nyq = bf_hi(*(LAS u32*)(yb + (hw + 4 * a) * 1024));
#pragma unroll
      for (int hh = 0; hh < 2; ++hh) {
        f32x4 gre = mfma32_bf16(av, bi1re[hh], z4), gim = mfma32_bf16(av, bi1im[hh], z4);
        u32x4 bb = cmul_pack<false, true>(gre, gim, t4c[hh], t4s[hh]);
        f32x4 d = mfma32_bf16(bb, bc3re, f32x4{nyq, -nyq, nyq, -nyq});
        ro[a][2 * hh] = pk_bf16(__builtin_fmaf(d[0], FSC, bf_lo(xr[a][2 * hh])), __builtin_fmaf(d[1], FSC, bf_hi(xr[a][2 * hh])));
        ro[a][2 * hh + 1] = pk_bf16(__builtin_fmaf(d[2], FSC, bf_lo(xr[a][2 * hh + 1])), __builtin_fmaf(d[3], FSC, bf_hi(xr[a][2 * hh + 1])));
      }
      PIN(ro[a]);
    }
    STAMP(14);
#pragma unroll
    for (int a = 0; a < 2; ++a) st_sc1(rw, WS_R + (b + 256 * hw + 1024 * a) * 1024 + (4 * p16 + t) * 16, ro[a]);
    WAITV(0);
    if (lane < 2) st_sc1_2(rw, WS_ST2 + b * 64 + hw * 8 + lane * 32, u32x2{lane ? stat[1] : stat[0], tag});
    STAMP(11);
  };

  if (w < 4) {
    if (is_gemm) {
      tag = load_tag();
      const int h = lane >> 5;
      int xoff[4], woff[2];
#pragma unroll
      for (int ks = 0; ks < 4; ++ks) { int tok = lane & 31; xoff[ks] = tok * 128 + (((2 * ks + h) ^ ((tok >> 1) & 7)) << 4); PIN(xoff[ks]); }
      {
        int arr = t & 1, q = p16 >> 2, p = lane & 3;
#pragma unroll
        for (int m = 0; m < 2; ++m) {
          woff[m] = 16384 + arr * 16384 + (8 * h + q) * 256 + (((hw * 2 + m) ^ (q | (arr << 2))) << 5) + (p & 1) * 16 + (p >> 1) * 8;
          PIN(woff[m]);
        }
      }
      auto rdx = [&](Frag& f, int st, int ks, int n) { f.bx[n] = *(LAS u32x4*)(sm + (st % 3) * G1_STAGE + xoff[ks] + n * 4096); };
      auto rdw = [&](Frag& f, int st, int ks, int m) {
        LAS char* buf = sm + (st % 3) * G1_STAGE;
        u32x2 lo = tr_read(buf + woff[m] + ks * 4096), hi = tr_read(buf + woff[m] + ks * 4096 + 1024);
        f.aw[m] = u32x4{lo[0], lo[1], hi[0], hi[1]};
      };
      f32x16 acc[2][4];
#pragma unroll
      for (int m = 0; m < 2; ++m)
#pragma unroll
        for (int n = 0; n < 4; ++n)
#pragma unroll
          for (int k = 0; k < 16; ++k) acc[m][n][k] = 0.f;
      Frag fr[2];
      u32 qa[2], qb[4], pv;
      auto twait = [&](int off) {
#pragma nounroll
        for (int spin = 0; spin < SPIN_CAP; ++spin) { if (all_ok(*(volatile LAS u32*)(sm + off + (lane & 3) * 4) == tag)) break; }
        asm volatile("" ::: "memory");
      };
      SB();
      STAMP(21);
      g1_issue(1, 0); g1_issue(1, 1);
      WAITV(4);
      {
        LAS char* mb = sm + G1_STAGE + 32768 + 3072;
        *(volatile LAS u32*)(mb + (lane & 3) * 4096 + hw * 4) = tag;
#pragma nounroll
        for (int spin = 0; spin < SPIN_CAP; ++spin) { if (all_ok(*(volatile LAS u32*)(mb + hw * 4096 + (lane & 3) * 4) == tag)) break; }
        asm volatile("" ::: "memory");
      }
      STAMP(1);
      rdw(fr[0], 0, 0, 0); rdx(fr[0], 0, 0, 0); rdx(fr[0], 0, 0, 1); rdx(fr[0], 0, 0, 2); rdx(fr[0], 0, 0, 3); rdw(fr[0], 0, 0, 1);
      static_for<0, 30>([&](auto KS_) {
        constexpr int KS = decltype(KS_)::v, st = KS >> 2, ks = KS & 3;
        constexpr int nst = ks < 3 ? st : st + 1, nks = (ks + 1) & 3;
        Frag& f = fr[KS & 1]; Frag& g = fr[(KS + 1) & 1];
        SB();
        if constexpr (ks == 3) {
          if constexpr (st == 0) WAITV(0);
          if constexpr (st < 6) BAR();
          else { if (__builtin_expect(!all_ok(pv == tag), 0)) twait(FS + 1072); asm volatile("" ::: "memory"); }
          STAMP(2 + st); SB();
        }
        acc[0][0] = mfma3232(f.aw[0], f.bx[0], acc[0][0]); SB(); rdw(g, nst, nks, 0); SB();
        acc[0][1] = mfma3232(f.aw[0], f.bx[1], acc[0][1]); SB(); rdx(g, nst, nks, 0); SB();
        if constexpr (KS == 0 && S1PRE != 6) { g1_issue(1, 2); SB(); }
        if constexpr (KS == 1 && S1PRE != 6) { g1_issue(1, 5); SB(); }
        acc[0][2] = mfma3232(f.aw[0], f.bx[2], acc[0][2]); SB(); rdx(g, nst, nks, 1); SB();
        acc[0][3] = mfma3232(f.aw[0], f.bx[3], acc[0][3]); SB(); rdx(g, nst, nks, 2); SB();
        if constexpr (KS == 0 && S1PRE != 6) { g1_issue(1, 3); SB(); }
        acc[1][3] = mfma3232(f.aw[1], f.bx[3], acc[1][3]); SB(); rdx(g, nst, nks, 3); SB();
        acc[1][2] = mfma3232(f.aw[1], f.bx[2], acc[1][2]); SB(); rdw(g, nst, nks, 1); SB();
        acc[1][1] = mfma3232(f.aw[1], f.bx[1], acc[1][1]); SB();
        if constexpr (KS == 0 && S1PRE != 6) { g1_issue(1, 4); SB(); }
        if constexpr (KS == 26) { pv = *(volatile LAS u32*)(sm + FS + 1072 + (lane & 3) * 4); SB(); }
        if constexpr (KS == 28) { pv = *(volatile LAS u32*)(sm + FS + 1088 + (lane & 3) * 4); SB(); }
        if constexpr (KS == 29) {
          if (__builtin_expect(!all_ok(pv == tag), 0)) twait(FS + 1088);
          asm volatile("" ::: "memory");
          LAS char* cp = sm + FS + (lane & 31) * 4;
          qa[0] = *(LAS u32*)(cp + hw * 2048); qa[1] = *(LAS u32*)(cp + hw * 2048 + 128);
#pragma unroll
          for (int n = 0; n < 4; ++n) qb[n] = *(LAS u32*)(cp + n * 2048 + 256);
          SB();
        }
        acc[1][0] = mfma3232(f.aw[1], f.bx[0], acc[1][0]); SB();
      });
      rdw(fr[1], 7, 3, 0); rdx(fr[1], 7, 3, 0); rdw(fr[1], 7, 3, 1); rdx(fr[1], 7, 3, 1); rdx(fr[1], 7, 3, 2); rdx(fr[1], 7, 3, 3);
      __builtin_amdgcn_s_setprio(3);
      STAMP(9);
      u32x4 ca[2] = {u32x4{h ? 0u : qa[0], 0u, 0u, 0u}, u32x4{h ? 0u : qa[1], 0u, 0u, 0u}}, cb = {0u, 0u, 0u, 0u};
      const u32 hoff = WS_H + ((mi * 8 + (t & 1)) * 56 + fj * 4 + hw) * 1024 + h * 256 + p16 * 16;
      float ev[8];
      auto epi_a = [&](int q) {
#pragma unroll
        for (int k = 0; k < 8; ++k) ev[k] = relu(acc[q & 1][q >> 1][k]);
      };
      auto epi_b = [&](int q) {
#pragma unroll
        for (int k = 0; k < 8; ++k) ev[k] *= acc[q & 1][q >> 1][8 + k];
      };
      auto epi_c = [&](int q) {
        __builtin_amdgcn_raw_buffer_store_b128(u32x4{pk_bf16(ev[0], ev[1]), pk_bf16(ev[2], ev[3]), pk_bf16(ev[4], ev[5]), pk_bf16(ev[6], ev[7])}, rw,
                                               hoff + (q & 1) * 512, (q >> 1) * (2 * 56 * 1024), 16);
      };
#pragma unroll
      for (int q = 0; q < 8; ++q) {
        const int m = q & 1, n = q >> 1;
        SB();
        if (m == 0) cb[0] = qb[n];
        acc[m][n] = mfma3232(fr[0].aw[m], fr[0].bx[n], acc[m][n]); SB();
        if (q >= 2) { epi_c(q - 2); SB(); }
        acc[m][n] = mfma3232(ca[m], cb, acc[m][n]); SB();
        if (q >= 1) { epi_a(q - 1); SB(); }
        acc[m][n] = mfma3232(fr[1].aw[m], fr[1].bx[n], acc[m][n]); SB();
        if (q >= 1) { epi_b(q - 1); SB(); }
      }
      epi_c(6); SB(); epi_a(7); epi_b(7); epi_c(7);
      *(volatile LAS u32*)(sm + FS + 1056 + hw * 4) = tag;
      STAMP(10);
      wd_setup();
      WAITV(4);
      if (lane == 0) __builtin_amdgcn_raw_buffer_store_b32(tag, rw, WS_F2 + (mi * 2) * 256 + (fj * 4 + hw) * 4, 0, 16);
      WAITV(1);
      if (lane == 0) __builtin_amdgcn_raw_buffer_store_b32(tag, rw, WS_F2 + (mi * 2 + 1) * 256 + (fj * 4 + hw) * 4, 0, 16);
      STAMP(12);
      __builtin_amdgcn_s_setprio(HPRIO);
    } else {
      wd_setup();
      const int un = b >> 1, ua = un >= 7, uc = (b & 1) * 7 + (ua ? un - 7 : un);
      const u32x4 scs = {ua ? ka[8] : ka[6], ua ? ka[9] : ka[7], b < 28 ? 1835008u : 0u, 0x00020000u};
      const u32 vcs = (hw * 4 + t) * 3584 + uc * 256 + p16 * 16;
      u32x4 vv[16];
      f32x4 a0 = z4, a1 = z4;
      u32 sfa, sfb;
      u32 sel_lo = 0x00003f80u, sel_hi = 0x3f800000u;
      PIN(sel_lo); PIN(sel_hi);
      const unsigned long long t_start = __builtin_amdgcn_s_memrealtime();
#pragma unroll
      for (int half = 0; half < 2; ++half) {
        if (half == 1) { while (__builtin_amdgcn_s_memrealtime() - t_start < CS_DELAY) __builtin_amdgcn_s_sleep(4); }
#pragma unroll
        for (int k = 0; k < 16; ++k) ALD4S(vv[k], vcs, scs, (half * 16 + k) * 16 * 3584, "");
        if (half == 0) {
          const int q = 64 * g + 16 * hw + p16, ks = 8 * q + jp, km = (2048 - ks) & 2047, col = (t >> 1) * 256;
          const GAS unsigned short* wa = (t & 1) ? wi : wr;
          asm volatile("global_load_ushort %0, %1, off" : "=v"(sfa) : "v"(wa + ks * DF_ + col) : "memory");
          asm volatile("global_load_ushort %0, %1, off" : "=v"(sfb) : "v"(wa + km * DF_ + col) : "memory");
          tag = load_tag();
        }
        STAMP(23 + 3 * half);
        static_for<0, 4>([&](auto Q_) {
          constexpr int Q = decltype(Q_)::v;
          if (half == 0) WAITV(14 - 4 * Q); else WAITV(12 - 4 * Q);
#pragma unroll
          for (int k = 4 * Q; k < 4 * Q + 4; ++k) {
            PIN(vv[k]);
#pragma unroll
            for (int e = 0; e < 2; ++e) {
              const u32 p = vv[k][e], q = vv[k][2 + e];
              a0[2 * e] = __builtin_amdgcn_fdot2_f32_bf16(__builtin_bit_cast(b16x2, p), __builtin_bit_cast(b16x2, sel_lo), a0[2 * e], false);
              a0[2 * e + 1] = __builtin_amdgcn_fdot2_f32_bf16(__builtin_bit_cast(b16x2, p), __builtin_bit_cast(b16x2, sel_hi), a0[2 * e + 1], false);
              a1[2 * e] = __builtin_amdgcn_fdot2_f32_bf16(__builtin_bit_cast(b16x2, q), __builtin_bit_cast(b16x2, sel_lo), a1[2 * e], false);
              a1[2 * e + 1] = __builtin_amdgcn_fdot2_f32_bf16(__builtin_bit_cast(b16x2, q), __builtin_bit_cast(b16x2, sel_hi), a1[2 * e + 1], false);
            }
          }
        });
        PIN(a0); PIN(a1);
        STAMP(24 + 3 * half);
        if (half == 0) {
          WAITV(0);
          PIN(sfa); PIN(sfb);
          const float wh = 0.5f * ((t & 1) ? bf_lo(sfa) - bf_lo(sfb) : bf_lo(sfa) + bf_lo(sfb));
          const u32 wb = pk_bf16(wh, wh) & 0xffffu;
          auto s2 = __builtin_amdgcn_permlane16_swap(wb, wb, false, false);
          if ((t & 1) == 0) st_sc1_2(rw, WS_SF + jp * 4096 + (t >> 1) * 2048 + (64 * g + 4 * p16 + hw) * 8, u32x2{(u32)s2[0] | ((u32)s2[1] << 16), tag});
          STAMP(25);
        }
      }
      if (b < 28) {
#pragma unroll
        for (int k = 0; k < 4; ++k) { a0[k] = xrow_sum(a0[k]); a1[k] = xrow_sum(a1[k]); }
        const float s0 = t == 0 ? a0[0] : t == 1 ? a0[2] : t == 2 ? a1[0] : a1[2];
        const float s1 = t == 0 ? a0[1] : t == 1 ? a0[3] : t == 2 ? a1[1] : a1[3];
        st_sc1(rw, WS_CS + hw * (2 * F_ * 8) + (ua * 14 + uc) * 1024 + p16 * 64 + t * 16, u32x4{__builtin_bit_cast(u32, s0), tag, __builtin_bit_cast(u32, s1), tag});
      }
      STAMP(3);
    }
    {
      const int off = is_gemm ? FS + 1056 : FS + 1024;
#pragma nounroll
      for (int spin = 0; spin < SPIN_CAP; ++spin) { if (all_ok(*(volatile LAS u32*)(sm + off + (lane & 3) * 4) == tag)) break; __builtin_amdgcn_s_sleep(1); }
      asm volatile("" ::: "memory");
    }
    wd_issue(w, 0, 6); wd_issue(w, 1, 7); wd_issue(w, 2, 8);
    {
#pragma nounroll
      for (int spin = 0; spin < SPIN_CAP; ++spin) { if (all_ok(*(volatile LAS u32*)(sm + FS + 1024 + hw * 4) == tag)) break; __builtin_amdgcn_s_sleep(1); }
      asm volatile("" ::: "memory");
    }
    wd_issue(w, 6, 1);
    WAITV(4);
    wd_read(0, 6); wd_read(1, 7); wd_read(2, 8);
    asm volatile("s_waitcnt lgkmcnt(0)" ::: "memory");
    wd_issue(w, 3, 6); wd_issue(w, 4, 7); wd_issue(w, 5, 8);
    STAMP(13);
  } else if (is_gemm) helper(IC<1>{});
  else helper(IC<0>{});

  wait_h_and_load();
  __builtin_amdgcn_s_setprio(0);
#pragma unroll
  for (int i = 3; i < 7; ++i) {
    LAS char* p0 = sm + (w < 4 ? wslot(i == 6 ? 1 : i + 3) : wslot(i == 6 ? 0 : i));
#pragma unroll
    for (int u = 0; u < 4; ++u) {
      LAS char* p = p0 + (wbase ^ (u << 5));
      u32x2 lo = tr_read(p), hi = tr_read(p + 512);
      wf[i][u] = u32x4{lo[0], lo[1], hi[0], hi[1]};
    }
  }
  STAMP(16);
  const int oc = lane & 7, otok = (w & 1) * 8 + (lane >> 3);
  const int orow = rt * 64 + (((w >> 1) + ct) & 3) * 16 + otok;
  const u32 roff = WS_R + orow * 1024 + (ct * 8 + oc) * 16;
  const u32 soff_ = WS_ST2 + ((orow & 255) * 8 + (orow >> 8)) * 8;
  u32x4 r0; u32x2 sto; bool rok;
#pragma unroll
  for (int tt = 0; tt < 4; ++tt) {
    f32x4 acc[4] = {z4, z4, z4, z4};
#pragma unroll
    for (int ks = 0; ks < 7; ++ks) {
#pragma unroll
      for (int u = 0; u < 4; ++u) acc[u] = mfma32_bf16(wf[ks][u], hf[tt][ks], acc[u]);
      if (tt == 0) { SB(); hf[3][ks] = ld_h(3, ks); SB(); }
    }
    if (tt == 1) sto = ld_sc1_2(rw, soff_);
    if (tt == 3) { asm volatile("" : "+v"(sto) :: "memory"); rok = all_ok(sto[1] == tag); r0 = ld_sc1(rw, roff); }
#pragma unroll
    for (int u = 0; u < 4; ++u) *(LAS f32x4*)(sm + (w < 4 ? wslot(tt < 3 ? 6 + tt : 1) : wslot(tt < 3 ? 3 + tt : 0)) + hw * 4096 + u * 1024 + t * 256 + ((p16 ^ (4 * u + (t & 2))) << 4)) = acc[u];
    SB();
  }
  STAMP(17);
  BAR();
  f32x4 sa = z4, sb = z4;
  {
    const int tt = w >> 1;
    const int o0 = (oc >> 1) * 1024 + (oc & 1) * 512 + ((otok ^ (2 * oc)) << 4);
    const int bm = (tt < 3 ? wslot(6) + tt * 16384 : wslot(1)) + o0, bh = (tt < 3 ? wslot(3) + tt * 16384 : wslot(0)) + o0;
#pragma unroll
    for (int pw = 0; pw < 4; ++pw) {
      sa += *(LAS f32x4*)(sm + bm + pw * 4096) + *(LAS f32x4*)(sm + bh + pw * 4096);
      sb += *(LAS f32x4*)(sm + bm + 256 + pw * 4096) + *(LAS f32x4*)(sm + bh + 256 + pw * 4096);
    }
  }
  STAMP(22);
  if (__builtin_expect(!rok, 0)) {
#pragma nounroll
    for (int spin = 0; spin < SPIN_CAP; ++spin) {
      asm volatile("" ::: "memory");
      sto = ld_sc1_2(rw, soff_);
      if (all_ok(sto[1] == tag)) break;
    }
    asm volatile("" ::: "memory");
    r0 = ld_sc1(rw, roff);
  }
  {
    const float rs = __builtin_amdgcn_rcpf(bf_hi(sto[0])), rs2 = rs * rs;
    *(GAS u32x4*)(out + (size_t)orow * 1024 + ct * 128 + oc * 16) =
        u32x4{pk_bf16(__builtin_fmaf(sa[0], rs2, bf_lo(r0[0])), __builtin_fmaf(sa[1], rs2, bf_hi(r0[0]))),
              pk_bf16(__builtin_fmaf(sa[2], rs2, bf_lo(r0[1])), __builtin_fmaf(sa[3], rs2, bf_hi(r0[1]))),
              pk_bf16(__builtin_fmaf(sb[0], rs2, bf_lo(r0[2])), __builtin_fmaf(sb[1], rs2, bf_hi(r0[2]))),
              pk_bf16(__builtin_fmaf(sb[2], rs2, bf_lo(r0[3])), __builtin_fmaf(sb[3], rs2, bf_hi(r0[3])))};
  }
  if (b == 0 && tid == 0) __builtin_amdgcn_raw_buffer_store_b32(tag, rw, WS_EPOCH, 0, 16);
  STAMP(18);
  STAMP_DUMP();
}

#if defined(__HIP_DEVICE_COMPILE__)
#pragma clang attribute pop
#endif

extern "C" void kernel_launch(void* const* d_in, const int* in_sizes, int n_in, void* d_out, int out_size, void* d_ws, size_t ws_size,
                              hipStream_t stream) {
  (void)hipFuncSetAttribute((const void*)fused_kernel, hipFuncAttributeMaxDynamicSharedMemorySize, LDS_BYTES);
  fused_kernel<<<256, 512, LDS_BYTES, stream>>>((const char*)d_in[0], (const unsigned short*)d_in[1], (const unsigned short*)d_in[2], (const char*)d_in[4],
                                                (const char*)d_in[5], (const char*)d_in[6], (char*)d_ws, (char*)d_out);
}
```

```cpp
#include <hip/hip_runtime.h>
#include <stdint.h>

#define LAS __attribute__((address_space(3)))
#define GAS __attribute__((address_space(1)))
#define DI __device__ __forceinline__
typedef unsigned u32;
typedef unsigned u32x2 __attribute__((ext_vector_type(2)));
typedef unsigned u32x4 __attribute__((ext_vector_type(4)));
typedef float f32x2 __attribute__((ext_vector_type(2)));
typedef float f32x4 __attribute__((ext_vector_type(4)));
typedef float f32x16 __attribute__((ext_vector_type(16)));
typedef short s16x4 __attribute__((ext_vector_type(4)));
typedef _Float16 h16x2 __attribute__((ext_vector_type(2)));
typedef _Float16 h16x4 __attribute__((ext_vector_type(4)));
typedef _Float16 h16x8 __attribute__((ext_vector_type(8)));
typedef __bf16 b16x2 __attribute__((ext_vector_type(2)));
typedef __bf16 b16x8 __attribute__((ext_vector_type(8)));
typedef __amdgpu_buffer_rsrc_t rsrc_t;

constexpr int F_ = 1792, DF_ = 257;
constexpr u32 WS_EPOCH = 0;
constexpr u32 WS_F2 = 4096;
constexpr u32 WS_ST = 16384;
constexpr u32 WS_CS = WS_ST + 16384;
constexpr u32 WS_U = 262144;
constexpr u32 WS_V = WS_U + 4194304;
constexpr u32 WS_R = WS_V + 4194304;
constexpr u32 WS_ST2 = 229376;
constexpr u32 WS_SF = 196608;
constexpr u32 WS_H = WS_R + 4194304;
constexpr u32 WS_END = WS_H + 7340032;
constexpr int LDS_BYTES = 163840;
constexpr int G1_STAGE = 49152;
constexpr int FS = 3 * G1_STAGE;
#define SPIN_CAP (1 << 17)
constexpr int CODE_LINES = 292;
#define HPRIO 2
#define S1PRE 2
#define POLLSLEEP 2
#define CS_DELAY 200
#define WARM 0
#define UPOS 0
#define TV 2
#define V_DELAY 130
#define HW_EARLY 0
#define ROTM 1
constexpr float XS = 1.0f / 16;
constexpr float FSC = (2.0f / (2048.0f * 512.0f)) * 16.0f;
constexpr size_t WS_DBG = 64 << 20;
#define STAMP(k) do {} while (0)
#define STAMP_DECL
#define STAMP_DUMP() do {} while (0)

#if defined(__HIP_DEVICE_COMPILE__)
#pragma clang attribute push(__attribute__((target("no-packed-fp32-ops"))), apply_to = function)
#endif
DI u32 pk_f16(float a, float b) { h16x2 p = {(_Float16)a, (_Float16)b}; return __builtin_bit_cast(u32, p); }
DI u32 pk_bf16(float a, float b) { b16x2 p = {(__bf16)a, (__bf16)b}; return __builtin_bit_cast(u32, p); }
DI float bf_lo(u32 v) { return __builtin_bit_cast(float, v << 16); }
DI float bf_hi(u32 v) { return __builtin_bit_cast(float, v & 0xffff0000u); }
DI float rnd_bf(float a) { return bf_lo(pk_bf16(a, a)); }
DI float relu(float a) { int i = __builtin_bit_cast(int, a); return __builtin_bit_cast(float, i > 0 ? i : 0); }
DI float cosr(float rev) { return __builtin_amdgcn_cosf(rev); }
DI float sinr(float rev) { return __builtin_amdgcn_sinf(rev); }
DI f32x4 mfma16_f16(u32x2 a, u32x2 b, f32x4 c) { return __builtin_amdgcn_mfma_f32_16x16x16f16(__builtin_bit_cast(h16x4, a), __builtin_bit_cast(h16x4, b), c, 0, 0, 0); }
DI f32x4 mfma16_bf16(u32x2 a, u32x2 b, f32x4 c) { return __builtin_amdgcn_mfma_f32_16x16x16bf16_1k(__builtin_bit_cast(s16x4, a), __builtin_bit_cast(s16x4, b), c, 0, 0, 0); }
DI f32x4 mfma32_f16(u32x4 a, u32x4 b, f32x4 c) { return __builtin_amdgcn_mfma_f32_16x16x32_f16(__builtin_bit_cast(h16x8, a), __builtin_bit_cast(h16x8, b), c, 0, 0, 0); }
DI f32x4 mfma32_bf16(u32x4 a, u32x4 b, f32x4 c) { return __builtin_amdgcn_mfma_f32_16x16x32_bf16(__builtin_bit_cast(b16x8, a), __builtin_bit_cast(b16x8, b), c, 0, 0, 0); }
DI f32x16 mfma3232(u32x4 a, u32x4 b, f32x16 c) { return __builtin_amdgcn_mfma_f32_32x32x16_bf16(__builtin_bit_cast(b16x8, a), __builtin_bit_cast(b16x8, b), c, 0, 0, 0); }
DI u32x2 tr_read(LAS char* p) { return __builtin_bit_cast(u32x2, __builtin_amdgcn_ds_read_tr16_b64_v4i16((LAS s16x4*)p)); }
#define SB() __builtin_amdgcn_sched_barrier(0)
#define BAR() asm volatile("s_waitcnt lgkmcnt(0)\n\ts_barrier" ::: "memory")
#define WAITV(n) asm volatile("s_waitcnt vmcnt(%0)" :: "i"(n) : "memory")
#define PIN(x) asm volatile("" : "+v"(x))
DI u32x4 mk_srd(const void* p, u32 bytes) { uint64_t a = (uint64_t)p; return u32x4{(u32)a, (u32)(a >> 32), bytes, 0x00020000u}; }
DI void glds16(u32 ldsbase, int imm, u32 voff, u32x4 srd, u32 soff) {
  asm volatile("s_add_u32 m0, %0, %1\n\ts_nop 0\n\tbuffer_load_dwordx4 %2, %3, %4 offen lds" :: "s"(ldsbase), "i"(imm), "v"(voff), "s"(srd), "s"(soff) : "memory", "scc");
}
DI void glds4(u32 ldsaddr, u32 voff, u32x4 srd, u32 soff) {
  asm volatile("s_mov_b32 m0, %0\n\ts_nop 0\n\tbuffer_load_dword %1, %2, %3 offen lds" :: "s"(ldsaddr), "v"(voff), "s"(srd), "s"(soff) : "memory");
}
#define ALD4(dst, voff, srd, IMM, FL) asm volatile("buffer_load_dwordx4 %0, %1, %2, 0 offen offset:" #IMM FL : "=v"(dst) : "v"(voff), "s"(srd) : "memory")
#define ALD2(dst, voff, srd, IMM, FL) asm volatile("buffer_load_dwordx2 %0, %1, %2, 0 offen offset:" #IMM FL : "=v"(dst) : "v"(voff), "s"(srd) : "memory")
#define ALD1(dst, voff, srd, IMM, FL) asm volatile("buffer_load_dword %0, %1, %2, 0 offen offset:" #IMM FL : "=v"(dst) : "v"(voff), "s"(srd) : "memory")
DI u32x4 ld_sc1(rsrc_t r, u32 off) { return __builtin_amdgcn_raw_buffer_load_b128(r, off, 0, 16); }
DI u32x2 ld_sc1_2(rsrc_t r, u32 off) { return __builtin_amdgcn_raw_buffer_load_b64(r, off, 0, 16); }
DI void st_sc1(rsrc_t r, u32 off, u32x4 v) { __builtin_amdgcn_raw_buffer_store_b128(v, r, off, 0, 16); }
DI void st_sc1_2(rsrc_t r, u32 off, u32x2 v) { __builtin_amdgcn_raw_buffer_store_b64(v, r, off, 0, 16); }
DI bool all_ok(bool ok) { return __builtin_amdgcn_ballot_w64(!ok) == 0; }
DI bool tags_ok(u32x4 a, u32 tag) { return (a[1] == tag) & (a[3] == tag); }

template <int CTRL> DI float dpp_add(float v) {
  int o = __builtin_amdgcn_update_dpp(0, __builtin_bit_cast(int, v), CTRL, 0xf, 0xf, true);
  return v + __builtin_bit_cast(float, o);
}
DI float row_sum(float v) { v = dpp_add<0xB1>(v); v = dpp_add<0x4E>(v); v = dpp_add<0x141>(v); return dpp_add<0x140>(v); }
DI float xrow_sum(float v) {
  u32 u = __builtin_bit_cast(u32, v);
  auto a = __builtin_amdgcn_permlane16_swap(u, u, false, false);
  v = __builtin_bit_cast(float, (u32)a[0]) + __builtin_bit_cast(float, (u32)a[1]);
  u = __builtin_bit_cast(u32, v);
  auto b = __builtin_amdgcn_permlane32_swap(u, u, false, false);
  return __builtin_bit_cast(float, (u32)b[0]) + __builtin_bit_cast(float, (u32)b[1]);
}
template <bool NEG, bool BF> DI u32x4 cmul_pack(f32x4 re, f32x4 im, f32x4 c, f32x4 s) {
  f32x4 a = NEG ? re * c + im * s : re * c - im * s;
  f32x4 b = NEG ? im * c - re * s : im * c + re * s;
  u32x4 o;
#pragma unroll
  for (int r = 0; r < 4; ++r) o[r] = BF ? pk_bf16(a[r], b[r]) : pk_f16(a[r], b[r]);
  return o;
}
DI u32 rot16(u32 v) { return __builtin_amdgcn_alignbit(v, v, 16); }
template <bool NEG> DI void cmul_half(f32x4& a, f32x4& b, f32x4 re, f32x4 im, f32x4 c, f32x4 s) {
  a = NEG ? re * c + im * s : re * c - im * s;
  b = NEG ? im * c - re * s : im * c + re * s;
}
template <bool BF> DI u32x4 pack4(f32x4 a, f32x4 b) {
  u32x4 o;
#pragma unroll
  for (int r = 0; r < 4; ++r) o[r] = BF ? pk_bf16(a[r], b[r]) : pk_f16(a[r], b[r]);
  return o;
}
DI void glds_a(u32 ldsbase, int imm, u32 v0, u32 v1, u32x4 srd, u32 soff) {
  asm volatile("s_add_u32 m0, %0, %1\n\ts_nop 0\n\tbuffer_load_dwordx4 %2, %4, %5 offen lds\n\tbuffer_load_dwordx4 %3, %4, %5 offen offset:1024 lds"
               :: "s"(ldsbase), "i"(imm), "v"(v0), "v"(v1), "s"(srd), "s"(soff) : "memory", "scc");
}
DI void glds_b(u32 v2, u32 v3, u32x4 srd, u32 soff) {
  asm volatile("buffer_load_dwordx4 %0, %2, %3 offen offset:2048 lds\n\tbuffer_load_dwordx4 %1, %2, %3 offen offset:3072 lds"
               :: "v"(v2), "v"(v3), "s"(srd), "s"(soff) : "memory");
}
DI u32x4 cmulc_f16(f32x4 re, f32x4 im, u32x4 tw) {
  u32 z0 = pk_f16(re[0], im[0]), z1 = pk_f16(re[1], im[1]), z2 = pk_f16(re[2], im[2]), z3 = pk_f16(re[3], im[3]);
  u32 o0, o1, o2, o3;
  const u32 t0 = tw[0], t1 = tw[1], t2 = tw[2], t3 = tw[3];
#define PKM " op_sel_hi:[1,0]\n\t"
#define PKF " op_sel:[1,1,0] op_sel_hi:[0,1,1] neg_hi:[0,1,0]\n\t"
  asm("v_pk_mul_f16 %0, %4, %8" PKM "v_pk_mul_f16 %1, %5, %9" PKM "v_pk_mul_f16 %2, %6, %10" PKM "v_pk_mul_f16 %3, %7, %11" PKM
      "v_pk_fma_f16 %0, %4, %8, %0" PKF "v_pk_fma_f16 %1, %5, %9, %1" PKF "v_pk_fma_f16 %2, %6, %10, %2" PKF "v_pk_fma_f16 %3, %7, %11, %3" PKF "s_nop 1"
      : "=&v"(o0), "=&v"(o1), "=&v"(o2), "=&v"(o3) : "v"(z0), "v"(z1), "v"(z2), "v"(z3), "v"(t0), "v"(t1), "v"(t2), "v"(t3));
  return u32x4{o0, o1, o2, o3};
}
template <int N> struct IC { static constexpr int v = N; };
template <int A, int B, class Fn> DI void static_for(Fn&& f) { if constexpr (A < B) { f(IC<A>{}); static_for<A + 1, B>(f); } }

struct Frag { u32x4 bx[4], aw[2]; };
#define ALD4S(dst, voff, srd, SOFF, FL) asm volatile("buffer_load_dwordx4 %0, %1, %2, %3 offen" FL : "=v"(dst) : "v"(voff), "s"(srd), "s"(SOFF) : "memory")
#define ALD2S(dst, voff, srd, SOFF, FL) asm volatile("buffer_load_dwordx2 %0, %1, %2, %3 offen" FL : "=v"(dst) : "v"(voff), "s"(srd), "s"(SOFF) : "memory")
#define RAWBAR() do { asm volatile("s_barrier" ::: "memory"); } while (0)

typedef unsigned u32x16 __attribute__((ext_vector_type(16)));
__global__ __launch_bounds__(512, 2) void fused_kernel(const char* a_x, const unsigned short* a_wr, const unsigned short* a_wi, const char* a_wg,
                                                       const char* a_wu, const char* a_wd, char* a_ws, char* a_out) {
  extern __shared__ __attribute__((aligned(16))) char smem[];
  LAS char* sm = (LAS char*)smem;
  STAMP_DECL
  STAMP(0);
  u32x16 ka;
  asm volatile("s_load_dwordx16 %0, %1, 0x0" : "=s"(ka) : "s"(__builtin_amdgcn_kernarg_segment_ptr()) : "memory");
  const int b = blockIdx.x, tid = threadIdx.x, lane = tid & 63, w = __builtin_amdgcn_readfirstlane(tid >> 6);
  const int hw = w & 3, p16 = lane & 15, t = lane >> 4;
  const u32 lds0 = (u32)(uintptr_t)sm;
  {
    uint64_t pc; asm volatile("s_getpc_b64 %0" : "=s"(pc));
    asm volatile("s_cmp_gt_u32 %0, 7\n\ts_cbranch_scc1 1f\n\ts_mov_b32 m0, %1\n\ts_nop 0\n\tbuffer_load_dword %2, %3, 0 offen lds\n1:"
                 :: "s"(b), "s"(lds0), "v"(tid * 128), "s"(u32x4{(u32)pc, (u32)(pc >> 32), CODE_LINES * 128, 0x00020000u}) : "memory", "scc");
  }
  const bool is_gemm = b >= 32;
  const int xc = b & 7, r_ = (b - 32) >> 3;
  const int mi = (xc >> 1) * 4 + (r_ & 3), fj = (xc & 1) * 7 + (r_ >> 2);
  const int rot = ((r_ & 3) + (r_ >> 2)) & ROTM;
  const u32 lw = lds0 + hw * 4096;
  u32 vx[4], vg[4], vu[4];
#pragma unroll
  for (int j = 0; j < 4; ++j) {
    vx[j] = (mi * 128 + hw * 32 + j * 7 + (lane >> 3)) * 1024 + (((lane & 7) ^ ((4 * j + t) & 7)) << 4);
    vg[j] = (hw * 16 + j * 4 + t) * 3584 - j * 1024 + fj * 256 + ((p16 ^ (2 * t)) << 4);
    vu[j] = vg[j] ^ 128;
    PIN(vx[j]); PIN(vg[j]); PIN(vu[j]);
  }
  u32 vrow = (b + 256 * hw) * 1024 + lane * 16;
  asm volatile("s_cmp_lt_u32 %0, 4\n\ts_cbranch_scc1 1f\n\ts_setprio 2\n1:" :: "s"(w) : "scc");
  if (w < 4) vrow = 0x7ff00000u;
  const u32 lrow = w < 4 ? lw : lds0 + FS + hw * 1024;
  PIN(vrow);
  SB();
  asm volatile("s_waitcnt lgkmcnt(0)" : "+s"(ka) :: "memory");
#define KARG(i) (((uint64_t)ka[2 * (i) + 1] << 32) | ka[2 * (i)])
  const u32x4 sx = {ka[0], ka[1], 2097152u, 0x00020000u}, sg = {ka[6], ka[7], 1835008u, 0x00020000u}, su = {ka[8], ka[9], 1835008u, 0x00020000u};
  auto g1_issue = [&](int st, int k) {
    const int lb = (st % 3) * G1_STAGE + (k >> 1) * 16384, sk = st ^ rot;
    if (k == 0) glds_a(lw, lb, vx[0], vx[1], sx, sk * 128);
    if (k == 1) glds_b(vx[2], vx[3], sx, sk * 128);
    if (k == 2) glds_a(lw, lb, vg[0], vg[1], sg, sk * 64 * 3584);
    if (k == 3) glds_b(vg[2], vg[3], sg, sk * 64 * 3584);
    if (k == 4) glds_a(lw, lb, vu[0], vu[1], su, sk * 64 * 3584);
    if (k == 5) glds_b(vu[2], vu[3], su, sk * 64 * 3584);
  };
  glds16(lrow, 0, vrow, sx, 0);
  glds16(lrow + (w < 4 ? 0 : 4096), 0, vrow, sx, 1048576);
  if (w < 4 && is_gemm) {
#pragma unroll
    for (int k = 0; k < 6; ++k) g1_issue(0, k);
  }
  SB();
  STAMP(30);
  const f32x4 z4 = {0.f, 0.f, 0.f, 0.f};
  const GAS unsigned short* wr = (const GAS unsigned short*)KARG(1); const GAS unsigned short* wi = (const GAS unsigned short*)KARG(2);
  GAS char* ws = (GAS char*)KARG(6); GAS char* out = (GAS char*)KARG(7);
  auto load_tag = [&]() {
    u32 epoch;
    asm volatile("s_load_dword %0, %1, 0x0\n\ts_waitcnt lgkmcnt(0)" : "=s"(epoch) : "s"(ws) : "memory");
    return epoch * 0x9E3779B1u + 0x7F4A7C15u;
  };
  const rsrc_t rw = __builtin_amdgcn_make_buffer_rsrc((void*)ws, 0, WS_END, 0x00020000);
  const int jp = b & 7, g = b >> 3;
  const int rt = (b & 7) * 4 + ((b >> 3) & 3), ct = b >> 5;
  auto wslot = [&](int s) { return (s < 3 ? 2 * G1_STAGE : s < 6 ? -3 * 16384 : G1_STAGE - 6 * 16384) + s * 16384; };
  u32x4 wf[7][4];
  const int wbase = hw * 4096 + t * 1024 + (p16 >> 2) * 128 + (p16 & 3) * 8 + ((((p16 >> 3) | ((t & 1) << 1))) << 5);
  auto wd_read = [&](int ks, int s) {
#pragma unroll
    for (int u = 0; u < 4; ++u) {
      LAS char* p = sm + wslot(s) + (wbase ^ (u << 5));
      u32x2 lo = tr_read(p), hi = tr_read(p + 512);
      wf[ks][u] = u32x4{lo[0], lo[1], hi[0], hi[1]};
    }
  };
  u32 tag;
  auto ksof = [&](int i) { int k = i + 2 * (rt & 3); return k >= 7 ? k - 7 : k; };
  const u32 hs = WS_H + ((rt * 4) * 56 + w * 7) * 1024 + lane * 16;
  auto ld_h = [&](int j, int i) { return __builtin_amdgcn_raw_buffer_load_b128(rw, hs, (((j + ct) & 3) * 56 + ksof(i)) * 1024, 16); };
  u32x4 hf[4][7];
  const u32x4 sd_ = {ka[10], ka[11], 1835008u, 0x00020000u};
  u32 vwd[4];
  auto wd_setup = [&]() {
#pragma unroll
    for (int j = 0; j < 4; ++j) { vwd[j] = (j * 7 + (lane >> 3)) * 1024 + ct * 128 + (((lane & 7) ^ (((lane >> 4) & 1) << 1) ^ ((j & 1) << 2)) << 4); PIN(vwd[j]); }
  };
  auto wd_issue = [&](int kw, int i, int s) {
    const u32 so = kw * (224 * 1024) + ksof(i) * 32768;
    SB(); glds_a(lw, wslot(s), vwd[0], vwd[1], sd_, so); glds_b(vwd[2], vwd[3], sd_, so); SB();
  };
  auto wait_h_and_load = [&]() {
    const u32 foff = WS_F2 + rt * 256 + (w * 7 + (lane < 7 ? lane : 0)) * 4;
#pragma nounroll
    for (int spin = 0; spin < SPIN_CAP; ++spin) {
      asm volatile("" ::: "memory");
      u32 fl = __builtin_amdgcn_raw_buffer_load_b32(rw, foff, 0, 16);
      if (all_ok(fl == tag)) break;
      __builtin_amdgcn_s_sleep(POLLSLEEP);
    }
    __builtin_amdgcn_fence(__ATOMIC_ACQUIRE, "wavefront");
    asm volatile("" ::: "memory");
    STAMP(15);
#pragma unroll
    for (int tt = 0; tt < 3; ++tt)
#pragma unroll
      for (int i = 0; i < 7; ++i) hf[tt][i] = ld_h(tt, i);
    SB();
  };
  const float pf = (float)p16, tf = (float)t;

  auto helper = [&](auto G_) {
    constexpr bool G = decltype(G_)::v;
    const int ht = tid & 255;
    const u32x4 sw = {ka[12], ka[13], WS_END, 0x00020000u};
    const u32x4 sfr_ = {ka[2], ka[3], 2048 * DF_ * 2, 0x00020000u}, sfi_ = {ka[4], ka[5], 2048 * DF_ * 2, 0x00020000u};
    SB();
#define DMA(st, k) do { if (G) { SB(); g1_issue(st, k); SB(); } } while (0)
#define GWAIT(n) do { if (G) WAITV(n); } while (0)
#define WAITG(n) do { if (G) WAITV(n); else WAITV(0); } while (0)
#define GBAR() do { if (G) RAWBAR(); } while (0)
    auto hsync = [&](int off, int stride = 0) {
      asm volatile("s_waitcnt lgkmcnt(0)" ::: "memory");
      *(volatile LAS u32*)(sm + off + (lane & 3) * stride + hw * 4) = tag;
#pragma nounroll
      for (int spin = 0; spin < SPIN_CAP; ++spin) { if (all_ok(*(volatile LAS u32*)(sm + off + hw * stride + (lane & 3) * 4) == tag)) break; }
      asm volatile("" ::: "memory");
    };
#define XBAR(n) do { if (G) BAR(); else hsync(G1_STAGE + (n) * 16); } while (0)
    u32x4 b2re[2], b2im[2], tw[2], ac2re, ac2im, bc3re, bc3im, qa8, qa8i, bc1re, bc1im, ac4re, ac4im, t1, bi1re[2], bi1im[2];
    u32x2 b1re, b1im;
    f32x4 t2c, t2s, t4c[2], t4s[2];
    {
      const float d5 = pf * (1.0f / 512), a5 = 4.0f * tf * d5;
#pragma unroll
      for (int r = 0; r < 4; ++r) {
        const float th = a5 + r * d5, c = cosr(th * 32.0f), s = sinr(th * 32.0f);
        ac2re[r] = pk_f16(c, s); ac2im[r] = rot16(ac2re[r]) ^ 0x8000u;
        bc3im[r] = pk_bf16(s, c); bc3re[r] = rot16(bc3im[r]) ^ 0x80000000u;
#pragma unroll
        for (int hh = 0; hh < 2; ++hh) {
          const float tq = th + hh * (16.0f * d5);
          tw[hh][r] = pk_f16(cosr(tq), sinr(tq));
          b2re[hh][r] = pk_f16(cosr(tq * 16.0f), sinr(tq * 16.0f)); b2im[hh][r] = rot16(b2re[hh][r]) ^ 0x8000u;
        }
      }
      b1re = u32x2{__builtin_amdgcn_perm(ac2re[1], ac2re[0], 0x05040100u), __builtin_amdgcn_perm(ac2re[3], ac2re[2], 0x05040100u)};
      b1im = u32x2{__builtin_amdgcn_perm(ac2im[1], ac2im[0], 0x05040100u), __builtin_amdgcn_perm(ac2im[3], ac2im[2], 0x05040100u)};
      const int po = p16 & 1, j8 = p16 >> 1;
      const float j8f = (float)j8 * 0.125f;
      const float ra = 2.0f * tf * j8f, rb = ra + j8f;
      const float ca = cosr(ra), sa = sinr(ra), cb = cosr(rb), sb = sinr(rb);
      const u32 fa0 = pk_f16(ca, sa), fb0 = pk_f16(cb, sb);
      const u32 fa = po ? (rot16(fa0) ^ 0x8000u) : fa0, fb = po ? (rot16(fb0) ^ 0x8000u) : fb0;
      const u32 sel = (j8 & 3) == 0 ? 0x03020100u : (j8 < 4 ? 0x0c0c0100u : 0x01000c0cu);
      qa8 = u32x4{fa, fb, __builtin_amdgcn_perm(0u, fa, sel), __builtin_amdgcn_perm(0u, fb, sel)};
      const u32 ia = pk_bf16(sa, ca), ib = pk_bf16(sb, cb), pa = rot16(ia) ^ 0x80000000u, pb = rot16(ib) ^ 0x80000000u;
      const bool on = (po == 0) ? (t < 2) : (t >= 2);
      const u32 hc = pk_bf16(0.5f * ca, 0.f);
      qa8i = u32x4{po ? ia : pa, po ? ib : pb, (t & 1) == 0 ? (po ? hc << 16 : hc) : (on ? pa : 0u), on ? pb : 0u};
      PIN(ac2re); PIN(ac2im); PIN(bc3re); PIN(bc3im); PIN(tw[0]); PIN(tw[1]); PIN(b2re[0]); PIN(b2re[1]); PIN(b2im[0]); PIN(b2im[1]); PIN(b1re); PIN(b1im); PIN(qa8); PIN(qa8i);
    }
    SB();
    tag = load_tag();
    STAMP(21);
    LAS char* const img = sm + FS + hw * 1024;
    u32 stat[2];
    u32x4 xr[2];
    WAITV(0);
    STAMP(1);
    {
      const u32 vo = (8 * b + 2 * hw) * DF_ * 2 + (lane >> 5) * (DF_ * 2 + 2) + (lane & 31) * 16;
      glds16(lds0 + hw * 1024, FS + 8192, vo, sfr_, 0); glds16(lds0 + hw * 1024, FS + 12288, vo, sfi_, 0);
    }
    auto tpat = [&](int q, int u) { const u32 r = tag >> (1 + 7 * q + 3 * u); return (tag & 1u) | ((r & 3u) << 1) | ((r & 28u) << 14); };
    {
      float v[2][8], mean[2], var[2];
#pragma unroll
      for (int a = 0; a < 2; ++a) {
        const u32x4 xv = *(LAS u32x4*)(img + a * 4096 + (4 * p16 + t) * 16);
        xr[a] = xv;
#pragma unroll
        for (int k = 0; k < 4; ++k) { v[a][2 * k] = bf_lo(xv[k]); v[a][2 * k + 1] = bf_hi(xv[k]); }
      }
#pragma unroll
      for (int a = 0; a < 2; ++a) mean[a] = xrow_sum(row_sum(((v[a][0] + v[a][1]) + (v[a][2] + v[a][3])) + ((v[a][4] + v[a][5]) + (v[a][6] + v[a][7])))) * (1.0f / 512);
#pragma unroll
      for (int a = 0; a < 2; ++a) {
        float q0 = 0.f, q1 = 0.f;
#pragma unroll
        for (int k = 0; k < 4; ++k) { float d = v[a][k] - mean[a], e = v[a][k + 4] - mean[a]; q0 += d * d; q1 += e * e; }
        var[a] = xrow_sum(row_sum(q0 + q1)) * (1.0f / 512);
      }
#pragma unroll
      for (int a = 0; a < 2; ++a) {
        stat[a] = pk_bf16(mean[a], __builtin_amdgcn_sqrtf(rnd_bf(rnd_bf(var[a]) + 1e-5f)));
        const float sc = __builtin_amdgcn_rcpf(bf_hi(stat[a])) * XS, of = -bf_lo(stat[a]) * sc;
        u32x4 xh;
#pragma unroll
        for (int k = 0; k < 4; ++k) xh[k] = pk_f16(__builtin_fmaf(v[a][2 * k], sc, of), __builtin_fmaf(v[a][2 * k + 1], sc, of));
        *(LAS u32x4*)(img + a * 4096 + (((4 * p16 + t) ^ ((p16 >> 1) & 2)) << 4)) = xh;
      }
      asm volatile("" ::: "memory");
      STAMP(23);
#pragma unroll
      for (int k = 0; k < 6; ++k) DMA(2, k);
      u32x2 tra[2][2];
#pragma unroll
      for (int a = 0; a < 2; ++a)
#pragma unroll
        for (int hh = 0; hh < 2; ++hh) tra[a][hh] = tr_read(img + a * 4096 + (4 * t + (p16 >> 2)) * 64 + ((hh ^ (t & 1)) << 5) + (p16 & 3) * 8);
      f32x4 d1re[2][2], d1im[2][2], d2re[2], d2im[2];
#pragma unroll
      for (int a = 0; a < 2; ++a)
#pragma unroll
        for (int hh = 0; hh < 2; ++hh) { d1re[a][hh] = mfma16_f16(tra[a][hh], b1re, z4); d1im[a][hh] = mfma16_f16(tra[a][hh], b1im, z4); }
#pragma unroll
      for (int a = 0; a < 2; ++a)
#pragma unroll
        for (int hh = 0; hh < 2; ++hh) {
          u32x4 ta = cmulc_f16(d1re[a][hh], d1im[a][hh], tw[hh]);
          d2re[a] = mfma32_f16(ta, b2re[hh], hh ? d2re[a] : z4); d2im[a] = mfma32_f16(ta, b2im[hh], hh ? d2im[a] : z4);
        }
      float ny[2];
#pragma unroll
      for (int a = 0; a < 2; ++a) { f32x4 q = d1re[a][0] + d1re[a][1]; ny[a] = xrow_sum((q[0] - q[1]) + (q[2] - q[3])); }
      int e = 4 * p16 + t; e ^= (e >> 3) & 3;
#pragma unroll
      for (int a = 0; a < 2; ++a) {
        if (lane == 0) d2im[a][0] = ny[a];
        *(LAS u32x4*)(img + a * 4096 + e * 16) = pack4<false>(d2re[a], d2im[a]);
      }
    }
    STAMP(24);
    WAITG(12);
    XBAR(0);
    STAMP(2);
#pragma unroll
    for (int k = 0; k < 6; ++k) DMA(3, k);
    u32x4 uo[2][2];
    {
      u32x2 e0[2], e1[2];
#pragma unroll
      for (int k = 0; k < 2; ++k) {
        int e = 16 * hw + 8 * k + (p16 >> 1); e ^= (e >> 3) & 3;
        e0[k] = *(LAS u32x2*)(sm + FS + (2 * t) * 1024 + e * 16 + (p16 & 1) * 8);
        e1[k] = *(LAS u32x2*)(sm + FS + (2 * t + 1) * 1024 + e * 16 + (p16 & 1) * 8);
      }
      u32 wv[2][2][2];
      {
        LAS char* fp = sm + FS + 8192 + t * 1024 + hw * 128 + p16 * 4;
#pragma unroll
        for (int hf = 0; hf < 2; ++hf)
#pragma unroll
          for (int k = 0; k < 2; ++k)
#pragma unroll
            for (int u = 0; u < 2; ++u) {
              LAS char* q = fp + hf * 510 + k * 64 + u * 2;
              wv[hf][k][u] = (u32)*(LAS unsigned short*)q | ((u32)*(LAS unsigned short*)(q + 4096) << 16);
            }
      }
      f32x4 d8[4];
#pragma unroll
      for (int u = 0; u < 4; ++u) d8[u] = mfma16_f16(u32x2{qa8[0], qa8[1]}, u32x2{e0[u >> 1][u & 1], e1[u >> 1][u & 1]}, z4);
      f32x4 d8s = mfma16_f16(u32x2{qa8[2], qa8[3]}, u32x2{e0[0][0], e1[0][0]}, z4);
      if ((hw | p16) == 0) d8[0] = d8s;
      const u32 p0 = tpat(p16 & 3, 0), p1 = tpat(p16 & 3, 1);
#pragma unroll
      for (int hf = 0; hf < 2; ++hf)
#pragma unroll
        for (int k = 0; k < 2; ++k) {
          uo[hf][k] = u32x4{(pk_f16(d8[2 * k][2 * hf], d8[2 * k][2 * hf + 1]) & 0xfff8fff8u) | p0, wv[hf][k][0],
                            (pk_f16(d8[2 * k + 1][2 * hf], d8[2 * k + 1][2 * hf + 1]) & 0xfff8fff8u) | p1, wv[hf][k][1]};
          PIN(uo[hf][k]);
        }
    }
    auto ustore = [&]() {
      const u32 ub = WS_U + b * 16384 + (2 * t) * 2048 + hw * 512 + p16 * 16;
#pragma unroll
      for (int hf = 0; hf < 2; ++hf)
#pragma unroll
        for (int k = 0; k < 2; ++k) st_sc1(rw, ub + hf * 2048 + k * 256, uo[hf][k]);
      if (lane < 2) st_sc1_2(rw, WS_ST + b * 64 + hw * 8 + lane * 32, u32x2{lane ? stat[1] : stat[0], tag});
      STAMP(19);
    };
    ustore();
    GWAIT(12 + 5);
    GBAR();
    STAMP(3);
    {
      const float jf = (float)jp;
      const float d1 = (8.0f * pf + jf) * (1.0f / 2048), a1 = 4.0f * tf * d1, d2 = pf * (1.0f / 256), a2 = (32.0f * tf + jf) * (pf * (1.0f / 2048));
      DMA(4, 0); DMA(4, 1);
#pragma unroll
      for (int r = 0; r < 4; ++r) {
        DMA(4, 2 + r);
        const float n1 = a1 + r * d1, n2 = a2 + r * d2;
        bc1re[r] = pk_f16(cosr(n1 * 16.0f), sinr(n1 * 16.0f)); bc1im[r] = rot16(bc1re[r]) ^ 0x8000u;
        t1[r] = pk_f16(cosr(n1), sinr(n1));
        ac4im[r] = pk_bf16(sinr(n2 * 16.0f), cosr(n2 * 16.0f)); ac4re[r] = rot16(ac4im[r]) ^ 0x80000000u;
        t2c[r] = cosr(n2); t2s[r] = sinr(n2);
      }
      PIN(bc1re); PIN(bc1im); PIN(ac4re); PIN(ac4im); PIN(t1); PIN(t2c); PIN(t2s);
    }
    u32x4 ug[4];
    const int uq = ht & 3;
    const int ui = (((ht >> 2) & 3) << 4) + (ht >> 4);
    const u32 uoff = WS_U + ui * 16384 + jp * 2048 + g * 64 + uq * 16;
    auto caddr = [&](int i) { return (LAS char*)(sm + FS + uq * 2048 + ((((i & 192) + 4 * (i & 15) + ((i >> 4) & 3)) * 4) ^ (uq << 5))); };
    if (!G) {
#pragma nounroll
      for (int spin = 0; spin < SPIN_CAP; ++spin) {
        asm volatile("" ::: "memory");
        u32x2 q = ld_sc1_2(rw, WS_ST + (b + 224) * 64 + hw * 8);
        if (all_ok(q[1] == tag)) break;
        __builtin_amdgcn_s_sleep(8);
      }
      __builtin_amdgcn_s_sleep(20);
    }
    STAMP(26);
#pragma unroll
    for (int k = 0; k < 4; ++k) ALD4S(ug[k], uoff, sw, k * 64 * 16384, " sc1");
    WAITG(5 + 12 + 4);
    XBAR(2);
    STAMP(4);
#pragma unroll
    for (int k = 0; k < 6; ++k) DMA(5, k);
    STAMP(27);
    GWAIT(12);
    PIN(ug[0]); PIN(ug[1]); PIN(ug[2]); PIN(ug[3]);
    const u32 c0 = tpat(uq, 0), c1 = tpat(uq, 1);
#define CHK1 all_ok(((((ug[0][0] ^ c0) | (ug[1][0] ^ c0) | (ug[2][0] ^ c0) | (ug[3][0] ^ c0) | (ug[0][2] ^ c1) | (ug[1][2] ^ c1) | (ug[2][2] ^ c1) | (ug[3][2] ^ c1)) & 0x00070007u) == 0))
    if (__builtin_expect(!CHK1, 0)) {
#pragma nounroll
      for (int spin = 0; spin < SPIN_CAP; ++spin) {
        asm volatile("" ::: "memory");
#pragma unroll
        for (int k = 0; k < 4; ++k) ug[k] = ld_sc1(rw, uoff + k * 64 * 16384);
        if (CHK1) break;
      }
    }
    {
      LAS char* cp = caddr(ui);
      const u32 x0 = c0 ^ 0x00040004u, x1 = c1 ^ 0x00040004u;
#pragma unroll
      for (int k = 0; k < 4; ++k) {
        *(LAS u32*)(cp + k * 256) = ug[k][0] ^ x0; *(LAS u32*)(cp + k * 256 + 1024) = ug[k][2] ^ x1;
        *(LAS u32*)(cp + k * 256 + 8192) = ug[k][1]; *(LAS u32*)(cp + k * 256 + 9216) = ug[k][3];
      }
    }
    u32x4 fsp[2];
    const bool special = !G && b < 8 && hw == 0;
    if (special) {
#pragma nounroll
      for (int spin = 0; spin < SPIN_CAP; ++spin) {
        asm volatile("" ::: "memory");
        bool ok = true;
#pragma unroll
        for (int c = 0; c < 2; ++c) {
          const u32x4 fa = ld_sc1(rw, WS_SF + jp * 4096 + c * 2048 + lane * 32), fb = ld_sc1(rw, WS_SF + jp * 4096 + c * 2048 + lane * 32 + 16);
          ok &= tags_ok(fa, tag) & tags_ok(fb, tag);
          fsp[c] = u32x4{fa[0], fa[2], fb[0], fb[2]};
        }
        if (all_ok(ok)) break;
      }
    }
    STAMP(28);
    XBAR(3);
    STAMP(5);
    LAS char* const colp = sm + FS + hw * 2048 + ((lane * 16) ^ (hw << 5));
    {
      u32x4 uin[2], fin[2];
#pragma unroll
      for (int a = 0; a < 2; ++a) { uin[a] = *(LAS u32x4*)(colp + a * 1024); fin[a] = *(LAS u32x4*)(colp + 8192 + a * 1024); }
      const bool two = special && (jp & 3) == 0;
      f32x4 dre[2], dim[2], keep;
      auto col_xform = [&](auto NA_) {
        constexpr int NA = decltype(NA_)::v;
        u32x4 ta[2];
        DMA(6, 0);
#pragma unroll
        for (int a = 0; a < NA; ++a) { dre[a] = mfma32_f16(uin[a], bc1re, z4); dim[a] = mfma32_f16(uin[a], bc1im, z4); }
        DMA(6, 1);
#pragma unroll
        for (int a = 0; a < NA; ++a) { ta[a] = cmulc_f16(dre[a], dim[a], t1); dre[a] = mfma32_f16(ac2re, ta[a], z4); dim[a] = mfma32_f16(ac2im, ta[a], z4); }
        DMA(6, 2);
#pragma unroll
        for (int a = 0; a < NA; ++a) {
          f32x4 fr_, fi_;
#pragma unroll
          for (int r = 0; r < 4; ++r) { fr_[r] = bf_lo(fin[a][r]); fi_[r] = bf_hi(fin[a][r]); }
          ta[a] = cmul_pack<false, true>(dre[a], dim[a], fr_, fi_); dre[a] = mfma32_bf16(ta[a], bc3re, z4); dim[a] = mfma32_bf16(ta[a], bc3im, z4);
          DMA(6, 3 + a);
        }
#pragma unroll
        for (int a = 0; a < NA; ++a) {
          ta[a] = cmul_pack<false, true>(dre[a], dim[a], t2c, t2s); dre[a] = mfma32_bf16(ac4re, ta[a], z4); dim[a] = mfma32_bf16(ac4im, ta[a], z4);
          if (a == 0) DMA(6, 5);
        }
      };
      if (!G) {
        if (special) {
          fin[0] = jp > 4 ? fsp[1] : fsp[0];
          if (two) {
            const u32x4 us = uin[0];
            uin[0] = us >> 16; fin[0] = fsp[1];
            col_xform(IC<1>{});
            keep = dre[0];
            uin[0] = us & 0xffffu; fin[0] = fsp[0];
          }
        }
      }
      col_xform(IC<2>{});
      if (!G) { if (two) dim[0] = keep; }
#pragma unroll
      for (int a = 0; a < 2; ++a) *(LAS u32x4*)(colp + a * 1024) = pack4<true>(dre[a], dim[a]);
      {
        const u32 vo = b * 7168 + hw * 2048 + lane * 16;
        glds_a(lds0 + hw * 2048, FS + 8192, vo, (hw < 3 ? vo : 0x7fffffffu), sd_, 0);
      }
#define NW 2
    }
    STAMP(29);
    GWAIT(12 + NW);
    XBAR(4);
    STAMP(6);
    {
      const int ps = ht >> 2;
      u32 vo[4][2];
#pragma unroll
      for (int k = 0; k < 4; ++k) { LAS char* cp = caddr((ps >> 5) + 2 * k + 8 * (ps & 31)); vo[k][0] = *(LAS u32*)cp; vo[k][1] = *(LAS u32*)(cp + 1024); }
#pragma unroll
      for (int k = 0; k < 6; ++k) DMA(7, k);
#pragma unroll
      for (int k = 0; k < 4; ++k) st_sc1(rw, WS_V + b * 16384 + k * 4096 + ht * 16, u32x4{vo[k][0], tag, vo[k][1], tag});
    }
    const unsigned long long t_v = __builtin_amdgcn_s_memrealtime();
    STAMP(20);
    GWAIT(NW + 12 + 4);
    XBAR(5);
    STAMP(7);
    wd_setup();
    u32x2 csv[4], stv;
    const u32 csoff = WS_CS + ((t & 1) * F_ + fj * 128 + hw * 32 + (lane >> 5) * 16 + ((p16 >> 2) & 1) * 8 + (p16 >> 3) * 4 + (p16 & 3)) * 8;
    const u32 stoff = WS_ST + (((mi & 1) * 128 + hw * 32 + (lane & 31)) * 8 + (mi >> 1)) * 8;
    if (G) {
      ALD2S(csv[0], csoff, sw, 0, " sc1"); ALD2S(csv[1], csoff, sw, 2 * F_ * 8, " sc1"); ALD2S(csv[2], csoff, sw, 4 * F_ * 8, " sc1"); ALD2S(csv[3], csoff, sw, 6 * F_ * 8, " sc1");
      ALD2(stv, stoff, sw, 0, " sc1");
    }
    if (G) { WAITV(4 + 5); *(volatile LAS u32*)(sm + FS + 1072 + hw * 4) = tag; }
#pragma unroll
    for (int hh = 0; hh < 2; ++hh) {
      wd_issue(w, hh, hh);
      const float n2 = (float)(8 * (p16 >> 2) + (p16 & 3)) + 4.0f * hh;
#pragma unroll
      for (int m = 0; m < 4; ++m) {
        float rev = n2 * ((tf + 4.0f * m) * (1.0f / 32));
        bi1im[hh][m] = pk_bf16(sinr(rev), cosr(rev)); bi1re[hh][m] = rot16(bi1im[hh][m]) ^ 0x80000000u;
        float rev2 = n2 * ((4.0f * tf + m) * (1.0f / 512));
        t4c[hh][m] = cosr(rev2); t4s[hh][m] = sinr(rev2);
      }
      PIN(bi1re[hh]); PIN(bi1im[hh]); PIN(t4c[hh]); PIN(t4s[hh]);
    }
    wd_issue(w, 2, 2);
    if (G) {
      WAITV(12);
      PIN(csv[0]); PIN(csv[1]); PIN(csv[2]); PIN(csv[3]); PIN(stv);
#define CHK3 all_ok((stv[1] == tag) & (csv[0][1] == tag) & (csv[1][1] == tag) & (csv[2][1] == tag) & (csv[3][1] == tag))
      if (__builtin_expect(!CHK3, 0)) {
#pragma nounroll
        for (int spin = 0; spin < SPIN_CAP; ++spin) {
          asm volatile("" ::: "memory");
#pragma unroll
          for (int k = 0; k < 4; ++k) csv[k] = ld_sc1_2(rw, csoff + k * 2 * F_ * 8);
          stv = ld_sc1_2(rw, stoff);
          if (CHK3) break;
        }
      }
      const u32 s0 = csv[0][0], s1 = csv[1][0], s2 = csv[2][0], s3 = csv[3][0];
      const float cs = (__builtin_bit_cast(float, s0) + __builtin_bit_cast(float, s1)) + (__builtin_bit_cast(float, s2) + __builtin_bit_cast(float, s3));
      const float ch = rnd_bf(cs);
      LAS char* cp = sm + FS + hw * 2048 + lane * 4;
      *(LAS u32*)cp = pk_bf16(ch, cs - ch);
      *(LAS u32*)(cp + 256) = __builtin_amdgcn_perm(0u, stv[0], 0x01000100u) ^ 0x80008000u;
    }
    u32x4 rv[2][2];
    const u32 voff = WS_V + ((b & 7) * 32 + (b >> 3)) * 64 + (p16 & 3) * 16 + ((8 * hw + (p16 >> 2)) * 8 + 2 * t) * 16384;
    if (!G) {
      WAITV(12);
#pragma nounroll
      for (int spin = 0; spin < SPIN_CAP; ++spin) {
        asm volatile("" ::: "memory");
        u32x2 q = ld_sc1_2(rw, WS_V + (b + 224) * 16384 + hw * 4096);
        if (all_ok(q[1] == tag)) break;
        __builtin_amdgcn_s_sleep(8);
      }
      __builtin_amdgcn_s_sleep(20);
    }
    hsync(G ? FS + 1088 : G1_STAGE + 96);
    if (G) { while (__builtin_amdgcn_s_memrealtime() - t_v < V_DELAY) __builtin_amdgcn_s_sleep(2); }
    STAMP(8);
    {
      ALD4S(rv[0][0], voff, sw, 0, " sc1"); ALD4S(rv[0][1], voff, sw, 16384, " sc1"); ALD4S(rv[1][0], voff, sw, 32 * 16384, " sc1"); ALD4S(rv[1][1], voff, sw, 33 * 16384, " sc1");
    }
    auto mwait = [&](int off) {
      if (G) {
#pragma nounroll
        for (int spin = 0; spin < SPIN_CAP; ++spin) { if (all_ok(*(volatile LAS u32*)(sm + off + (lane & 3) * 4) == tag)) break; __builtin_amdgcn_s_sleep(1); }
        asm volatile("" ::: "memory");
      }
    };
    WAITV(4);
    wd_read(0, 0); wd_read(1, 1); wd_read(2, 2);
    STAMP(9);
    WAITV(0);
    PIN(rv[0][0]); PIN(rv[0][1]); PIN(rv[1][0]); PIN(rv[1][1]);
#define CHK4 all_ok(tags_ok(rv[0][0], tag) & tags_ok(rv[0][1], tag) & tags_ok(rv[1][0], tag) & tags_ok(rv[1][1], tag))
    if (__builtin_expect(!CHK4, 0)) {
#pragma nounroll
      for (int spin = 0; spin < SPIN_CAP; ++spin) {
        asm volatile("" ::: "memory");
#pragma unroll
        for (int k = 0; k < 2; ++k) { rv[k][0] = ld_sc1(rw, voff + k * 32 * 16384); rv[k][1] = ld_sc1(rw, voff + k * 32 * 16384 + 16384); }
        if (CHK4) break;
        __builtin_amdgcn_s_sleep(4);
      }
    }
    STAMP(12);
    LAS char* const yb = sm + FS + 8192;
#pragma unroll
    for (int k = 0; k < 2; ++k) {
      f32x4 da = mfma16_bf16(u32x2{qa8i[0], qa8i[1]}, u32x2{rv[k][0][0], rv[k][1][0]}, z4), db = mfma16_bf16(u32x2{qa8i[0], qa8i[1]}, u32x2{rv[k][0][2], rv[k][1][2]}, z4);
      if (k == 0) { f32x4 ds = mfma16_bf16(u32x2{qa8i[2], qa8i[3]}, u32x2{rv[0][0][0], rv[0][1][0]}, z4); if ((hw | p16) == 0) da = ds; }
      *(LAS u32x2*)(yb + (2 * t) * 1024 + (32 * hw + 16 * k + p16) * 8) = u32x2{pk_bf16(da[0], da[1]), pk_bf16(db[0], db[1])};
      *(LAS u32x2*)(yb + (2 * t + 1) * 1024 + (32 * hw + 16 * k + p16) * 8) = u32x2{pk_bf16(da[2], da[3]), pk_bf16(db[2], db[3])};
    }
    hsync(FS + 1024);
    STAMP(13);
    u32x4 ro[2];
#pragma unroll
    for (int a = 0; a < 2; ++a) {
      u32x4 av;
#pragma unroll
      for (int m = 0; m < 4; ++m) av[m] = *(LAS u32*)(yb + (hw + 4 * a) * 1024 + (lane + 64 * m) * 4);
      const float nyq = bf_hi(*(LAS u32*)(yb + (hw + 4 * a) * 1024));
#pragma unroll
      for (int hh = 0; hh < 2; ++hh) {
        f32x4 gre = mfma32_bf16(av, bi1re[hh], z4), gim = mfma32_bf16(av, bi1im[hh], z4);
        u32x4 bb = cmul_pack<false, true>(gre, gim, t4c[hh], t4s[hh]);
        f32x4 d = mfma32_bf16(bb, bc3re, f32x4{nyq, -nyq, nyq, -nyq});
        ro[a][2 * hh] = pk_bf16(__builtin_fmaf(d[0], FSC, bf_lo(xr[a][2 * hh])), __builtin_fmaf(d[1], FSC, bf_hi(xr[a][2 * hh])));
        ro[a][2 * hh + 1] = pk_bf16(__builtin_fmaf(d[2], FSC, bf_lo(xr[a][2 * hh + 1])), __builtin_fmaf(d[3], FSC, bf_hi(xr[a][2 * hh + 1])));
      }
      PIN(ro[a]);
    }
    STAMP(14);
    mwait(FS + 1056);
#pragma unroll
    for (int a = 0; a < 2; ++a) st_sc1(rw, WS_R + (b + 256 * hw + 1024 * a) * 1024 + (4 * p16 + t) * 16, ro[a]);
    wd_issue(w, 3, 3); wd_issue(w, 4, 4); wd_issue(w, 5, 5); wd_issue(w, 6, 0);
    WAITV(16);
    if (lane < 2) st_sc1_2(rw, WS_ST2 + b * 64 + hw * 8 + lane * 32, u32x2{lane ? stat[1] : stat[0], tag});
    STAMP(11);
  };

  if (w < 4) {
    if (is_gemm) {
      tag = load_tag();
      const int h = lane >> 5;
      int xoff[4], woff[2];
#pragma unroll
      for (int ks = 0; ks < 4; ++ks) { int tok = lane & 31; xoff[ks] = tok * 128 + (((2 * ks + h) ^ ((tok >> 1) & 7)) << 4); PIN(xoff[ks]); }
      {
        int arr = t & 1, q = p16 >> 2, p = lane & 3;
#pragma unroll
        for (int m = 0; m < 2; ++m) {
          woff[m] = 16384 + arr * 16384 + (8 * h + q) * 256 + (((hw * 2 + m) ^ (q | (arr << 2))) << 5) + (p & 1) * 16 + (p >> 1) * 8;
          PIN(woff[m]);
        }
      }
      auto rdx = [&](Frag& f, int st, int ks, int n) { f.bx[n] = *(LAS u32x4*)(sm + (st % 3) * G1_STAGE + xoff[ks] + n * 4096); };
      auto rdw = [&](Frag& f, int st, int ks, int m) {
        LAS char* buf = sm + (st % 3) * G1_STAGE;
        u32x2 lo = tr_read(buf + woff[m] + ks * 4096), hi = tr_read(buf + woff[m] + ks * 4096 + 1024);
        f.aw[m] = u32x4{lo[0], lo[1], hi[0], hi[1]};
      };
      f32x16 acc[2][4];
#pragma unroll
      for (int m = 0; m < 2; ++m)
#pragma unroll
        for (int n = 0; n < 4; ++n)
#pragma unroll
          for (int k = 0; k < 16; ++k) acc[m][n][k] = 0.f;
      Frag fr[2];
      u32 qa[2], qb[4], pv;
      auto twait = [&](int off) {
#pragma nounroll
        for (int spin = 0; spin < SPIN_CAP; ++spin) { if (all_ok(*(volatile LAS u32*)(sm + off + (lane & 3) * 4) == tag)) break; }
        asm volatile("" ::: "memory");
      };
      SB();
      STAMP(21);
      g1_issue(1, 0); g1_issue(1, 1);
      WAITV(4);
      {
        LAS char* mb = sm + G1_STAGE + 32768 + 3072;
        *(volatile LAS u32*)(mb + (lane & 3) * 4096 + hw * 4) = tag;
#pragma nounroll
        for (int spin = 0; spin < SPIN_CAP; ++spin) { if (all_ok(*(volatile LAS u32*)(mb + hw * 4096 + (lane & 3) * 4) == tag)) break; }
        asm volatile("" ::: "memory");
      }
      STAMP(1);
      rdw(fr[0], 0, 0, 0); rdx(fr[0], 0, 0, 0); rdx(fr[0], 0, 0, 1); rdx(fr[0], 0, 0, 2); rdx(fr[0], 0, 0, 3); rdw(fr[0], 0, 0, 1);
      static_for<0, 30>([&](auto KS_) {
        constexpr int KS = decltype(KS_)::v, st = KS >> 2, ks = KS & 3;
        constexpr int nst = ks < 3 ? st : st + 1, nks = (ks + 1) & 3;
        Frag& f = fr[KS & 1]; Frag& g = fr[(KS + 1) & 1];
        SB();
        if constexpr (ks == 3) {
          if constexpr (st == 0) WAITV(0);
          if constexpr (st < 6) BAR();
          else { if (__builtin_expect(!all_ok(pv == tag), 0)) twait(FS + 1072); asm volatile("" ::: "memory"); }
          STAMP(2 + st); SB();
        }
        acc[0][0] = mfma3232(f.aw[0], f.bx[0], acc[0][0]); SB(); rdw(g, nst, nks, 0); SB();
        acc[0][1] = mfma3232(f.aw[0], f.bx[1], acc[0][1]); SB(); rdx(g, nst, nks, 0); SB();
        if constexpr (KS == 0 && S1PRE != 6) { g1_issue(1, 2); SB(); }
        if constexpr (KS == 1 && S1PRE != 6) { g1_issue(1, 5); SB(); }
        acc[0][2] = mfma3232(f.aw[0], f.bx[2], acc[0][2]); SB(); rdx(g, nst, nks, 1); SB();
        acc[0][3] = mfma3232(f.aw[0], f.bx[3], acc[0][3]); SB(); rdx(g, nst, nks, 2); SB();
        if constexpr (KS == 0 && S1PRE != 6) { g1_issue(1, 3); SB(); }
        acc[1][3] = mfma3232(f.aw[1], f.bx[3], acc[1][3]); SB(); rdx(g, nst, nks, 3); SB();
        acc[1][2] = mfma3232(f.aw[1], f.bx[2], acc[1][2]); SB(); rdw(g, nst, nks, 1); SB();
        acc[1][1] = mfma3232(f.aw[1], f.bx[1], acc[1][1]); SB();
        if constexpr (KS == 0 && S1PRE != 6) { g1_issue(1, 4); SB(); }
        if constexpr (KS == 26) { pv = *(volatile LAS u32*)(sm + FS + 1072 + (lane & 3) * 4); SB(); }
        if constexpr (KS == 28) { pv = *(volatile LAS u32*)(sm + FS + 1088 + (lane & 3) * 4); SB(); }
        if constexpr (KS == 29) {
          if (__builtin_expect(!all_ok(pv == tag), 0)) twait(FS + 1088);
          asm volatile("" ::: "memory");
          LAS char* cp = sm + FS + (lane & 31) * 4;
          qa[0] = *(LAS u32*)(cp + hw * 2048); qa[1] = *(LAS u32*)(cp + hw * 2048 + 128);
#pragma unroll
          for (int n = 0; n < 4; ++n) qb[n] = *(LAS u32*)(cp + n * 2048 + 256);
          SB();
        }
        acc[1][0] = mfma3232(f.aw[1], f.bx[0], acc[1][0]); SB();
      });
      rdw(fr[1], 7, 3, 0); rdx(fr[1], 7, 3, 0); rdw(fr[1], 7, 3, 1); rdx(fr[1], 7, 3, 1); rdx(fr[1], 7, 3, 2); rdx(fr[1], 7, 3, 3);
      __builtin_amdgcn_s_setprio(3);
      STAMP(9);
      u32x4 ca[2] = {u32x4{h ? 0u : qa[0], 0u, 0u, 0u}, u32x4{h ? 0u : qa[1], 0u, 0u, 0u}}, cb = {0u, 0u, 0u, 0u};
      const u32 hoff = WS_H + ((mi * 8 + (t & 1)) * 56 + fj * 4 + hw) * 1024 + h * 256 + p16 * 16;
      float ev[8];
      auto epi_a = [&](int q) {
#pragma unroll
        for (int k = 0; k < 8; ++k) ev[k] = relu(acc[q & 1][q >> 1][k]);
      };
      auto epi_b = [&](int q) {
#pragma unroll
        for (int k = 0; k < 8; ++k) ev[k] *= acc[q & 1][q >> 1][8 + k];
      };
      auto epi_c = [&](int q) {
        __builtin_amdgcn_raw_buffer_store_b128(u32x4{pk_bf16(ev[0], ev[1]), pk_bf16(ev[2], ev[3]), pk_bf16(ev[4], ev[5]), pk_bf16(ev[6], ev[7])}, rw,
                                               hoff + (q & 1) * 512, (q >> 1) * (2 * 56 * 1024), 16);
      };
#pragma unroll
      for (int q = 0; q < 8; ++q) {
        const int m = q & 1, n = q >> 1;
        SB();
        if (m == 0) cb[0] = qb[n];
        acc[m][n] = mfma3232(fr[0].aw[m], fr[0].bx[n], acc[m][n]); SB();
        if (q >= 2) { epi_c(q - 2); SB(); }
        acc[m][n] = mfma3232(ca[m], cb, acc[m][n]); SB();
        if (q >= 1) { epi_a(q - 1); SB(); }
        acc[m][n] = mfma3232(fr[1].aw[m], fr[1].bx[n], acc[m][n]); SB();
        if (q >= 1) { epi_b(q - 1); SB(); }
      }
      epi_c(6); SB(); epi_a(7); epi_b(7); epi_c(7);
      *(volatile LAS u32*)(sm + FS + 1056 + hw * 4) = tag;
      STAMP(10);
      wd_setup();
      WAITV(4);
      if (lane == 0) __builtin_amdgcn_raw_buffer_store_b32(tag, rw, WS_F2 + (mi * 2) * 256 + (fj * 4 + hw) * 4, 0, 16);
      WAITV(1);
      if (lane == 0) __builtin_amdgcn_raw_buffer_store_b32(tag, rw, WS_F2 + (mi * 2 + 1) * 256 + (fj * 4 + hw) * 4, 0, 16);
      STAMP(12);
      __builtin_amdgcn_s_setprio(HPRIO);
    } else {
      wd_setup();
      const int un = b >> 1, ua = un >= 7, uc = (b & 1) * 7 + (ua ? un - 7 : un);
      const u32x4 scs = {ua ? ka[8] : ka[6], ua ? ka[9] : ka[7], b < 28 ? 1835008u : 0u, 0x00020000u};
      const u32 vcs = (hw * 4 + t) * 3584 + uc * 256 + p16 * 16;
      u32x4 vv[16];
      f32x4 a0 = z4, a1 = z4;
      u32 sfa, sfb;
      u32 sel_lo = 0x00003f80u, sel_hi = 0x3f800000u;
      PIN(sel_lo); PIN(sel_hi);
      const unsigned long long t_start = __builtin_amdgcn_s_memrealtime();
#pragma unroll
      for (int half = 0; half < 2; ++half) {
        if (half == 1) { while (__builtin_amdgcn_s_memrealtime() - t_start < CS_DELAY) __builtin_amdgcn_s_sleep(4); }
#pragma unroll
        for (int k = 0; k < 16; ++k) ALD4S(vv[k], vcs, scs, (half * 16 + k) * 16 * 3584, "");
        if (half == 0) {
          const int q = 64 * g + 16 * hw + p16, ks = 8 * q + jp, km = (2048 - ks) & 2047, col = (t >> 1) * 256;
          const GAS unsigned short* wa = (t & 1) ? wi : wr;
          asm volatile("global_load_ushort %0, %1, off" : "=v"(sfa) : "v"(wa + ks * DF_ + col) : "memory");
          asm volatile("global_load_ushort %0, %1, off" : "=v"(sfb) : "v"(wa + km * DF_ + col) : "memory");
          tag = load_tag();
        }
        STAMP(23 + 3 * half);
        static_for<0, 4>([&](auto Q_) {
          constexpr int Q = decltype(Q_)::v;
          if (half == 0) WAITV(14 - 4 * Q); else WAITV(12 - 4 * Q);
#pragma unroll
          for (int k = 4 * Q; k < 4 * Q + 4; ++k) {
            PIN(vv[k]);
#pragma unroll
            for (int e = 0; e < 2; ++e) {
              const u32 p = vv[k][e], q = vv[k][2 + e];
              a0[2 * e] = __builtin_amdgcn_fdot2_f32_bf16(__builtin_bit_cast(b16x2, p), __builtin_bit_cast(b16x2, sel_lo), a0[2 * e], false);
              a0[2 * e + 1] = __builtin_amdgcn_fdot2_f32_bf16(__builtin_bit_cast(b16x2, p), __builtin_bit_cast(b16x2, sel_hi), a0[2 * e + 1], false);
              a1[2 * e] = __builtin_amdgcn_fdot2_f32_bf16(__builtin_bit_cast(b16x2, q), __builtin_bit_cast(b16x2, sel_lo), a1[2 * e], false);
              a1[2 * e + 1] = __builtin_amdgcn_fdot2_f32_bf16(__builtin_bit_cast(b16x2, q), __builtin_bit_cast(b16x2, sel_hi), a1[2 * e + 1], false);
            }
          }
        });
        PIN(a0); PIN(a1);
        STAMP(24 + 3 * half);
        if (half == 0) {
          WAITV(0);
          PIN(sfa); PIN(sfb);
          const float wh = 0.5f * ((t & 1) ? bf_lo(sfa) - bf_lo(sfb) : bf_lo(sfa) + bf_lo(sfb));
          const u32 wb = pk_bf16(wh, wh) & 0xffffu;
          auto s2 = __builtin_amdgcn_permlane16_swap(wb, wb, false, false);
          if ((t & 1) == 0) st_sc1_2(rw, WS_SF + jp * 4096 + (t >> 1) * 2048 + (64 * g + 4 * p16 + hw) * 8, u32x2{(u32)s2[0] | ((u32)s2[1] << 16), tag});
          STAMP(25);
        }
      }
      if (b < 28) {
#pragma unroll
        for (int k = 0; k < 4; ++k) { a0[k] = xrow_sum(a0[k]); a1[k] = xrow_sum(a1[k]); }
        const float s0 = t == 0 ? a0[0] : t == 1 ? a0[2] : t == 2 ? a1[0] : a1[2];
        const float s1 = t == 0 ? a0[1] : t == 1 ? a0[3] : t == 2 ? a1[1] : a1[3];
        st_sc1(rw, WS_CS + hw * (2 * F_ * 8) + (ua * 14 + uc) * 1024 + p16 * 64 + t * 16, u32x4{__builtin_bit_cast(u32, s0), tag, __builtin_bit_cast(u32, s1), tag});
      }
      STAMP(3);
    }
    {
      const int off = is_gemm ? FS + 1056 : FS + 1024;
#pragma nounroll
      for (int spin = 0; spin < SPIN_CAP; ++spin) { if (all_ok(*(volatile LAS u32*)(sm + off + (lane & 3) * 4) == tag)) break; __builtin_amdgcn_s_sleep(1); }
      asm volatile("" ::: "memory");
    }
    wd_issue(w, 0, 6); wd_issue(w, 1, 7); wd_issue(w, 2, 8);
    {
#pragma nounroll
      for (int spin = 0; spin < SPIN_CAP; ++spin) { if (all_ok(*(volatile LAS u32*)(sm + FS + 1024 + hw * 4) == tag)) break; __builtin_amdgcn_s_sleep(1); }
      asm volatile("" ::: "memory");
    }
    wd_issue(w, 6, 1);
    WAITV(4);
    wd_read(0, 6); wd_read(1, 7); wd_read(2, 8);
    asm volatile("s_waitcnt lgkmcnt(0)" ::: "memory");
    wd_issue(w, 3, 6); wd_issue(w, 4, 7); wd_issue(w, 5, 8);
    STAMP(13);
  } else if (is_gemm) helper(IC<1>{});
  else helper(IC<0>{});

  wait_h_and_load();
  __builtin_amdgcn_s_setprio(0);
#pragma unroll
  for (int i = 3; i < 7; ++i) {
    LAS char* p0 = sm + (w < 4 ? wslot(i == 6 ? 1 : i + 3) : wslot(i == 6 ? 0 : i));
#pragma unroll
    for (int u = 0; u < 4; ++u) {
      LAS char* p = p0 + (wbase ^ (u << 5));
      u32x2 lo = tr_read(p), hi = tr_read(p + 512);
      wf[i][u] = u32x4{lo[0], lo[1], hi[0], hi[1]};
    }
  }
  STAMP(16);
  const int oc = lane & 7, otok = (w & 1) * 8 + (lane >> 3);
  const int orow = rt * 64 + (((w >> 1) + ct) & 3) * 16 + otok;
  const u32 roff = WS_R + orow * 1024 + (ct * 8 + oc) * 16;
  const u32 soff_ = WS_ST2 + ((orow & 255) * 8 + (orow >> 8)) * 8;
  u32x4 r0; u32x2 sto; bool rok;
#pragma unroll
  for (int tt = 0; tt < 4; ++tt) {
    f32x4 acc[4] = {z4, z4, z4, z4};
#pragma unroll
    for (int ks = 0; ks < 7; ++ks) {
#pragma unroll
      for (int u = 0; u < 4; ++u) acc[u] = mfma32_bf16(wf[ks][u], hf[tt][ks], acc[u]);
      if (tt == 0) { SB(); hf[3][ks] = ld_h(3, ks); SB(); }
    }
    if (tt == 1) sto = ld_sc1_2(rw, soff_);
    if (tt == 3) { asm volatile("" : "+v"(sto) :: "memory"); rok = all_ok(sto[1] == tag); r0 = ld_sc1(rw, roff); }
#pragma unroll
    for (int u = 0; u < 4; ++u) *(LAS f32x4*)(sm + (w < 4 ? wslot(tt < 3 ? 6 + tt : 1) : wslot(tt < 3 ? 3 + tt : 0)) + hw * 4096 + u * 1024 + t * 256 + ((p16 ^ (4 * u + (t & 2))) << 4)) = acc[u];
    SB();
  }
  STAMP(17);
  BAR();
  f32x4 sa = z4, sb = z4;
  {
    const int tt = w >> 1;
    const int o0 = (oc >> 1) * 1024 + (oc & 1) * 512 + ((otok ^ (2 * oc)) << 4);
    const int bm = (tt < 3 ? wslot(6) + tt * 16384 : wslot(1)) + o0, bh = (tt < 3 ? wslot(3) + tt * 16384 : wslot(0)) + o0;
#pragma unroll
    for (int pw = 0; pw < 4; ++pw) {
      sa += *(LAS f32x4*)(sm + bm + pw * 4096) + *(LAS f32x4*)(sm + bh + pw * 4096);
      sb += *(LAS f32x4*)(sm + bm + 256 + pw * 4096) + *(LAS f32x4*)(sm + bh + 256 + pw * 4096);
    }
  }
  STAMP(22);
  if (__builtin_expect(!rok, 0)) {
#pragma nounroll
    for (int spin = 0; spin < SPIN_CAP; ++spin) {
      asm volatile("" ::: "memory");
      sto = ld_sc1_2(rw, soff_);
      if (all_ok(sto[1] == tag)) break;
    }
    asm volatile("" ::: "memory");
    r0 = ld_sc1(rw, roff);
  }
  {
    const float rs = __builtin_amdgcn_rcpf(bf_hi(sto[0])), rs2 = rs * rs;
    *(GAS u32x4*)(out + (size_t)orow * 1024 + ct * 128 + oc * 16) =
        u32x4{pk_bf16(__builtin_fmaf(sa[0], rs2, bf_lo(r0[0])), __builtin_fmaf(sa[1], rs2, bf_hi(r0[0]))),
              pk_bf16(__builtin_fmaf(sa[2], rs2, bf_lo(r0[1])), __builtin_fmaf(sa[3], rs2, bf_hi(r0[1]))),
              pk_bf16(__builtin_fmaf(sb[0], rs2, bf_lo(r0[2])), __builtin_fmaf(sb[1], rs2, bf_hi(r0[2]))),
              pk_bf16(__builtin_fmaf(sb[2], rs2, bf_lo(r0[3])), __builtin_fmaf(sb[3], rs2, bf_hi(r0[3])))};
  }
  if (b == 0 && tid == 0) __builtin_amdgcn_raw_buffer_store_b32(tag, rw, WS_EPOCH, 0, 16);
  STAMP(18);
  STAMP_DUMP();
}

#if defined(__HIP_DEVICE_COMPILE__)
#pragma clang attribute pop
#endif

extern "C" void kernel_launch(void* const* d_in, const int* in_sizes, int n_in, void* d_out, int out_size, void* d_ws, size_t ws_size,
                              hipStream_t stream) {
  (void)hipFuncSetAttribute((const void*)fused_kernel, hipFuncAttributeMaxDynamicSharedMemorySize, LDS_BYTES);
  fused_kernel<<<256, 512, LDS_BYTES, stream>>>((const char*)d_in[0], (const unsigned short*)d_in[1], (const unsigned short*)d_in[2], (const char*)d_in[4],
                                                (const char*)d_in[5], (const char*)d_in[6], (char*)d_ws, (char*)d_out);
}
```

```cpp
#include <hip/hip_runtime.h>
#include <stdint.h>

#define LAS __attribute__((address_space(3)))
#define GAS __attribute__((address_space(1)))
#define DI __device__ __forceinline__
typedef unsigned u32;
typedef unsigned u32x2 __attribute__((ext_vector_type(2)));
typedef unsigned u32x4 __attribute__((ext_vector_type(4)));
typedef float f32x2 __attribute__((ext_vector_type(2)));
typedef float f32x4 __attribute__((ext_vector_type(4)));
typedef float f32x16 __attribute__((ext_vector_type(16)));
typedef short s16x4 __attribute__((ext_vector_type(4)));
typedef _Float16 h16x2 __attribute__((ext_vector_type(2)));
typedef _Float16 h16x4 __attribute__((ext_vector_type(4)));
typedef _Float16 h16x8 __attribute__((ext_vector_type(8)));
typedef __bf16 b16x2 __attribute__((ext_vector_type(2)));
typedef __bf16 b16x8 __attribute__((ext_vector_type(8)));
typedef __amdgpu_buffer_rsrc_t rsrc_t;

constexpr int F_ = 1792, DF_ = 257;
constexpr u32 WS_EPOCH = 0;
constexpr u32 WS_ST = 16384;
constexpr u32 WS_CS = WS_ST + 16384;
constexpr u32 WS_U = 262144;
constexpr u32 WS_V = WS_U + 4194304;
constexpr u32 WS_R = WS_V + 4194304;
constexpr u32 WS_ST2 = 229376;
constexpr u32 WS_SF = 196608;
constexpr u32 WS_F2 = WS_R + 2097152;
constexpr u32 WS_H = WS_R + 4194304;
constexpr u32 WS_END = WS_H + 7340032;
constexpr int LDS_BYTES = 163840;
constexpr int G1_STAGE = 49152;
constexpr int MB = 3 * G1_STAGE + 8192 + 1024;
constexpr int FS = 3 * G1_STAGE;
#define SPIN_CAP (1 << 17)
constexpr int CODE_LINES = 292;
#define HPRIO 2
#define S1PRE 2
#define POLLSLEEP 2
#define CS_DELAY 250
#define WARM 0
#define UPOS 0
#define TV 2
#define V_DELAY 110
#define HWIN 1
#define MTP 1
#define ROTM 1
constexpr float XS = 1.0f / 16;
constexpr float FSC = (2.0f / (2048.0f * 512.0f)) * 16.0f;
constexpr size_t WS_DBG = 64 << 20;
#define SMASK 0xffffffffu
#define STAMP(k) do {} while (0)
#define STAMP_DECL
#define STAMP_DUMP() do {} while (0)

#if defined(__HIP_DEVICE_COMPILE__)
#pragma clang attribute push(__attribute__((target("no-packed-fp32-ops"))), apply_to = function)
#endif
DI u32 pk_f16(float a, float b) { h16x2 p = {(_Float16)a, (_Float16)b}; return __builtin_bit_cast(u32, p); }
DI u32 pk_bf16(float a, float b) { b16x2 p = {(__bf16)a, (__bf16)b}; return __builtin_bit_cast(u32, p); }
DI float bf_lo(u32 v) { return __builtin_bit_cast(float, v << 16); }
DI float bf_hi(u32 v) { return __builtin_bit_cast(float, v & 0xffff0000u); }
DI float rnd_bf(float a) { return bf_lo(pk_bf16(a, a)); }
DI float relu(float a) { int i = __builtin_bit_cast(int, a); return __builtin_bit_cast(float, i > 0 ? i : 0); }
DI float cosr(float rev) { return __builtin_amdgcn_cosf(rev); }
DI float sinr(float rev) { return __builtin_amdgcn_sinf(rev); }
DI f32x4 mfma16_f16(u32x2 a, u32x2 b, f32x4 c) { return __builtin_amdgcn_mfma_f32_16x16x16f16(__builtin_bit_cast(h16x4, a), __builtin_bit_cast(h16x4, b), c, 0, 0, 0); }
DI f32x4 mfma16_bf16(u32x2 a, u32x2 b, f32x4 c) { return __builtin_amdgcn_mfma_f32_16x16x16bf16_1k(__builtin_bit_cast(s16x4, a), __builtin_bit_cast(s16x4, b), c, 0, 0, 0); }
DI f32x4 mfma32_f16(u32x4 a, u32x4 b, f32x4 c) { return __builtin_amdgcn_mfma_f32_16x16x32_f16(__builtin_bit_cast(h16x8, a), __builtin_bit_cast(h16x8, b), c, 0, 0, 0); }
DI f32x4 mfma32_bf16(u32x4 a, u32x4 b, f32x4 c) { return __builtin_amdgcn_mfma_f32_16x16x32_bf16(__builtin_bit_cast(b16x8, a), __builtin_bit_cast(b16x8, b), c, 0, 0, 0); }
DI f32x16 mfma3232(u32x4 a, u32x4 b, f32x16 c) { return __builtin_amdgcn_mfma_f32_32x32x16_bf16(__builtin_bit_cast(b16x8, a), __builtin_bit_cast(b16x8, b), c, 0, 0, 0); }
DI u32x2 tr_read(LAS char* p) { return __builtin_bit_cast(u32x2, __builtin_amdgcn_ds_read_tr16_b64_v4i16((LAS s16x4*)p)); }
#define SB() __builtin_amdgcn_sched_barrier(0)
#define BAR() asm volatile("s_waitcnt lgkmcnt(0)\n\ts_barrier" ::: "memory")
#define WAITV(n) asm volatile("s_waitcnt vmcnt(%0)" :: "i"(n) : "memory")
#define PIN(x) asm volatile("" : "+v"(x))
DI u32x4 mk_srd(const void* p, u32 bytes) { uint64_t a = (uint64_t)p; return u32x4{(u32)a, (u32)(a >> 32), bytes, 0x00020000u}; }
DI void glds16(u32 ldsbase, int imm, u32 voff, u32x4 srd, u32 soff) {
  asm volatile("s_add_u32 m0, %0, %1\n\ts_nop 0\n\tbuffer_load_dwordx4 %2, %3, %4 offen lds" :: "s"(ldsbase), "i"(imm), "v"(voff), "s"(srd), "s"(soff) : "memory", "scc");
}
DI void glds4(u32 ldsaddr, u32 voff, u32x4 srd, u32 soff) {
  asm volatile("s_mov_b32 m0, %0\n\ts_nop 0\n\tbuffer_load_dword %1, %2, %3 offen lds" :: "s"(ldsaddr), "v"(voff), "s"(srd), "s"(soff) : "memory");
}
#define ALD4(dst, voff, srd, IMM, FL) asm volatile("buffer_load_dwordx4 %0, %1, %2, 0 offen offset:" #IMM FL : "=v"(dst) : "v"(voff), "s"(srd) : "memory")
#define ALD2(dst, voff, srd, IMM, FL) asm volatile("buffer_load_dwordx2 %0, %1, %2, 0 offen offset:" #IMM FL : "=v"(dst) : "v"(voff), "s"(srd) : "memory")
#define ALD1(dst, voff, srd, IMM, FL) asm volatile("buffer_load_dword %0, %1, %2, 0 offen offset:" #IMM FL : "=v"(dst) : "v"(voff), "s"(srd) : "memory")
DI u32x4 ld_sc1(rsrc_t r, u32 off) { return __builtin_amdgcn_raw_buffer_load_b128(r, off, 0, 16); }
DI u32x2 ld_sc1_2(rsrc_t r, u32 off) { return __builtin_amdgcn_raw_buffer_load_b64(r, off, 0, 16); }
DI void st_sc1(rsrc_t r, u32 off, u32x4 v) { __builtin_amdgcn_raw_buffer_store_b128(v, r, off, 0, 16); }
DI void st_sc1_2(rsrc_t r, u32 off, u32x2 v) { __builtin_amdgcn_raw_buffer_store_b64(v, r, off, 0, 16); }
DI bool all_ok(bool ok) { return __builtin_amdgcn_ballot_w64(!ok) == 0; }
DI bool tags_ok(u32x4 a, u32 tag) { return (a[1] == tag) & (a[3] == tag); }

template <int CTRL> DI float dpp_add(float v) {
  int o = __builtin_amdgcn_update_dpp(0, __builtin_bit_cast(int, v), CTRL, 0xf, 0xf, true);
  return v + __builtin_bit_cast(float, o);
}
DI float row_sum(float v) { v = dpp_add<0xB1>(v); v = dpp_add<0x4E>(v); v = dpp_add<0x141>(v); return dpp_add<0x140>(v); }
DI float xrow_sum(float v) {
  u32 u = __builtin_bit_cast(u32, v);
  auto a = __builtin_amdgcn_permlane16_swap(u, u, false, false);
  v = __builtin_bit_cast(float, (u32)a[0]) + __builtin_bit_cast(float, (u32)a[1]);
  u = __builtin_bit_cast(u32, v);
  auto b = __builtin_amdgcn_permlane32_swap(u, u, false, false);
  return __builtin_bit_cast(float, (u32)b[0]) + __builtin_bit_cast(float, (u32)b[1]);
}
template <bool NEG, bool BF> DI u32x4 cmul_pack(f32x4 re, f32x4 im, f32x4 c, f32x4 s) {
  f32x4 a = NEG ? re * c + im * s : re * c - im * s;
  f32x4 b = NEG ? im * c - re * s : im * c + re * s;
  u32x4 o;
#pragma unroll
  for (int r = 0; r < 4; ++r) o[r] = BF ? pk_bf16(a[r], b[r]) : pk_f16(a[r], b[r]);
  return o;
}
DI u32 rot16(u32 v) { return __builtin_amdgcn_alignbit(v, v, 16); }
template <bool NEG> DI void cmul_half(f32x4& a, f32x4& b, f32x4 re, f32x4 im, f32x4 c, f32x4 s) {
  a = NEG ? re * c + im * s : re * c - im * s;
  b = NEG ? im * c - re * s : im * c + re * s;
}
template <bool BF> DI u32x4 pack4(f32x4 a, f32x4 b) {
  u32x4 o;
#pragma unroll
  for (int r = 0; r < 4; ++r) o[r] = BF ? pk_bf16(a[r], b[r]) : pk_f16(a[r], b[r]);
  return o;
}
DI void glds_a(u32 ldsbase, int imm, u32 v0, u32 v1, u32x4 srd, u32 soff) {
  asm volatile("s_add_u32 m0, %0, %1\n\ts_nop 0\n\tbuffer_load_dwordx4 %2, %4, %5 offen lds\n\tbuffer_load_dwordx4 %3, %4, %5 offen offset:1024 lds"
               :: "s"(ldsbase), "i"(imm), "v"(v0), "v"(v1), "s"(srd), "s"(soff) : "memory", "scc");
}
DI void glds_b(u32 v2, u32 v3, u32x4 srd, u32 soff) {
  asm volatile("buffer_load_dwordx4 %0, %2, %3 offen offset:2048 lds\n\tbuffer_load_dwordx4 %1, %2, %3 offen offset:3072 lds"
               :: "v"(v2), "v"(v3), "s"(srd), "s"(soff) : "memory");
}
DI void glds_c(int j, u32 v, u32x4 srd, u32 soff) {
  asm volatile("buffer_load_dwordx4 %0, %1, %2 offen offset:%3 lds" :: "v"(v), "s"(srd), "s"(soff), "i"(j * 1024) : "memory");
}
DI u32x4 cmulc_f16(f32x4 re, f32x4 im, u32x4 tw) {
  u32 z0 = pk_f16(re[0], im[0]), z1 = pk_f16(re[1], im[1]), z2 = pk_f16(re[2], im[2]), z3 = pk_f16(re[3], im[3]);
  u32 o0, o1, o2, o3;
  const u32 t0 = tw[0], t1 = tw[1], t2 = tw[2], t3 = tw[3];
#define PKM " op_sel_hi:[1,0]\n\t"
#define PKF " op_sel:[1,1,0] op_sel_hi:[0,1,1] neg_hi:[0,1,0]\n\t"
  asm("v_pk_mul_f16 %0, %4, %8" PKM "v_pk_mul_f16 %1, %5, %9" PKM "v_pk_mul_f16 %2, %6, %10" PKM "v_pk_mul_f16 %3, %7, %11" PKM
      "v_pk_fma_f16 %0, %4, %8, %0" PKF "v_pk_fma_f16 %1, %5, %9, %1" PKF "v_pk_fma_f16 %2, %6, %10, %2" PKF "v_pk_fma_f16 %3, %7, %11, %3" PKF "s_nop 1"
      : "=&v"(o0), "=&v"(o1), "=&v"(o2), "=&v"(o3) : "v"(z0), "v"(z1), "v"(z2), "v"(z3), "v"(t0), "v"(t1), "v"(t2), "v"(t3));
  return u32x4{o0, o1, o2, o3};
}
template <int N> struct IC { static constexpr int v = N; };
template <int A, int B, class Fn> DI void static_for(Fn&& f) { if constexpr (A < B) { f(IC<A>{}); static_for<A + 1, B>(f); } }

struct Frag { u32x4 bx[4], aw[2]; };
#define ALD4S(dst, voff, srd, SOFF, FL) asm volatile("buffer_load_dwordx4 %0, %1, %2, %3 offen" FL : "=v"(dst) : "v"(voff), "s"(srd), "s"(SOFF) : "memory")
#define ALD2S(dst, voff, srd, SOFF, FL) asm volatile("buffer_load_dwordx2 %0, %1, %2, %3 offen" FL : "=v"(dst) : "v"(voff), "s"(srd), "s"(SOFF) : "memory")
#define RAWBAR() do { asm volatile("s_barrier" ::: "memory"); } while (0)

typedef unsigned u32x16 __attribute__((ext_vector_type(16)));
__global__ __launch_bounds__(512, 2) void fused_kernel(const char* a_x, const unsigned short* a_wr, const unsigned short* a_wi, const char* a_wg,
                                                       const char* a_wu, const char* a_wd, char* a_ws, char* a_out) {
  extern __shared__ __attribute__((aligned(16))) char smem[];
  LAS char* sm = (LAS char*)smem;
  STAMP_DECL
  STAMP(0);
  u32x16 ka;
  asm volatile("s_load_dwordx16 %0, %1, 0x0" : "=s"(ka) : "s"(__builtin_amdgcn_kernarg_segment_ptr()) : "memory");
  const int b = blockIdx.x, tid = threadIdx.x, lane = tid & 63, w = __builtin_amdgcn_readfirstlane(tid >> 6);
  const int hw = w & 3, p16 = lane & 15, t = lane >> 4;
  const u32 lds0 = (u32)(uintptr_t)sm;
  {
    uint64_t pc; asm volatile("s_getpc_b64 %0" : "=s"(pc));
    asm volatile("s_cmp_gt_u32 %0, 7\n\ts_cbranch_scc1 1f\n\ts_mov_b32 m0, %1\n\ts_nop 0\n\tbuffer_load_dword %2, %3, 0 offen lds\n1:"
                 :: "s"(b), "s"(lds0), "v"(tid * 128), "s"(u32x4{(u32)pc, (u32)(pc >> 32), CODE_LINES * 128, 0x00020000u}) : "memory", "scc");
  }
  const bool is_gemm = b >= 32;
  const int xc = b & 7, r_ = (b - 32) >> 3;
  const int mi = (xc >> 1) * 4 + (r_ & 3), fj = (xc & 1) * 7 + (r_ >> 2);
  const int rot = ((r_ & 3) + (r_ >> 2)) & ROTM;
  const u32 lw = lds0 + hw * 4096;
  u32 vx[4], vg[4], vu[4];
#pragma unroll
  for (int j = 0; j < 4; ++j) {
    vx[j] = (mi * 128 + hw * 32 + j * 7 + (lane >> 3)) * 1024 + (((lane & 7) ^ ((4 * j + t) & 7)) << 4);
    vg[j] = (hw * 16 + j * 4 + t) * 3584 - j * 1024 + fj * 256 + ((p16 ^ (2 * t)) << 4);
    vu[j] = vg[j] ^ 128;
    PIN(vx[j]); PIN(vg[j]); PIN(vu[j]);
  }
  u32 vrow = (b + 256 * hw) * 1024 + lane * 16;
  asm volatile("s_cmp_lt_u32 %0, 4\n\ts_cbranch_scc1 1f\n\ts_setprio 2\n1:" :: "s"(w) : "scc");
  if (w < 4) vrow = 0x7ff00000u;
  const u32 lrow = w < 4 ? lw : lds0 + FS + hw * 1024;
  PIN(vrow);
  SB();
  asm volatile("s_waitcnt lgkmcnt(0)" : "+s"(ka) :: "memory");
#define KARG(i) (((uint64_t)ka[2 * (i) + 1] << 32) | ka[2 * (i)])
  const u32x4 sx = {ka[0], ka[1], 2097152u, 0x00020000u}, sg = {ka[6], ka[7], 1835008u, 0x00020000u}, su = {ka[8], ka[9], 1835008u, 0x00020000u};
  auto g1_issue = [&](int st, int k) {
    const int lb = (st % 3) * G1_STAGE + (k >> 1) * 16384, sk = st ^ rot;
    if (k == 0) glds_a(lw, lb, vx[0], vx[1], sx, sk * 128);
    if (k == 1) glds_b(vx[2], vx[3], sx, sk * 128);
    if (k == 2) glds_a(lw, lb, vg[0], vg[1], sg, sk * 64 * 3584);
    if (k == 3) glds_b(vg[2], vg[3], sg, sk * 64 * 3584);
    if (k == 4) glds_a(lw, lb, vu[0], vu[1], su, sk * 64 * 3584);
    if (k == 5) glds_b(vu[2], vu[3], su, sk * 64 * 3584);
  };
  glds16(lrow, 0, vrow, sx, 0);
  glds16(lrow + (w < 4 ? 0 : 4096), 0, vrow, sx, 1048576);
  if (w < 4 && is_gemm) {
#pragma unroll
    for (int k = 0; k < 6; ++k) g1_issue(0, k);
  }
  SB();
  STAMP(30);
  const f32x4 z4 = {0.f, 0.f, 0.f, 0.f};
  const GAS unsigned short* wr = (const GAS unsigned short*)KARG(1); const GAS unsigned short* wi = (const GAS unsigned short*)KARG(2);
  GAS char* ws = (GAS char*)KARG(6); GAS char* out = (GAS char*)KARG(7);
  auto load_tag = [&]() {
    u32 epoch;
    asm volatile("s_load_dword %0, %1, 0x0\n\ts_waitcnt lgkmcnt(0)" : "=s"(epoch) : "s"(ws) : "memory");
    return epoch * 0x9E3779B1u + 0x7F4A7C15u;
  };
  const rsrc_t rw = __builtin_amdgcn_make_buffer_rsrc((void*)ws, 0, WS_END, 0x00020000);
  const int jp = b & 7, g = b >> 3;
  const int rt = (b & 7) * 4 + ((b >> 3) & 3), ct = b >> 5;
  auto wslot = [&](int s) { return (s < 3 ? 2 * G1_STAGE : s < 6 ? -3 * 16384 : G1_STAGE - 6 * 16384) + s * 16384; };
  u32x4 wf[7][4];
  const int wbase = hw * 4096 + t * 1024 + (p16 >> 2) * 128 + (p16 & 3) * 8 + ((((p16 >> 3) | ((t & 1) << 1))) << 5);
  auto wd_read = [&](int ks, int s) {
#pragma unroll
    for (int u = 0; u < 4; ++u) {
      LAS char* p = sm + wslot(s) + (wbase ^ (u << 5));
      u32x2 lo = tr_read(p), hi = tr_read(p + 512);
      wf[ks][u] = u32x4{lo[0], lo[1], hi[0], hi[1]};
    }
  };
  u32 tag;
  auto ksof = [&](int i) { int k = i + 2 * (rt & 3); return k >= 7 ? k - 7 : k; };
  const u32 hs = WS_H + ((rt * 4) * 56 + w * 7) * 1024 + lane * 16;
  auto ld_h = [&](int j, int i) { return __builtin_amdgcn_raw_buffer_load_b128(rw, hs, (((j + ct) & 3) * 56 + ksof(i)) * 1024, 16); };
  u32x4 hf[4][7];
  const u32x4 sd_ = {ka[10], ka[11], 1835008u, 0x00020000u};
  u32 vwd[4];
  auto wd_setup = [&]() {
#pragma unroll
    for (int j = 0; j < 4; ++j) { vwd[j] = (j * 7 + (lane >> 3)) * 1024 + ct * 128 + (((lane & 7) ^ (((lane >> 4) & 1) << 1) ^ ((j & 1) << 2)) << 4); PIN(vwd[j]); }
  };
  auto wd_issue = [&](int kw, int i, int s) {
    const u32 so = kw * (224 * 1024) + ksof(i) * 32768;
    SB(); glds_a(lw, wslot(s), vwd[0], vwd[1], sd_, so); glds_b(vwd[2], vwd[3], sd_, so); SB();
  };
  auto wait_h_and_load = [&]() {
    if (w < 4) {
      const int kb = hw * 7 + (lane < 7 ? lane : lane < 14 ? lane + 21 : 0);
      const u32 foff = WS_F2 + rt * 2048 + (kb >> 2) * 128 + (kb & 3) * 4;
#pragma nounroll
      for (int spin = 0; spin < SPIN_CAP; ++spin) {
        asm volatile("" ::: "memory");
        u32 fl = __builtin_amdgcn_raw_buffer_load_b32(rw, foff, 0, 16);
        if (all_ok(fl == tag)) break;
        __builtin_amdgcn_s_sleep(POLLSLEEP);
      }
      asm volatile("" ::: "memory");
      *(volatile LAS u32*)(sm + MB + 96 + hw * 4) = tag;
    } else {
#pragma nounroll
      for (int spin = 0; spin < SPIN_CAP; ++spin) { if (all_ok(*(volatile LAS u32*)(sm + MB + 96 + hw * 4) == tag)) break; __builtin_amdgcn_s_sleep(1); }
    }
    __builtin_amdgcn_fence(__ATOMIC_ACQUIRE, "wavefront");
    asm volatile("" ::: "memory");
    STAMP(15);
#pragma unroll
    for (int tt = 0; tt < HWIN; ++tt)
#pragma unroll
      for (int i = 0; i < 7; ++i) hf[tt][i] = ld_h(tt, i);
    SB();
  };
  u32 stat01 = 0;
  const float pf = (float)p16, tf = (float)t;

  auto helper = [&](auto G_) {
    constexpr bool G = decltype(G_)::v;
    const int ht = tid & 255;
    const u32x4 sw = {ka[12], ka[13], WS_END, 0x00020000u};
    const u32x4 sfr_ = {ka[2], ka[3], 2048 * DF_ * 2, 0x00020000u}, sfi_ = {ka[4], ka[5], 2048 * DF_ * 2, 0x00020000u};
    SB();
#define DMA(st, k) do { if (G) { SB(); g1_issue(st, k); SB(); } } while (0)
    auto g1_one = [&](int st, int p) {
      const int lb = (st % 3) * G1_STAGE + (p >> 2) * 16384, sk = st ^ rot, j = p & 3;
      const u32 v = p < 4 ? vx[j] : p < 8 ? vg[j] : vu[j];
      const u32x4 sr = p < 4 ? sx : p < 8 ? sg : su;
      const u32 so = p < 4 ? sk * 128 : sk * 64 * 3584;
      if (j == 0) glds16(lw, lb, v, sr, so); else glds_c(j, v, sr, so);
    };
#define D1(st, p) do { if (G) { SB(); g1_one(st, p); SB(); } } while (0)
#define GWAIT(n) do { if (G) WAITV(n); } while (0)
#define WAITG(n) do { if (G) WAITV(n); else WAITV(0); } while (0)
#define GBAR() do { if (G) RAWBAR(); } while (0)
    auto hsync = [&](int off, int stride = 0) {
      asm volatile("s_waitcnt lgkmcnt(0)" ::: "memory");
      *(volatile LAS u32*)(sm + off + (lane & 3) * stride + hw * 4) = tag;
#pragma nounroll
      for (int spin = 0; spin < SPIN_CAP; ++spin) { if (all_ok(*(volatile LAS u32*)(sm + off + hw * stride + (lane & 3) * 4) == tag)) break; }
      asm volatile("" ::: "memory");
    };
#define XBAR(n) do { if (G) BAR(); else hsync(G1_STAGE + (n) * 16); } while (0)
    u32x4 b2re[2], b2im[2], tw[2], ac2re, ac2im, bc3re, bc3im, qa8, qa8i, bc1re, bc1im, ac4re, ac4im, t1, bi1re[2], bi1im[2];
    u32x2 b1re, b1im;
    f32x4 t2c, t2s, t4c[2], t4s[2];
    {
      const float d5 = pf * (1.0f / 512), a5 = 4.0f * tf * d5;
#pragma unroll
      for (int r = 0; r < 4; ++r) {
        const float th = a5 + r * d5, c = cosr(th * 32.0f), s = sinr(th * 32.0f);
        ac2re[r] = pk_f16(c, s); ac2im[r] = rot16(ac2re[r]) ^ 0x8000u;
        bc3im[r] = pk_bf16(s, c); bc3re[r] = rot16(bc3im[r]) ^ 0x80000000u;
#pragma unroll
        for (int hh = 0; hh < 2; ++hh) {
          const float tq = th + hh * (16.0f * d5);
          tw[hh][r] = pk_f16(cosr(tq), sinr(tq));
          b2re[hh][r] = pk_f16(cosr(tq * 16.0f), sinr(tq * 16.0f)); b2im[hh][r] = rot16(b2re[hh][r]) ^ 0x8000u;
        }
      }
      b1re = u32x2{__builtin_amdgcn_perm(ac2re[1], ac2re[0], 0x05040100u), __builtin_amdgcn_perm(ac2re[3], ac2re[2], 0x05040100u)};
      b1im = u32x2{__builtin_amdgcn_perm(ac2im[1], ac2im[0], 0x05040100u), __builtin_amdgcn_perm(ac2im[3], ac2im[2], 0x05040100u)};
      const int po = p16 & 1, j8 = p16 >> 1;
      const float j8f = (float)j8 * 0.125f;
      const float ra = 2.0f * tf * j8f, rb = ra + j8f;
      const float ca = cosr(ra), sa = sinr(ra), cb = cosr(rb), sb = sinr(rb);
      const u32 fa0 = pk_f16(ca, sa), fb0 = pk_f16(cb, sb);
      const u32 fa = po ? (rot16(fa0) ^ 0x8000u) : fa0, fb = po ? (rot16(fb0) ^ 0x8000u) : fb0;
      const u32 sel = (j8 & 3) == 0 ? 0x03020100u : (j8 < 4 ? 0x0c0c0100u : 0x01000c0cu);
      qa8 = u32x4{fa, fb, __builtin_amdgcn_perm(0u, fa, sel), __builtin_amdgcn_perm(0u, fb, sel)};
      const u32 ia = pk_bf16(sa, ca), ib = pk_bf16(sb, cb), pa = rot16(ia) ^ 0x80000000u, pb = rot16(ib) ^ 0x80000000u;
      const bool on = (po == 0) ? (t < 2) : (t >= 2);
      const u32 hc = pk_bf16(0.5f * ca, 0.f);
      qa8i = u32x4{po ? ia : pa, po ? ib : pb, (t & 1) == 0 ? (po ? hc << 16 : hc) : (on ? pa : 0u), on ? pb : 0u};
      PIN(ac2re); PIN(ac2im); PIN(bc3re); PIN(bc3im); PIN(tw[0]); PIN(tw[1]); PIN(b2re[0]); PIN(b2re[1]); PIN(b2im[0]); PIN(b2im[1]); PIN(b1re); PIN(b1im); PIN(qa8); PIN(qa8i);
    }
    SB();
    tag = load_tag();
    STAMP(21);
    LAS char* const img = sm + FS + hw * 1024;
    u32 stat[2];
    u32x4 xr[2];
    WAITV(0);
    STAMP(1);
    {
      const u32 vo = (8 * b + 2 * hw) * DF_ * 2 + (lane >> 5) * (DF_ * 2 + 2) + (lane & 31) * 16;
      glds16(lds0 + hw * 1024, FS + 8192, vo, sfr_, 0); glds16(lds0 + hw * 1024, FS + 12288, vo, sfi_, 0);
    }
    auto tpat = [&](int q, int u) { const u32 r = tag >> (1 + 7 * q + 3 * u); return (tag & 1u) | ((r & 3u) << 1) | ((r & 28u) << 14); };
    {
      float v[2][8], mean[2], var[2];
#pragma unroll
      for (int a = 0; a < 2; ++a) {
        const u32x4 xv = *(LAS u32x4*)(img + a * 4096 + (4 * p16 + t) * 16);
        xr[a] = xv;
#pragma unroll
        for (int k = 0; k < 4; ++k) { v[a][2 * k] = bf_lo(xv[k]); v[a][2 * k + 1] = bf_hi(xv[k]); }
      }
#pragma unroll
      for (int a = 0; a < 2; ++a) mean[a] = xrow_sum(row_sum(((v[a][0] + v[a][1]) + (v[a][2] + v[a][3])) + ((v[a][4] + v[a][5]) + (v[a][6] + v[a][7])))) * (1.0f / 512);
#pragma unroll
      for (int a = 0; a < 2; ++a) {
        float q0 = 0.f, q1 = 0.f;
#pragma unroll
        for (int k = 0; k < 4; ++k) { float d = v[a][k] - mean[a], e = v[a][k + 4] - mean[a]; q0 += d * d; q1 += e * e; }
        var[a] = xrow_sum(row_sum(q0 + q1)) * (1.0f / 512);
      }
#pragma unroll
      for (int a = 0; a < 2; ++a) {
        stat[a] = pk_bf16(mean[a], __builtin_amdgcn_sqrtf(rnd_bf(rnd_bf(var[a]) + 1e-5f)));
        const float sc = __builtin_amdgcn_rcpf(bf_hi(stat[a])) * XS, of = -bf_lo(stat[a]) * sc;
        u32x4 xh;
#pragma unroll
        for (int k = 0; k < 4; ++k) xh[k] = pk_f16(__builtin_fmaf(v[a][2 * k], sc, of), __builtin_fmaf(v[a][2 * k + 1], sc, of));
        *(LAS u32x4*)(img + a * 4096 + (((4 * p16 + t) ^ ((p16 >> 1) & 2)) << 4)) = xh;
      }
      asm volatile("" ::: "memory");
      STAMP(23);
      u32x2 tra[2][2];
#pragma unroll
      for (int a = 0; a < 2; ++a)
#pragma unroll
        for (int hh = 0; hh < 2; ++hh) tra[a][hh] = tr_read(img + a * 4096 + (4 * t + (p16 >> 2)) * 64 + ((hh ^ (t & 1)) << 5) + (p16 & 3) * 8);
      D1(2, 0); D1(2, 1);
      f32x4 d1re[2][2], d1im[2][2], d2re[2], d2im[2];
#pragma unroll
      for (int a = 0; a < 2; ++a) {
#pragma unroll
        for (int hh = 0; hh < 2; ++hh) { d1re[a][hh] = mfma16_f16(tra[a][hh], b1re, z4); d1im[a][hh] = mfma16_f16(tra[a][hh], b1im, z4); }
        D1(2, 2 + a);
      }
#pragma unroll
      for (int a = 0; a < 2; ++a)
#pragma unroll
        for (int hh = 0; hh < 2; ++hh) {
          u32x4 ta = cmulc_f16(d1re[a][hh], d1im[a][hh], tw[hh]);
          d2re[a] = mfma32_f16(ta, b2re[hh], hh ? d2re[a] : z4); d2im[a] = mfma32_f16(ta, b2im[hh], hh ? d2im[a] : z4);
          D1(2, 4 + 2 * a + hh);
        }
      float ny[2];
#pragma unroll
      for (int a = 0; a < 2; ++a) { f32x4 q = d1re[a][0] + d1re[a][1]; ny[a] = xrow_sum((q[0] - q[1]) + (q[2] - q[3])); D1(2, 8 + a); }
      int e = 4 * p16 + t; e ^= (e >> 3) & 3;
#pragma unroll
      for (int a = 0; a < 2; ++a) {
        if (lane == 0) d2im[a][0] = ny[a];
        *(LAS u32x4*)(img + a * 4096 + e * 16) = pack4<false>(d2re[a], d2im[a]);
        D1(2, 10 + a);
      }
    }
    STAMP(24);
    WAITG(12);
    XBAR(0);
    STAMP(2);
    D1(3, 0);
    u32x4 uo[2][2];
    {
      u32x2 e0[2], e1[2];
#pragma unroll
      for (int k = 0; k < 2; ++k) {
        int e = 16 * hw + 8 * k + (p16 >> 1); e ^= (e >> 3) & 3;
        e0[k] = *(LAS u32x2*)(sm + FS + (2 * t) * 1024 + e * 16 + (p16 & 1) * 8);
        e1[k] = *(LAS u32x2*)(sm + FS + (2 * t + 1) * 1024 + e * 16 + (p16 & 1) * 8);
      }
      D1(3, 1);
      u32 wl[2][2][2], wh[2][2][2];
      {
        LAS char* fp = sm + FS + 8192 + t * 1024 + hw * 128 + p16 * 4;
#pragma unroll
        for (int hf = 0; hf < 2; ++hf)
#pragma unroll
          for (int k = 0; k < 2; ++k) {
#pragma unroll
            for (int u = 0; u < 2; ++u) {
              LAS char* q = fp + hf * 510 + k * 64 + u * 2;
              wl[hf][k][u] = *(LAS unsigned short*)q; wh[hf][k][u] = *(LAS unsigned short*)(q + 4096);
            }
            if (k) D1(3, 2 + hf);
          }
      }
      f32x4 d8[4];
#pragma unroll
      for (int u = 0; u < 4; ++u) d8[u] = mfma16_f16(u32x2{qa8[0], qa8[1]}, u32x2{e0[u >> 1][u & 1], e1[u >> 1][u & 1]}, z4);
      f32x4 d8s = mfma16_f16(u32x2{qa8[2], qa8[3]}, u32x2{e0[0][0], e1[0][0]}, z4);
      D1(3, 4);
      u32 p0 = tpat(p16 & 3, 0), p1 = tpat(p16 & 3, 1);
      PIN(p0); PIN(p1);
      D1(3, 5);
      if ((hw | p16) == 0) d8[0] = d8s;
      D1(3, 6); D1(3, 7);
#pragma unroll
      for (int hf = 0; hf < 2; ++hf)
#pragma unroll
        for (int k = 0; k < 2; ++k) {
          uo[hf][k] = u32x4{(pk_f16(d8[2 * k][2 * hf], d8[2 * k][2 * hf + 1]) & 0xfff8fff8u) | p0, wl[hf][k][0] | (wh[hf][k][0] << 16),
                            (pk_f16(d8[2 * k + 1][2 * hf], d8[2 * k + 1][2 * hf + 1]) & 0xfff8fff8u) | p1, wl[hf][k][1] | (wh[hf][k][1] << 16)};
          PIN(uo[hf][k]);
          D1(3, 8 + 2 * hf + k);
        }
    }
    auto ustore = [&]() {
      const u32 ub = WS_U + b * 16384 + (2 * t) * 2048 + hw * 512 + p16 * 16;
#pragma unroll
      for (int hf = 0; hf < 2; ++hf)
#pragma unroll
        for (int k = 0; k < 2; ++k) st_sc1(rw, ub + hf * 2048 + k * 256, uo[hf][k]);
      if (lane < 2) st_sc1_2(rw, WS_ST + b * 64 + hw * 8 + lane * 32, u32x2{lane ? stat[1] : stat[0], tag});
      STAMP(19);
    };
    ustore();
    GWAIT(12 + 5);
    GBAR();
    STAMP(3);
    {
      const float jf = (float)jp;
      const float d1 = (8.0f * pf + jf) * (1.0f / 2048), a1 = 4.0f * tf * d1, d2 = pf * (1.0f / 256), a2 = (32.0f * tf + jf) * (pf * (1.0f / 2048));
#pragma unroll
      for (int r = 0; r < 4; ++r) {
        D1(4, 3 * r);
        const float n1 = a1 + r * d1, n2 = a2 + r * d2;
        u32 q0 = pk_f16(cosr(n1 * 16.0f), sinr(n1 * 16.0f)), q1 = rot16(q0) ^ 0x8000u, q2 = pk_f16(cosr(n1), sinr(n1));
        PIN(q0); PIN(q1); PIN(q2);
        bc1re[r] = q0; bc1im[r] = q1; t1[r] = q2;
        D1(4, 3 * r + 1);
        u32 q3 = pk_bf16(sinr(n2 * 16.0f), cosr(n2 * 16.0f)), q4 = rot16(q3) ^ 0x80000000u;
        PIN(q3); PIN(q4);
        ac4im[r] = q3; ac4re[r] = q4;
        D1(4, 3 * r + 2);
        float q5 = cosr(n2), q6 = sinr(n2);
        PIN(q5); PIN(q6);
        t2c[r] = q5; t2s[r] = q6;
      }
      PIN(bc1re); PIN(bc1im); PIN(ac4re); PIN(ac4im); PIN(t1); PIN(t2c); PIN(t2s);
    }
    {
      const u32 vo = b * 7168 + hw * 2048 + lane * 16;
      glds_a(lds0 + hw * 2048, G ? FS : 0, vo, (hw < 3 ? vo : 0x7fffffffu), sd_, 0);
    }
    GWAIT(5 + 12 + 2);
    XBAR(1);
    STAMP(4);
#pragma unroll
    for (int hh = 0; hh < 2; ++hh) {
      const float n2 = (float)(8 * (p16 >> 2) + (p16 & 3)) + 4.0f * hh;
#pragma unroll
      for (int m = 0; m < 4; ++m) {
        D1(5, 6 * hh + (3 * m) / 2);
        float rev = n2 * ((tf + 4.0f * m) * (1.0f / 32));
        u32 q0 = pk_bf16(sinr(rev), cosr(rev)), q1 = rot16(q0) ^ 0x80000000u;
        PIN(q0); PIN(q1);
        bi1im[hh][m] = q0; bi1re[hh][m] = q1;
        if (m & 1) D1(5, 6 * hh + (3 * m) / 2 + 1);
        float rev2 = n2 * ((4.0f * tf + m) * (1.0f / 512));
        float q2 = cosr(rev2), q3 = sinr(rev2);
        PIN(q2); PIN(q3);
        t4c[hh][m] = q2; t4s[hh][m] = q3;
      }
      PIN(bi1re[hh]); PIN(bi1im[hh]); PIN(t4c[hh]); PIN(t4s[hh]);
    }
    u32x4 ug[4];
    const int uq = ht & 3;
    const int ui = (((ht >> 2) & 3) << 4) + (ht >> 4);
    const u32 uoff = WS_U + ui * 16384 + jp * 2048 + g * 64 + uq * 16;
    auto caddr = [&](int i) { return (LAS char*)(sm + FS + uq * 2048 + ((((i & 192) + 4 * (i & 15) + ((i >> 4) & 3)) * 4) ^ (uq << 5))); };
    if (!G) {
#pragma nounroll
      for (int spin = 0; spin < SPIN_CAP; ++spin) {
        asm volatile("" ::: "memory");
        u32x2 q = ld_sc1_2(rw, WS_ST + (b + 224) * 64 + hw * 8);
        if (all_ok(q[1] == tag)) break;
        __builtin_amdgcn_s_sleep(8);
      }
      __builtin_amdgcn_s_sleep(20);
    }
    STAMP(26);
#pragma unroll
    for (int k = 0; k < 4; ++k) ALD4S(ug[k], uoff, sw, k * 64 * 16384, " sc1");
    GWAIT(12 + 4);
    GBAR();
    STAMP(5);
#pragma unroll
    for (int k = 0; k < 8; ++k) D1(6, k);
    STAMP(27);
    WAITG(8);
    PIN(ug[0]); PIN(ug[1]); PIN(ug[2]); PIN(ug[3]);
    const u32 c0 = tpat(uq, 0), c1 = tpat(uq, 1);
#define CHK1 all_ok(((((ug[0][0] ^ c0) | (ug[1][0] ^ c0) | (ug[2][0] ^ c0) | (ug[3][0] ^ c0) | (ug[0][2] ^ c1) | (ug[1][2] ^ c1) | (ug[2][2] ^ c1) | (ug[3][2] ^ c1)) & 0x00070007u) == 0))
    if (__builtin_expect(!CHK1, 0)) {
#pragma nounroll
      for (int spin = 0; spin < SPIN_CAP; ++spin) {
        asm volatile("" ::: "memory");
#pragma unroll
        for (int k = 0; k < 4; ++k) ug[k] = ld_sc1(rw, uoff + k * 64 * 16384);
        if (CHK1) break;
      }
    }
    {
      LAS char* cp = caddr(ui);
      const u32 x0 = c0 ^ 0x00040004u, x1 = c1 ^ 0x00040004u;
#pragma unroll
      for (int k = 0; k < 4; ++k) {
        *(LAS u32*)(cp + k * 256) = ug[k][0] ^ x0; *(LAS u32*)(cp + k * 256 + 1024) = ug[k][2] ^ x1;
        *(LAS u32*)(cp + k * 256 + 8192) = ug[k][1]; *(LAS u32*)(cp + k * 256 + 9216) = ug[k][3];
        D1(6, 8 + k);
      }
    }
    u32x4 fsp[2];
    const bool special = !G && b < 8 && hw == 0;
    if (special) {
#pragma nounroll
      for (int spin = 0; spin < SPIN_CAP; ++spin) {
        asm volatile("" ::: "memory");
        bool ok = true;
#pragma unroll
        for (int c = 0; c < 2; ++c) {
          const u32x4 fa = ld_sc1(rw, WS_SF + jp * 4096 + c * 2048 + lane * 32), fb = ld_sc1(rw, WS_SF + jp * 4096 + c * 2048 + lane * 32 + 16);
          ok &= tags_ok(fa, tag) & tags_ok(fb, tag);
          fsp[c] = u32x4{fa[0], fa[2], fb[0], fb[2]};
        }
        if (all_ok(ok)) break;
      }
    }
    STAMP(28);
    XBAR(3);
    STAMP(6);
    LAS char* const colp = sm + FS + hw * 2048 + ((lane * 16) ^ (hw << 5));
    {
      u32x4 uin[2], fin[2];
#pragma unroll
      for (int a = 0; a < 2; ++a) { uin[a] = *(LAS u32x4*)(colp + a * 1024); fin[a] = *(LAS u32x4*)(colp + 8192 + a * 1024); }
      const bool two = special && (jp & 3) == 0;
      f32x4 dre[2], dim[2], keep;
      auto col_xform = [&](auto NA_) {
        constexpr int NA = decltype(NA_)::v;
        u32x4 ta[2];
        D1(7, 0); D1(7, 1);
#pragma unroll
        for (int a = 0; a < NA; ++a) { dre[a] = mfma32_f16(uin[a], bc1re, z4); dim[a] = mfma32_f16(uin[a], bc1im, z4); }
        D1(7, 2);
#pragma unroll
        for (int a = 0; a < NA; ++a) { ta[a] = cmulc_f16(dre[a], dim[a], t1); dre[a] = mfma32_f16(ac2re, ta[a], z4); dim[a] = mfma32_f16(ac2im, ta[a], z4); D1(7, 3 + a); }
#pragma unroll
        for (int a = 0; a < NA; ++a) {
          f32x4 fr_, fi_, ca, cb;
#pragma unroll
          for (int r = 0; r < 4; ++r) { fr_[r] = bf_lo(fin[a][r]); fi_[r] = bf_hi(fin[a][r]); }
          PIN(fr_); PIN(fi_);
          D1(7, 5 + 2 * a);
          ta[a] = cmul_pack<false, true>(dre[a], dim[a], fr_, fi_); dre[a] = mfma32_bf16(ta[a], bc3re, z4); dim[a] = mfma32_bf16(ta[a], bc3im, z4);
          D1(7, 6 + 2 * a);
        }
#pragma unroll
        for (int a = 0; a < NA; ++a) {
          ta[a] = cmul_pack<false, true>(dre[a], dim[a], t2c, t2s); dre[a] = mfma32_bf16(ac4re, ta[a], z4); dim[a] = mfma32_bf16(ac4im, ta[a], z4);
          D1(7, 9 + a);
        }
        D1(7, 11);
      };
      if (!G) {
        if (special) {
          fin[0] = jp > 4 ? fsp[1] : fsp[0];
          if (two) {
            const u32x4 us = uin[0];
            uin[0] = us >> 16; fin[0] = fsp[1];
            col_xform(IC<1>{});
            keep = dre[0];
            uin[0] = us & 0xffffu; fin[0] = fsp[0];
          }
        }
      }
      col_xform(IC<2>{});
      if (!G) { if (two) dim[0] = keep; }
#pragma unroll
      for (int a = 0; a < 2; ++a) *(LAS u32x4*)(colp + a * 1024) = pack4<true>(dre[a], dim[a]);
    }
    u32x2 csv[4], stv;
    const u32 csoff = WS_CS + ((t & 1) * F_ + fj * 128 + hw * 32 + (lane >> 5) * 16 + ((p16 >> 2) & 1) * 8 + (p16 >> 3) * 4 + (p16 & 3)) * 8;
    const u32 stoff = WS_ST + (((mi & 1) * 128 + hw * 32 + (lane & 31)) * 8 + (mi >> 1)) * 8;
    if (G) {
      ALD2S(csv[0], csoff, sw, 0, " sc1"); ALD2S(csv[1], csoff, sw, 2 * F_ * 8, " sc1"); ALD2S(csv[2], csoff, sw, 4 * F_ * 8, " sc1"); ALD2S(csv[3], csoff, sw, 6 * F_ * 8, " sc1");
      ALD2(stv, stoff, sw, 0, " sc1");
    }
    STAMP(29);
    GWAIT(12 + 5);
    XBAR(4);
    STAMP(7);
    {
      const int ps = ht >> 2;
      u32 vo[4][2];
#pragma unroll
      for (int k = 0; k < 4; ++k) { LAS char* cp = caddr((ps >> 5) + 2 * k + 8 * (ps & 31)); vo[k][0] = *(LAS u32*)cp; vo[k][1] = *(LAS u32*)(cp + 1024); }
#pragma unroll
      for (int k = 0; k < 4; ++k) st_sc1(rw, WS_V + b * 16384 + k * 4096 + ht * 16, u32x4{vo[k][0], tag, vo[k][1], tag});
    }
    const unsigned long long t_v = __builtin_amdgcn_s_memrealtime();
    STAMP(20);
    wd_setup();
    if (G) {
      WAITV(4);
      *(volatile LAS u32*)(sm + MB + 48 + hw * 4) = tag;
      PIN(csv[0]); PIN(csv[1]); PIN(csv[2]); PIN(csv[3]); PIN(stv);
#define CHK3 all_ok((stv[1] == tag) & (csv[0][1] == tag) & (csv[1][1] == tag) & (csv[2][1] == tag) & (csv[3][1] == tag))
      if (__builtin_expect(!CHK3, 0)) {
#pragma nounroll
        for (int spin = 0; spin < SPIN_CAP; ++spin) {
          asm volatile("" ::: "memory");
#pragma unroll
          for (int k = 0; k < 4; ++k) csv[k] = ld_sc1_2(rw, csoff + k * 2 * F_ * 8);
          stv = ld_sc1_2(rw, stoff);
          if (CHK3) break;
        }
      }
      const u32 s0 = csv[0][0], s1 = csv[1][0], s2 = csv[2][0], s3 = csv[3][0];
      const float cs = (__builtin_bit_cast(float, s0) + __builtin_bit_cast(float, s1)) + (__builtin_bit_cast(float, s2) + __builtin_bit_cast(float, s3));
      const float ch = rnd_bf(cs);
      LAS char* cp = sm + FS + 8192 + hw * 2048 + lane * 4;
      *(LAS u32*)cp = pk_bf16(ch, cs - ch);
      *(LAS u32*)(cp + 256) = __builtin_amdgcn_perm(0u, stv[0], 0x01000100u) ^ 0x80008000u;
      asm volatile("s_waitcnt lgkmcnt(0)" ::: "memory");
      *(volatile LAS u32*)(sm + MB + 80 + hw * 4) = tag;
    }
    wd_issue(w, 0, 0); wd_issue(w, 1, 1); wd_issue(w, 2, 2);
    if (G) WAITV(12);
    u32x4 rv[2][2];
    const u32 voff = WS_V + ((b & 7) * 32 + (b >> 3)) * 64 + (p16 & 3) * 16 + ((8 * hw + (p16 >> 2)) * 8 + 2 * t) * 16384;
    if (!G) {
      WAITV(12);
#pragma nounroll
      for (int spin = 0; spin < SPIN_CAP; ++spin) {
        asm volatile("" ::: "memory");
        u32x2 q = ld_sc1_2(rw, WS_V + (b + 224) * 16384 + hw * 4096);
        if (all_ok(q[1] == tag)) break;
        __builtin_amdgcn_s_sleep(8);
      }
      __builtin_amdgcn_s_sleep(20);
    }
    hsync(G ? MB + 64 : G1_STAGE + 96);
    if (G) { while (__builtin_amdgcn_s_memrealtime() - t_v < V_DELAY) __builtin_amdgcn_s_sleep(2); }
    STAMP(8);
    {
      ALD4S(rv[0][0], voff, sw, 0, " sc1"); ALD4S(rv[0][1], voff, sw, 16384, " sc1"); ALD4S(rv[1][0], voff, sw, 32 * 16384, " sc1"); ALD4S(rv[1][1], voff, sw, 33 * 16384, " sc1");
    }
    auto mwait = [&](int off) {
      if (G) {
#pragma nounroll
        for (int spin = 0; spin < SPIN_CAP; ++spin) { if (all_ok(*(volatile LAS u32*)(sm + off + (lane & 3) * 4) == tag)) break; __builtin_amdgcn_s_sleep(1); }
        asm volatile("" ::: "memory");
      }
    };
    WAITV(4);
    wd_read(0, 0); wd_read(1, 1); wd_read(2, 2);
    asm volatile("s_waitcnt lgkmcnt(0)" ::: "memory");
    *(volatile LAS u32*)(sm + MB + 112 + hw * 4) = tag;
    STAMP(9);
    WAITV(0);
    PIN(rv[0][0]); PIN(rv[0][1]); PIN(rv[1][0]); PIN(rv[1][1]);
#define CHK4 all_ok(tags_ok(rv[0][0], tag) & tags_ok(rv[0][1], tag) & tags_ok(rv[1][0], tag) & tags_ok(rv[1][1], tag))
    if (__builtin_expect(!CHK4, 0)) {
#pragma nounroll
      for (int spin = 0; spin < SPIN_CAP; ++spin) {
        asm volatile("" ::: "memory");
#pragma unroll
        for (int k = 0; k < 2; ++k) { rv[k][0] = ld_sc1(rw, voff + k * 32 * 16384); rv[k][1] = ld_sc1(rw, voff + k * 32 * 16384 + 16384); }
        if (CHK4) break;
        __builtin_amdgcn_s_sleep(4);
      }
    }
    STAMP(12);
    LAS char* const yb = sm + FS;
#pragma unroll
    for (int k = 0; k < 2; ++k) {
      f32x4 da = mfma16_bf16(u32x2{qa8i[0], qa8i[1]}, u32x2{rv[k][0][0], rv[k][1][0]}, z4), db = mfma16_bf16(u32x2{qa8i[0], qa8i[1]}, u32x2{rv[k][0][2], rv[k][1][2]}, z4);
      if (k == 0) { f32x4 ds = mfma16_bf16(u32x2{qa8i[2], qa8i[3]}, u32x2{rv[0][0][0], rv[0][1][0]}, z4); if ((hw | p16) == 0) da = ds; }
      *(LAS u32x2*)(yb + (2 * t) * 1024 + (32 * hw + 16 * k + p16) * 8) = u32x2{pk_bf16(da[0], da[1]), pk_bf16(db[0], db[1])};
      *(LAS u32x2*)(yb + (2 * t + 1) * 1024 + (32 * hw + 16 * k + p16) * 8) = u32x2{pk_bf16(da[2], da[3]), pk_bf16(db[2], db[3])};
    }
    hsync(MB + 0);
    STAMP(13);
    u32x4 ro[2];
#pragma unroll
    for (int a = 0; a < 2; ++a) {
      u32x4 av;
#pragma unroll
      for (int m = 0; m < 4; ++m) av[m] = *(LAS u32*)(yb + (hw + 4 * a) * 1024 + (lane + 64 * m) * 4);
      const float nyq = bf_hi(*(LAS u32*)(yb + (hw + 4 * a) * 1024));
#pragma unroll
      for (int hh = 0; hh < 2; ++hh) {
        f32x4 gre = mfma32_bf16(av, bi1re[hh], z4), gim = mfma32_bf16(av, bi1im[hh], z4);
        u32x4 bb = cmul_pack<false, true>(gre, gim, t4c[hh], t4s[hh]);
        f32x4 d = mfma32_bf16(bb, bc3re, f32x4{nyq, -nyq, nyq, -nyq});
        ro[a][2 * hh] = pk_bf16(__builtin_fmaf(d[0], FSC, bf_lo(xr[a][2 * hh])), __builtin_fmaf(d[1], FSC, bf_hi(xr[a][2 * hh])));
        ro[a][2 * hh + 1] = pk_bf16(__builtin_fmaf(d[2], FSC, bf_lo(xr[a][2 * hh + 1])), __builtin_fmaf(d[3], FSC, bf_hi(xr[a][2 * hh + 1])));
      }
      PIN(ro[a]);
    }
    STAMP(14);
    mwait(MB + 32);
#pragma unroll
    for (int a = 0; a < 2; ++a) st_sc1(rw, WS_R + (b + 256 * hw + 1024 * a) * 1024 + (4 * p16 + t) * 16, ro[a]);
    stat01 = lane ? stat[1] : stat[0];
    STAMP(11);
  };

  if (w < 4) {
    if (is_gemm) {
      tag = load_tag();
      const int h = lane >> 5;
      int xoff[4], woff[2];
#pragma unroll
      for (int ks = 0; ks < 4; ++ks) { int tok = lane & 31; xoff[ks] = tok * 128 + (((2 * ks + h) ^ ((tok >> 1) & 7)) << 4); PIN(xoff[ks]); }
      {
        int arr = t & 1, q = p16 >> 2, p = lane & 3;
#pragma unroll
        for (int m = 0; m < 2; ++m) {
          woff[m] = 16384 + arr * 16384 + (8 * h + q) * 256 + (((hw * 2 + m) ^ (q | (arr << 2))) << 5) + (p & 1) * 16 + (p >> 1) * 8;
          PIN(woff[m]);
        }
      }
      auto rdx = [&](Frag& f, int st, int ks, int n) { f.bx[n] = *(LAS u32x4*)(sm + (st % 3) * G1_STAGE + xoff[ks] + n * 4096); };
      auto rdw = [&](Frag& f, int st, int ks, int m) {
        LAS char* buf = sm + (st % 3) * G1_STAGE;
        u32x2 lo = tr_read(buf + woff[m] + ks * 4096), hi = tr_read(buf + woff[m] + ks * 4096 + 1024);
        f.aw[m] = u32x4{lo[0], lo[1], hi[0], hi[1]};
      };
      f32x16 acc[2][4];
#pragma unroll
      for (int m = 0; m < 2; ++m)
#pragma unroll
        for (int n = 0; n < 4; ++n)
#pragma unroll
          for (int k = 0; k < 16; ++k) acc[m][n][k] = 0.f;
      Frag fr[2];
      u32 qa[2], qb[4], pv;
      auto twait = [&](int off) {
#pragma nounroll
        for (int spin = 0; spin < SPIN_CAP; ++spin) { if (all_ok(*(volatile LAS u32*)(sm + off + (lane & 3) * 4) == tag)) break; }
        asm volatile("" ::: "memory");
      };
      SB();
      STAMP(21);
      g1_issue(1, 0); g1_issue(1, 1);
      WAITV(4);
      {
        LAS char* mb = sm + G1_STAGE + 32768 + 3072;
        *(volatile LAS u32*)(mb + (lane & 3) * 4096 + hw * 4) = tag;
#pragma nounroll
        for (int spin = 0; spin < SPIN_CAP; ++spin) { if (all_ok(*(volatile LAS u32*)(mb + hw * 4096 + (lane & 3) * 4) == tag)) break; }
        asm volatile("" ::: "memory");
      }
      STAMP(1);
      rdw(fr[0], 0, 0, 0); rdx(fr[0], 0, 0, 0); rdx(fr[0], 0, 0, 1); rdx(fr[0], 0, 0, 2); rdx(fr[0], 0, 0, 3); rdw(fr[0], 0, 0, 1);
      static_for<0, 30>([&](auto KS_) {
        constexpr int KS = decltype(KS_)::v, st = KS >> 2, ks = KS & 3;
        constexpr int nst = ks < 3 ? st : st + 1, nks = (ks + 1) & 3;
        Frag& f = fr[KS & 1]; Frag& g = fr[(KS + 1) & 1];
        SB();
        if constexpr (ks == 3) {
          if constexpr (st == 0) WAITV(0);
          if constexpr (st < 6) BAR();
          else { if (__builtin_expect(!all_ok(pv == tag), 0)) twait(MB + 48); asm volatile("" ::: "memory"); }
          STAMP(2 + st); SB();
        }
        acc[0][0] = mfma3232(f.aw[0], f.bx[0], acc[0][0]); SB(); rdw(g, nst, nks, 0); SB();
        acc[0][1] = mfma3232(f.aw[0], f.bx[1], acc[0][1]); SB(); rdx(g, nst, nks, 0); SB();
        if constexpr (KS == 0 && S1PRE != 6) { g1_issue(1, 2); SB(); }
        if constexpr (KS == 1 && S1PRE != 6) { g1_issue(1, 5); SB(); }
        acc[0][2] = mfma3232(f.aw[0], f.bx[2], acc[0][2]); SB(); rdx(g, nst, nks, 1); SB();
        acc[0][3] = mfma3232(f.aw[0], f.bx[3], acc[0][3]); SB(); rdx(g, nst, nks, 2); SB();
        if constexpr (KS == 0 && S1PRE != 6) { g1_issue(1, 3); SB(); }
        acc[1][3] = mfma3232(f.aw[1], f.bx[3], acc[1][3]); SB(); rdx(g, nst, nks, 3); SB();
        acc[1][2] = mfma3232(f.aw[1], f.bx[2], acc[1][2]); SB(); rdw(g, nst, nks, 1); SB();
        acc[1][1] = mfma3232(f.aw[1], f.bx[1], acc[1][1]); SB();
        if constexpr (KS == 0 && S1PRE != 6) { g1_issue(1, 4); SB(); }
        if constexpr (KS == 26) { pv = *(volatile LAS u32*)(sm + MB + 48 + (lane & 3) * 4); SB(); }
        if constexpr (KS == 28) { pv = *(volatile LAS u32*)(sm + MB + 80 + (lane & 3) * 4); SB(); }
        if constexpr (KS == 29) {
          if (__builtin_expect(!all_ok(pv == tag), 0)) twait(MB + 80);
          asm volatile("" ::: "memory");
          LAS char* cp = sm + FS + 8192 + (lane & 31) * 4;
          qa[0] = *(LAS u32*)(cp + hw * 2048); qa[1] = *(LAS u32*)(cp + hw * 2048 + 128);
#pragma unroll
          for (int n = 0; n < 4; ++n) qb[n] = *(LAS u32*)(cp + n * 2048 + 256);
          SB();
        }
        acc[1][0] = mfma3232(f.aw[1], f.bx[0], acc[1][0]); SB();
      });
      rdw(fr[1], 7, 3, 0); rdx(fr[1], 7, 3, 0); rdw(fr[1], 7, 3, 1); rdx(fr[1], 7, 3, 1); rdx(fr[1], 7, 3, 2); rdx(fr[1], 7, 3, 3);
      __builtin_amdgcn_s_setprio(3);
      STAMP(9);
      u32x4 ca[2] = {u32x4{h ? 0u : qa[0], 0u, 0u, 0u}, u32x4{h ? 0u : qa[1], 0u, 0u, 0u}}, cb = {0u, 0u, 0u, 0u};
      const u32 hoff = WS_H + ((mi * 8 + (t & 1)) * 56 + fj * 4 + hw) * 1024 + h * 256 + p16 * 16;
      float ev[8];
      auto epi_a = [&](int q) {
#pragma unroll
        for (int k = 0; k < 8; ++k) ev[k] = relu(acc[q & 1][q >> 1][k]);
      };
      auto epi_b = [&](int q) {
#pragma unroll
        for (int k = 0; k < 8; ++k) ev[k] *= acc[q & 1][q >> 1][8 + k];
      };
      auto epi_c = [&](int q) {
        __builtin_amdgcn_raw_buffer_store_b128(u32x4{pk_bf16(ev[0], ev[1]), pk_bf16(ev[2], ev[3]), pk_bf16(ev[4], ev[5]), pk_bf16(ev[6], ev[7])}, rw,
                                               hoff + (q & 1) * 512, (q >> 1) * (2 * 56 * 1024), 16);
      };
#pragma unroll
      for (int q = 0; q < 8; ++q) {
        const int m = q & 1, n = q >> 1;
        SB();
        if (m == 0) cb[0] = qb[n];
        acc[m][n] = mfma3232(fr[0].aw[m], fr[0].bx[n], acc[m][n]); SB();
        if (q >= 2) { epi_c(q - 2); SB(); }
        acc[m][n] = mfma3232(ca[m], cb, acc[m][n]); SB();
        if (q >= 1) { epi_a(q - 1); SB(); }
        acc[m][n] = mfma3232(fr[1].aw[m], fr[1].bx[n], acc[m][n]); SB();
        if (q >= 1) { epi_b(q - 1); SB(); }
      }
      epi_c(6); SB(); epi_a(7); epi_b(7); epi_c(7);
      *(volatile LAS u32*)(sm + MB + 32 + hw * 4) = tag;
      STAMP(10);
      wd_setup();
      WAITV(4);
      if (lane == 0) __builtin_amdgcn_raw_buffer_store_b32(tag, rw, WS_F2 + (mi * 2) * 2048 + fj * 128 + hw * 4, 0, 16);
      WAITV(1);
      if (lane == 0) __builtin_amdgcn_raw_buffer_store_b32(tag, rw, WS_F2 + (mi * 2 + 1) * 2048 + fj * 128 + hw * 4, 0, 16);
      STAMP(12);
      __builtin_amdgcn_s_setprio(HPRIO);
    } else {
      wd_setup();
      const int un = b >> 1, ua = un >= 7, uc = (b & 1) * 7 + (ua ? un - 7 : un);
      const u32x4 scs = {ua ? ka[8] : ka[6], ua ? ka[9] : ka[7], b < 28 ? 1835008u : 0u, 0x00020000u};
      const u32 vcs = (hw * 4 + t) * 3584 + uc * 256 + p16 * 16;
      u32x4 vv[16];
      f32x4 a0 = z4, a1 = z4;
      u32 sfa, sfb;
      u32 sel_lo = 0x00003f80u, sel_hi = 0x3f800000u;
      PIN(sel_lo); PIN(sel_hi);
      const unsigned long long t_start = __builtin_amdgcn_s_memrealtime();
#pragma unroll
      for (int half = 0; half < 2; ++half) {
        if (half == 1) { while (__builtin_amdgcn_s_memrealtime() - t_start < CS_DELAY) __builtin_amdgcn_s_sleep(4); }
#pragma unroll
        for (int k = 0; k < 16; ++k) ALD4S(vv[k], vcs, scs, (half * 16 + k) * 16 * 3584, "");
        if (half == 0) {
          const int q = 64 * g + 16 * hw + p16, ks = 8 * q + jp, km = (2048 - ks) & 2047, col = (t >> 1) * 256;
          const GAS unsigned short* wa = (t & 1) ? wi : wr;
          asm volatile("global_load_ushort %0, %1, off" : "=v"(sfa) : "v"(wa + ks * DF_ + col) : "memory");
          asm volatile("global_load_ushort %0, %1, off" : "=v"(sfb) : "v"(wa + km * DF_ + col) : "memory");
          tag = load_tag();
        }
        STAMP(23 + 3 * half);
        static_for<0, 4>([&](auto Q_) {
          constexpr int Q = decltype(Q_)::v;
          if (half == 0) WAITV(14 - 4 * Q); else WAITV(12 - 4 * Q);
#pragma unroll
          for (int k = 4 * Q; k < 4 * Q + 4; ++k) {
            PIN(vv[k]);
#pragma unroll
            for (int e = 0; e < 2; ++e) {
              const u32 p = vv[k][e], q = vv[k][2 + e];
              a0[2 * e] = __builtin_amdgcn_fdot2_f32_bf16(__builtin_bit_cast(b16x2, p), __builtin_bit_cast(b16x2, sel_lo), a0[2 * e], false);
              a0[2 * e + 1] = __builtin_amdgcn_fdot2_f32_bf16(__builtin_bit_cast(b16x2, p), __builtin_bit_cast(b16x2, sel_hi), a0[2 * e + 1], false);
              a1[2 * e] = __builtin_amdgcn_fdot2_f32_bf16(__builtin_bit_cast(b16x2, q), __builtin_bit_cast(b16x2, sel_lo), a1[2 * e], false);
              a1[2 * e + 1] = __builtin_amdgcn_fdot2_f32_bf16(__builtin_bit_cast(b16x2, q), __builtin_bit_cast(b16x2, sel_hi), a1[2 * e + 1], false);
            }
          }
        });
        PIN(a0); PIN(a1);
        STAMP(24 + 3 * half);
        if (half == 0) {
          WAITV(0);
          PIN(sfa); PIN(sfb);
          const float wh = 0.5f * ((t & 1) ? bf_lo(sfa) - bf_lo(sfb) : bf_lo(sfa) + bf_lo(sfb));
          const u32 wb = pk_bf16(wh, wh) & 0xffffu;
          auto s2 = __builtin_amdgcn_permlane16_swap(wb, wb, false, false);
          if ((t & 1) == 0) st_sc1_2(rw, WS_SF + jp * 4096 + (t >> 1) * 2048 + (64 * g + 4 * p16 + hw) * 8, u32x2{(u32)s2[0] | ((u32)s2[1] << 16), tag});
          STAMP(25);
        }
      }
      if (b < 28) {
#pragma unroll
        for (int k = 0; k < 4; ++k) { a0[k] = xrow_sum(a0[k]); a1[k] = xrow_sum(a1[k]); }
        const float s0 = t == 0 ? a0[0] : t == 1 ? a0[2] : t == 2 ? a1[0] : a1[2];
        const float s1 = t == 0 ? a0[1] : t == 1 ? a0[3] : t == 2 ? a1[1] : a1[3];
        st_sc1(rw, WS_CS + hw * (2 * F_ * 8) + (ua * 14 + uc) * 1024 + p16 * 64 + t * 16, u32x4{__builtin_bit_cast(u32, s0), tag, __builtin_bit_cast(u32, s1), tag});
      }
      STAMP(3);
    }
    {
      const int off = is_gemm ? MB + 32 : MB + 112;
#pragma nounroll
      for (int spin = 0; spin < SPIN_CAP; ++spin) { if (all_ok(*(volatile LAS u32*)(sm + off + (lane & 3) * 4) == tag)) break; __builtin_amdgcn_s_sleep(1); }
      asm volatile("" ::: "memory");
    }
    __builtin_amdgcn_s_setprio(MTP);
    wd_issue(w, 0, 6); wd_issue(w, 1, 7); wd_issue(w, 2, 8);
    wd_issue(w + 4, 3, 3); wd_issue(w + 4, 4, 4); wd_issue(w + 4, 5, 5);
    {
#pragma nounroll
      for (int spin = 0; spin < SPIN_CAP; ++spin) { if (all_ok(*(volatile LAS u32*)(sm + MB + 112 + hw * 4) == tag)) break; __builtin_amdgcn_s_sleep(1); }
      asm volatile("" ::: "memory");
    }
    wd_issue(w, 5, 2); wd_issue(w, 6, 1); wd_issue(w + 4, 6, 0);
    WAITV(24);
    wd_read(0, 6); wd_read(1, 7); wd_read(2, 8);
    asm volatile("s_waitcnt lgkmcnt(0)" ::: "memory");
    wd_issue(w, 3, 6); wd_issue(w, 4, 7);
    STAMP(13);
  } else if (is_gemm) helper(IC<1>{});
  else helper(IC<0>{});

  wait_h_and_load();
  __builtin_amdgcn_s_setprio(0);
  WAITV(7 * HWIN);
  if (w >= 4 && lane < 2) st_sc1_2(rw, WS_ST2 + b * 64 + hw * 8 + lane * 32, u32x2{stat01, tag});
#pragma unroll
  for (int i = 3; i < 7; ++i) {
    LAS char* p0 = sm + (w < 4 ? wslot(i == 6 ? 1 : i == 5 ? 2 : i + 3) : wslot(i == 6 ? 0 : i));
#pragma unroll
    for (int u = 0; u < 4; ++u) {
      LAS char* p = p0 + (wbase ^ (u << 5));
      u32x2 lo = tr_read(p), hi = tr_read(p + 512);
      wf[i][u] = u32x4{lo[0], lo[1], hi[0], hi[1]};
    }
  }
  STAMP(16);
  const int oc = lane & 7, otok = (w & 1) * 8 + (lane >> 3);
  const int orow = rt * 64 + (((w >> 1) + ct) & 3) * 16 + otok;
  const u32 roff = WS_R + orow * 1024 + (ct * 8 + oc) * 16;
  const u32 soff_ = WS_ST2 + ((orow & 255) * 8 + (orow >> 8)) * 8;
  u32x4 r0; u32x2 sto; bool rok;
#pragma unroll
  for (int tt = 0; tt < 4; ++tt) {
    f32x4 acc[4] = {z4, z4, z4, z4};
#pragma unroll
    for (int ks = 0; ks < 7; ++ks) {
#pragma unroll
      for (int u = 0; u < 4; ++u) acc[u] = mfma32_bf16(wf[ks][u], hf[tt][ks], acc[u]);
      if (tt + HWIN < 4) { SB(); hf[(tt + HWIN) & 3][ks] = ld_h(tt + HWIN, ks); SB(); }
    }
    if (tt == 2) sto = ld_sc1_2(rw, soff_);
    if (tt == 3) { SB(); asm volatile("" : "+v"(sto) :: "memory"); rok = all_ok(sto[1] == tag); r0 = ld_sc1(rw, roff); }
#pragma unroll
    for (int u = 0; u < 4; ++u) *(LAS f32x4*)(sm + (w < 4 ? wslot(tt < 3 ? 6 + tt : 1) : wslot(tt < 3 ? 3 + tt : 0)) + hw * 4096 + u * 1024 + t * 256 + ((p16 ^ (4 * u + (t & 2))) << 4)) = acc[u];
    SB();
  }
  STAMP(17);
  BAR();
  f32x4 sa = z4, sb = z4;
  {
    const int tt = w >> 1;
    const int o0 = (oc >> 1) * 1024 + (oc & 1) * 512 + ((otok ^ (2 * oc)) << 4);
    const int bm = (tt < 3 ? wslot(6) + tt * 16384 : wslot(1)) + o0, bh = (tt < 3 ? wslot(3) + tt * 16384 : wslot(0)) + o0;
#pragma unroll
    for (int pw = 0; pw < 4; ++pw) {
      sa += *(LAS f32x4*)(sm + bm + pw * 4096) + *(LAS f32x4*)(sm + bh + pw * 4096);
      sb += *(LAS f32x4*)(sm + bm + 256 + pw * 4096) + *(LAS f32x4*)(sm + bh + 256 + pw * 4096);
    }
  }
  STAMP(22);
  if (__builtin_expect(!rok, 0)) {
#pragma nounroll
    for (int spin = 0; spin < SPIN_CAP; ++spin) {
      asm volatile("" ::: "memory");
      sto = ld_sc1_2(rw, soff_);
      if (all_ok(sto[1] == tag)) break;
    }
    asm volatile("" ::: "memory");
    r0 = ld_sc1(rw, roff);
  }
  {
    const float rs = __builtin_amdgcn_rcpf(bf_hi(sto[0])), rs2 = rs * rs;
    *(GAS u32x4*)(out + (size_t)orow * 1024 + ct * 128 + oc * 16) =
        u32x4{pk_bf16(__builtin_fmaf(sa[0], rs2, bf_lo(r0[0])), __builtin_fmaf(sa[1], rs2, bf_hi(r0[0]))),
              pk_bf16(__builtin_fmaf(sa[2], rs2, bf_lo(r0[1])), __builtin_fmaf(sa[3], rs2, bf_hi(r0[1]))),
              pk_bf16(__builtin_fmaf(sb[0], rs2, bf_lo(r0[2])), __builtin_fmaf(sb[1], rs2, bf_hi(r0[2]))),
              pk_bf16(__builtin_fmaf(sb[2], rs2, bf_lo(r0[3])), __builtin_fmaf(sb[3], rs2, bf_hi(r0[3])))};
  }
  if (b == 0 && tid == 0) __builtin_amdgcn_raw_buffer_store_b32(tag, rw, WS_EPOCH, 0, 16);
  STAMP(18);
  STAMP_DUMP();
}

#if defined(__HIP_DEVICE_COMPILE__)
#pragma clang attribute pop
#endif

extern "C" void kernel_launch(void* const* d_in, const int* in_sizes, int n_in, void* d_out, int out_size, void* d_ws, size_t ws_size,
                              hipStream_t stream) {
  (void)hipFuncSetAttribute((const void*)fused_kernel, hipFuncAttributeMaxDynamicSharedMemorySize, LDS_BYTES);
  fused_kernel<<<256, 512, LDS_BYTES, stream>>>((const char*)d_in[0], (const unsigned short*)d_in[1], (const unsigned short*)d_in[2], (const char*)d_in[4],
                                                (const char*)d_in[5], (const char*)d_in[6], (char*)d_ws, (char*)d_out);
}
```

```cpp
#include <hip/hip_runtime.h>
#include <stdint.h>
#define LAS __attribute__((address_space(3)))
#define GAS __attribute__((address_space(1)))
#define DI __device__ __forceinline__
typedef unsigned u32;
typedef unsigned u32x2 __attribute__((ext_vector_type(2)));
typedef unsigned u32x4 __attribute__((ext_vector_type(4)));
typedef float f32x4 __attribute__((ext_vector_type(4)));
typedef float f32x16 __attribute__((ext_vector_type(16)));
typedef short s16x4 __attribute__((ext_vector_type(4)));
typedef _Float16 h16x2 __attribute__((ext_vector_type(2)));
typedef _Float16 h16x4 __attribute__((ext_vector_type(4)));
typedef _Float16 h16x8 __attribute__((ext_vector_type(8)));
typedef __bf16 b16x2 __attribute__((ext_vector_type(2)));
typedef __bf16 b16x8 __attribute__((ext_vector_type(8)));
typedef __amdgpu_buffer_rsrc_t rsrc_t;

constexpr int F_ = 1792, DF_ = 257;
constexpr u32 WS_EPOCH = 0;
constexpr u32 WS_ST = 16384;
constexpr u32 WS_CS = WS_ST + 16384;
constexpr u32 WS_U = 262144;
constexpr u32 WS_V = WS_U + 4194304;
constexpr u32 WS_R = WS_V + 4194304;
constexpr u32 WS_ST2 = 229376;
constexpr u32 WS_SF = 196608;
constexpr u32 WS_F2 = WS_R + 2097152;
constexpr u32 WS_H = WS_R + 4194304;
constexpr u32 WS_END = WS_H + 7340032;
constexpr int LDS_BYTES = 163840;
constexpr int G1_STAGE = 49152;
constexpr int MB = 3 * G1_STAGE + 8192 + 1024;
constexpr int FS = 3 * G1_STAGE;
constexpr int SPIN_CAP = 1 << 17;
constexpr int CODE_LINES = 288;
constexpr int CS_DELAY = 430;
constexpr float XS = 1.0f / 16;
constexpr float FSC = (2.0f / (2048.0f * 512.0f)) * 16.0f;

#if defined(__HIP_DEVICE_COMPILE__)
#pragma clang attribute push(__attribute__((target("no-packed-fp32-ops"))), apply_to = function)
#endif
DI u32 pk_f16(float a, float b) { h16x2 p = {(_Float16)a, (_Float16)b}; return __builtin_bit_cast(u32, p); }
DI u32 pk_bf16(float a, float b) { b16x2 p = {(__bf16)a, (__bf16)b}; return __builtin_bit_cast(u32, p); }
DI float bf_lo(u32 v) { return __builtin_bit_cast(float, v << 16); }
DI float bf_hi(u32 v) { return __builtin_bit_cast(float, v & 0xffff0000u); }
DI float rnd_bf(float a) { return bf_lo(pk_bf16(a, a)); }
DI float relu(float a) { int i = __builtin_bit_cast(int, a); return __builtin_bit_cast(float, i > 0 ? i : 0); }
DI float cosr(float rev) { return __builtin_amdgcn_cosf(rev); }
DI float sinr(float rev) { return __builtin_amdgcn_sinf(rev); }
DI f32x4 mfma16_f16(u32x2 a, u32x2 b, f32x4 c) { return __builtin_amdgcn_mfma_f32_16x16x16f16(__builtin_bit_cast(h16x4, a), __builtin_bit_cast(h16x4, b), c, 0, 0, 0); }
DI f32x4 mfma16_bf16(u32x2 a, u32x2 b, f32x4 c) { return __builtin_amdgcn_mfma_f32_16x16x16bf16_1k(__builtin_bit_cast(s16x4, a), __builtin_bit_cast(s16x4, b), c, 0, 0, 0); }
DI f32x4 mfma32_f16(u32x4 a, u32x4 b, f32x4 c) { return __builtin_amdgcn_mfma_f32_16x16x32_f16(__builtin_bit_cast(h16x8, a), __builtin_bit_cast(h16x8, b), c, 0, 0, 0); }
DI f32x4 mfma32_bf16(u32x4 a, u32x4 b, f32x4 c) { return __builtin_amdgcn_mfma_f32_16x16x32_bf16(__builtin_bit_cast(b16x8, a), __builtin_bit_cast(b16x8, b), c, 0, 0, 0); }
DI f32x16 mfma3232(u32x4 a, u32x4 b, f32x16 c) { return __builtin_amdgcn_mfma_f32_32x32x16_bf16(__builtin_bit_cast(b16x8, a), __builtin_bit_cast(b16x8, b), c, 0, 0, 0); }
DI u32x2 tr_read(LAS char* p) { return __builtin_bit_cast(u32x2, __builtin_amdgcn_ds_read_tr16_b64_v4i16((LAS s16x4*)p)); }
#define SB() __builtin_amdgcn_sched_barrier(0)
#define BAR() asm volatile("s_waitcnt lgkmcnt(0)\n\ts_barrier" ::: "memory")
#define WAITV(n) asm volatile("s_waitcnt vmcnt(%0)" :: "i"(n) : "memory")
#define PIN(x) asm volatile("" : "+v"(x))
DI void glds16(u32 ldsbase, int imm, u32 voff, u32x4 srd, u32 soff) {
  asm volatile("s_add_u32 m0, %0, %1\n\ts_nop 0\n\tbuffer_load_dwordx4 %2, %3, %4 offen lds" :: "s"(ldsbase), "i"(imm), "v"(voff), "s"(srd), "s"(soff) : "memory", "scc");
}
#define ALD2(dst, voff, srd, IMM, FL) asm volatile("buffer_load_dwordx2 %0, %1, %2, 0 offen offset:" #IMM FL : "=v"(dst) : "v"(voff), "s"(srd) : "memory")
DI u32x4 ld_sc1(rsrc_t r, u32 off) { return __builtin_amdgcn_raw_buffer_load_b128(r, off, 0, 16); }
DI u32x2 ld_sc1_2(rsrc_t r, u32 off) { return __builtin_amdgcn_raw_buffer_load_b64(r, off, 0, 16); }
DI void st_sc1(rsrc_t r, u32 off, u32x4 v) { __builtin_amdgcn_raw_buffer_store_b128(v, r, off, 0, 16); }
DI void st_sc1_2(rsrc_t r, u32 off, u32x2 v) { __builtin_amdgcn_raw_buffer_store_b64(v, r, off, 0, 16); }
DI bool all_ok(bool ok) { return __builtin_amdgcn_ballot_w64(!ok) == 0; }
DI bool tags_ok(u32x4 a, u32 tag) { return (a[1] == tag) & (a[3] == tag); }

template <int CTRL> DI float dpp_add(float v) {
  int o = __builtin_amdgcn_update_dpp(0, __builtin_bit_cast(int, v), CTRL, 0xf, 0xf, true);
  return v + __builtin_bit_cast(float, o);
}
DI float row_sum(float v) { v = dpp_add<0xB1>(v); v = dpp_add<0x4E>(v); v = dpp_add<0x141>(v); return dpp_add<0x140>(v); }
DI float xrow_sum(float v) {
  u32 u = __builtin_bit_cast(u32, v);
  auto a = __builtin_amdgcn_permlane16_swap(u, u, false, false);
  v = __builtin_bit_cast(float, (u32)a[0]) + __builtin_bit_cast(float, (u32)a[1]);
  u = __builtin_bit_cast(u32, v);
  auto b = __builtin_amdgcn_permlane32_swap(u, u, false, false);
  return __builtin_bit_cast(float, (u32)b[0]) + __builtin_bit_cast(float, (u32)b[1]);
}
template <bool NEG, bool BF> DI u32x4 cmul_pack(f32x4 re, f32x4 im, f32x4 c, f32x4 s) {
  f32x4 a = NEG ? re * c + im * s : re * c - im * s;
  f32x4 b = NEG ? im * c - re * s : im * c + re * s;
  u32x4 o;
#pragma unroll
  for (int r = 0; r < 4; ++r) o[r] = BF ? pk_bf16(a[r], b[r]) : pk_f16(a[r], b[r]);
  return o;
}
DI u32 rot16(u32 v) { return __builtin_amdgcn_alignbit(v, v, 16); }
template <bool BF> DI u32x4 pack4(f32x4 a, f32x4 b) {
  u32x4 o;
#pragma unroll
  for (int r = 0; r < 4; ++r) o[r] = BF ? pk_bf16(a[r], b[r]) : pk_f16(a[r], b[r]);
  return o;
}
DI void glds_a(u32 ldsbase, int imm, u32 v0, u32 v1, u32x4 srd, u32 soff) {
  asm volatile("s_add_u32 m0, %0, %1\n\ts_nop 0\n\tbuffer_load_dwordx4 %2, %4, %5 offen lds\n\tbuffer_load_dwordx4 %3, %4, %5 offen offset:1024 lds"
               :: "s"(ldsbase), "i"(imm), "v"(v0), "v"(v1), "s"(srd), "s"(soff) : "memory", "scc");
}
DI void glds_b(u32 v2, u32 v3, u32x4 srd, u32 soff) {
  asm volatile("buffer_load_dwordx4 %0, %2, %3 offen offset:2048 lds\n\tbuffer_load_dwordx4 %1, %2, %3 offen offset:3072 lds"
               :: "v"(v2), "v"(v3), "s"(srd), "s"(soff) : "memory");
}
DI void glds_c(int j, u32 v, u32x4 srd, u32 soff) {
  asm volatile("buffer_load_dwordx4 %0, %1, %2 offen offset:%3 lds" :: "v"(v), "s"(srd), "s"(soff), "i"(j * 1024) : "memory");
}
DI u32x4 cmulc_f16(f32x4 re, f32x4 im, u32x4 tw) {
  u32 z0 = pk_f16(re[0], im[0]), z1 = pk_f16(re[1], im[1]), z2 = pk_f16(re[2], im[2]), z3 = pk_f16(re[3], im[3]);
  u32 o0, o1, o2, o3;
  const u32 t0 = tw[0], t1 = tw[1], t2 = tw[2], t3 = tw[3];
#define PKM " op_sel_hi:[1,0]\n\t"
#define PKF " op_sel:[1,1,0] op_sel_hi:[0,1,1] neg_hi:[0,1,0]\n\t"
  asm("v_pk_mul_f16 %0, %4, %8" PKM "v_pk_mul_f16 %1, %5, %9" PKM "v_pk_mul_f16 %2, %6, %10" PKM "v_pk_mul_f16 %3, %7, %11" PKM
      "v_pk_fma_f16 %0, %4, %8, %0" PKF "v_pk_fma_f16 %1, %5, %9, %1" PKF "v_pk_fma_f16 %2, %6, %10, %2" PKF "v_pk_fma_f16 %3, %7, %11, %3" PKF "s_nop 1"
      : "=&v"(o0), "=&v"(o1), "=&v"(o2), "=&v"(o3) : "v"(z0), "v"(z1), "v"(z2), "v"(z3), "v"(t0), "v"(t1), "v"(t2), "v"(t3));
  return u32x4{o0, o1, o2, o3};
}
template <int N> struct IC { static constexpr int v = N; };
template <int A, int B, class Fn> DI void static_for(Fn&& f) { if constexpr (A < B) { f(IC<A>{}); static_for<A + 1, B>(f); } }

struct Frag { u32x4 bx[4], aw[2]; };
#define ALD4S(dst, voff, srd, SOFF, FL) asm volatile("buffer_load_dwordx4 %0, %1, %2, %3 offen" FL : "=v"(dst) : "v"(voff), "s"(srd), "s"(SOFF) : "memory")
#define ALD2S(dst, voff, srd, SOFF, FL) asm volatile("buffer_load_dwordx2 %0, %1, %2, %3 offen" FL : "=v"(dst) : "v"(voff), "s"(srd), "s"(SOFF) : "memory")
#define RAWBAR() do { asm volatile("s_barrier" ::: "memory"); } while (0)

typedef unsigned u32x16 __attribute__((ext_vector_type(16)));
__global__ __launch_bounds__(512, 2) void parallel_sum__spectral_gfnet__glu_gated_reglu__prompt_kernel(const char* a_x, const unsigned short* a_wr, const unsigned short* a_wi, const char* a_wg,
                                                       const char* a_wu, const char* a_wd, char* a_ws, char* a_out) {
  extern __shared__ __attribute__((aligned(16))) char smem[];
  LAS char* sm = (LAS char*)smem;
  u32x16 ka;
  asm volatile("s_load_dwordx16 %0, %1, 0x0" : "=s"(ka) : "s"(__builtin_amdgcn_kernarg_segment_ptr()) : "memory");
  const int b = blockIdx.x, tid = threadIdx.x, lane = tid & 63, w = __builtin_amdgcn_readfirstlane(tid >> 6);
  const int hw = w & 3, p16 = lane & 15, t = lane >> 4;
  const u32 lds0 = (u32)(uintptr_t)sm;
  {
    uint64_t pc; asm volatile("s_getpc_b64 %0" : "=s"(pc));
    asm volatile("s_cmp_gt_u32 %0, 7\n\ts_cbranch_scc1 1f\n\ts_mov_b32 m0, %1\n\ts_nop 0\n\tbuffer_load_dword %2, %3, 0 offen lds\n1:"
                 :: "s"(b), "s"(lds0), "v"(tid * 128), "s"(u32x4{(u32)pc, (u32)(pc >> 32), CODE_LINES * 128, 0x00020000u}) : "memory", "scc");
  }
  if (tid < 64) *(LAS u32*)(sm + MB + tid * 4) = 0;
  const bool is_gemm = b >= 32;
  const int xc = b & 7, r_ = (b - 32) >> 3;
  const int mi = (xc >> 1) * 4 + (r_ & 3), fj = (xc & 1) * 7 + (r_ >> 2);
  const int rot = ((r_ & 3) + (r_ >> 2)) & 1;
  const u32 lw = lds0 + hw * 4096;
  u32 vx[4], vg[4], vu[4];
#pragma unroll
  for (int j = 0; j < 4; ++j) {
    vx[j] = (mi * 128 + hw * 32 + j * 7 + (lane >> 3)) * 1024 + (((lane & 7) ^ ((4 * j + t) & 7)) << 4);
    vg[j] = (hw * 16 + j * 4 + t) * 3584 - j * 1024 + fj * 256 + ((p16 ^ (2 * t)) << 4);
    vu[j] = vg[j] ^ 128;
    PIN(vx[j]); PIN(vg[j]); PIN(vu[j]);
  }
  u32 vrow = (b + 256 * hw) * 1024 + lane * 16;
  asm volatile("s_cmp_lt_u32 %0, 4\n\ts_cbranch_scc1 1f\n\ts_setprio 2\n1:" :: "s"(w) : "scc");
  if (w < 4) vrow = 0x7ff00000u;
  const u32 lrow = w < 4 ? lw : lds0 + FS + hw * 1024;
  PIN(vrow);
  SB();
  asm volatile("s_waitcnt lgkmcnt(0)\n\ts_barrier" : "+s"(ka) :: "memory");
#define KARG(i) (((uint64_t)ka[2 * (i) + 1] << 32) | ka[2 * (i)])
  const u32x4 sx = {ka[0], ka[1], 2097152u, 0x00020000u}, sg = {ka[6], ka[7], 1835008u, 0x00020000u}, su = {ka[8], ka[9], 1835008u, 0x00020000u};
  auto g1_issue = [&](int st, int k) {
    const int lb = (st % 3) * G1_STAGE + (k >> 1) * 16384, sk = st ^ rot;
    if (k == 0) glds_a(lw, lb, vx[0], vx[1], sx, sk * 128);
    if (k == 1) glds_b(vx[2], vx[3], sx, sk * 128);
    if (k == 2) glds_a(lw, lb, vg[0], vg[1], sg, sk * 64 * 3584);
    if (k == 3) glds_b(vg[2], vg[3], sg, sk * 64 * 3584);
    if (k == 4) glds_a(lw, lb, vu[0], vu[1], su, sk * 64 * 3584);
    if (k == 5) glds_b(vu[2], vu[3], su, sk * 64 * 3584);
  };
  glds16(lrow, 0, vrow, sx, 0);
  glds16(lrow + (w < 4 ? 0 : 4096), 0, vrow, sx, 1048576);
  if (w < 4 && is_gemm) {
#pragma unroll
    for (int k = 0; k < 6; ++k) g1_issue(0, k);
  }
  SB();
  const f32x4 z4 = {0.f, 0.f, 0.f, 0.f};
  const GAS unsigned short* wr = (const GAS unsigned short*)KARG(1); const GAS unsigned short* wi = (const GAS unsigned short*)KARG(2);
  GAS char* ws = (GAS char*)KARG(6); GAS char* out = (GAS char*)KARG(7);
  auto load_tag = [&]() {
    u32 epoch;
    asm volatile("s_load_dword %0, %1, 0x0\n\ts_waitcnt lgkmcnt(0)" : "=s"(epoch) : "s"(ws) : "memory");
    return epoch * 0x9E3779B1u + 0x7F4A7C15u;
  };
  const rsrc_t rw = __builtin_amdgcn_make_buffer_rsrc((void*)ws, 0, WS_END, 0x00020000);
  const int jp = b & 7, g = b >> 3;
  const int rt = (b & 7) * 4 + ((b >> 3) & 3), ct = b >> 5;
  auto wslot = [&](int s) { return (s < 3 ? 2 * G1_STAGE : s < 6 ? -3 * 16384 : G1_STAGE - 6 * 16384) + s * 16384; };
  u32x4 wf[7][4];
  const int wbase = hw * 4096 + t * 1024 + (p16 >> 2) * 128 + (p16 & 3) * 8 + ((((p16 >> 3) | ((t & 1) << 1))) << 5);
  auto wd_read = [&](int ks, int s) {
#pragma unroll
    for (int u = 0; u < 4; ++u) {
      LAS char* p = sm + wslot(s) + (wbase ^ (u << 5));
      u32x2 lo = tr_read(p), hi = tr_read(p + 512);
      wf[ks][u] = u32x4{lo[0], lo[1], hi[0], hi[1]};
    }
  };
  u32 tag;
  auto ksof = [&](int i) { int k = i + 2 * (rt & 3); return k >= 7 ? k - 7 : k; };
  const u32 hs = WS_H + ((rt * 4) * 56 + w * 7) * 1024 + lane * 16;
  auto ld_h = [&](int j, int i) { return __builtin_amdgcn_raw_buffer_load_b128(rw, hs, (((j + ct) & 3) * 56 + ksof(i)) * 1024, 16); };
  u32x4 hf[4][7];
  const u32x4 sd_ = {ka[10], ka[11], 1835008u, 0x00020000u};
  u32 vwd[4];
  auto wd_setup = [&]() {
#pragma unroll
    for (int j = 0; j < 4; ++j) { vwd[j] = (j * 7 + (lane >> 3)) * 1024 + ct * 128 + (((lane & 7) ^ (((lane >> 4) & 1) << 1) ^ ((j & 1) << 2)) << 4); PIN(vwd[j]); }
  };
  auto wd_issue = [&](int kw, int i, int s) {
    const u32 so = kw * (224 * 1024) + ksof(i) * 32768;
    SB(); glds_a(lw, wslot(s), vwd[0], vwd[1], sd_, so); glds_b(vwd[2], vwd[3], sd_, so); SB();
  };
  auto wait_h_and_load = [&]() {
    if (w < 4) {
      const int kb = hw * 7 + (lane < 7 ? lane : lane < 14 ? lane + 21 : 0);
      const u32 foff = WS_F2 + rt * 2048 + (kb >> 2) * 128 + (kb & 3) * 4;
#pragma nounroll
      for (int spin = 0; spin < SPIN_CAP; ++spin) {
        asm volatile("" ::: "memory");
        u32 fl = __builtin_amdgcn_raw_buffer_load_b32(rw, foff, 0, 16);
        if (all_ok(fl == tag)) break;
        __builtin_amdgcn_s_sleep(2);
      }
      WAITV(0);
      *(volatile LAS u32*)(sm + MB + 96 + hw * 4) = tag;
    } else {
#pragma nounroll
      for (int spin = 0; spin < SPIN_CAP; ++spin) { if (all_ok(*(volatile LAS u32*)(sm + MB + 96 + hw * 4) == tag)) break; __builtin_amdgcn_s_sleep(1); }
    }
    __builtin_amdgcn_fence(__ATOMIC_ACQUIRE, "wavefront");
    asm volatile("" ::: "memory");
#pragma unroll
    for (int i = 0; i < 7; ++i) hf[0][i] = ld_h(0, i);
    SB();
  };
  u32 stat01 = 0;
  const float pf = (float)p16, tf = (float)t;
  auto helper = [&](auto G_) {
    constexpr bool G = decltype(G_)::v;
    const int ht = tid & 255;
    const u32x4 sw = {ka[12], ka[13], WS_END, 0x00020000u};
    const u32x4 sfr_ = {ka[2], ka[3], 2048 * DF_ * 2, 0x00020000u}, sfi_ = {ka[4], ka[5], 2048 * DF_ * 2, 0x00020000u};
    SB();
    auto g1_one = [&](int st, int p) {
      const int lb = (st % 3) * G1_STAGE + (p >> 2) * 16384, sk = st ^ rot, j = p & 3;
      const u32 v = p < 4 ? vx[j] : p < 8 ? vg[j] : vu[j];
      const u32x4 sr = p < 4 ? sx : p < 8 ? sg : su;
      const u32 so = p < 4 ? sk * 128 : sk * 64 * 3584;
      if (j == 0) glds16(lw, lb, v, sr, so); else glds_c(j, v, sr, so);
    };
#define D1(st, p) do { if (G) { SB(); g1_one(st, p); SB(); } } while (0)
#define GWAIT(n) do { if (G) WAITV(n); } while (0)
#define WAITG(n) do { if (G) WAITV(n); else WAITV(0); } while (0)
#define GBAR() do { if (G) RAWBAR(); } while (0)
    auto hsync = [&](int off) {
      asm volatile("s_waitcnt lgkmcnt(0)" ::: "memory");
      *(volatile LAS u32*)(sm + off + hw * 4) = tag;
#pragma nounroll
      for (int spin = 0; spin < SPIN_CAP; ++spin) { if (all_ok(*(volatile LAS u32*)(sm + off + (lane & 3) * 4) == tag)) break; }
      asm volatile("" ::: "memory");
    };
#define XBAR(n) do { if (G) BAR(); else hsync(G1_STAGE + (n) * 16); } while (0)
    u32x4 b2re[2], b2im[2], tw[2], ac2re, ac2im, bc3re, bc3im, qa8, qa8i, bc1re, bc1im, ac4re, ac4im, t1, bi1re[2], bi1im[2];
    u32x2 b1re, b1im;
    f32x4 t2c, t2s, t4c[2], t4s[2];
    {
      const float d5 = pf * (1.0f / 512), a5 = 4.0f * tf * d5;
#pragma unroll
      for (int r = 0; r < 4; ++r) {
        const float th = a5 + r * d5, c = cosr(th * 32.0f), s = sinr(th * 32.0f);
        ac2re[r] = pk_f16(c, s); ac2im[r] = rot16(ac2re[r]) ^ 0x8000u;
        bc3im[r] = pk_bf16(s, c); bc3re[r] = rot16(bc3im[r]) ^ 0x80000000u;
#pragma unroll
        for (int hh = 0; hh < 2; ++hh) {
          const float tq = th + hh * (16.0f * d5);
          tw[hh][r] = pk_f16(cosr(tq), sinr(tq));
          b2re[hh][r] = pk_f16(cosr(tq * 16.0f), sinr(tq * 16.0f)); b2im[hh][r] = rot16(b2re[hh][r]) ^ 0x8000u;
        }
      }
      b1re = u32x2{__builtin_amdgcn_perm(ac2re[1], ac2re[0], 0x05040100u), __builtin_amdgcn_perm(ac2re[3], ac2re[2], 0x05040100u)};
      b1im = u32x2{__builtin_amdgcn_perm(ac2im[1], ac2im[0], 0x05040100u), __builtin_amdgcn_perm(ac2im[3], ac2im[2], 0x05040100u)};
      const int po = p16 & 1, j8 = p16 >> 1;
      const float j8f = (float)j8 * 0.125f;
      const float ra = 2.0f * tf * j8f, rb = ra + j8f;
      const float ca = cosr(ra), sa = sinr(ra), cb = cosr(rb), sb = sinr(rb);
      const u32 fa0 = pk_f16(ca, sa), fb0 = pk_f16(cb, sb);
      const u32 fa = po ? (rot16(fa0) ^ 0x8000u) : fa0, fb = po ? (rot16(fb0) ^ 0x8000u) : fb0;
      const u32 sel = (j8 & 3) == 0 ? 0x03020100u : (j8 < 4 ? 0x0c0c0100u : 0x01000c0cu);
      qa8 = u32x4{fa, fb, __builtin_amdgcn_perm(0u, fa, sel), __builtin_amdgcn_perm(0u, fb, sel)};
      const u32 ia = pk_bf16(sa, ca), ib = pk_bf16(sb, cb), pa = rot16(ia) ^ 0x80000000u, pb = rot16(ib) ^ 0x80000000u;
      const bool on = (po == 0) ? (t < 2) : (t >= 2);
      const u32 hc = pk_bf16(0.5f * ca, 0.f);
      qa8i = u32x4{po ? ia : pa, po ? ib : pb, (t & 1) == 0 ? (po ? hc << 16 : hc) : (on ? pa : 0u), on ? pb : 0u};
      PIN(ac2re); PIN(ac2im); PIN(bc3re); PIN(bc3im); PIN(tw[0]); PIN(tw[1]); PIN(b2re[0]); PIN(b2re[1]); PIN(b2im[0]); PIN(b2im[1]); PIN(b1re); PIN(b1im); PIN(qa8); PIN(qa8i);
    }
    SB();
    tag = load_tag();
    LAS char* const img = sm + FS + hw * 1024;
    u32 stat[2];
    u32x4 xr[2];
    WAITV(0);
    {
      const u32 vo = (8 * b + 2 * hw) * DF_ * 2 + (lane >> 5) * (DF_ * 2 + 2) + (lane & 31) * 16;
      glds16(lds0 + hw * 1024, FS + 8192, vo, sfr_, 0); glds16(lds0 + hw * 1024, FS + 12288, vo, sfi_, 0);
    }
    auto tpat = [&](int q, int u) { const u32 r = tag >> (1 + 7 * q + 3 * u); return (tag & 1u) | ((r & 3u) << 1) | ((r & 28u) << 14); };
    {
      float v[2][8], mean[2], var[2];
#pragma unroll
      for (int a = 0; a < 2; ++a) {
        const u32x4 xv = *(LAS u32x4*)(img + a * 4096 + (4 * p16 + t) * 16);
        xr[a] = xv;
#pragma unroll
        for (int k = 0; k < 4; ++k) { v[a][2 * k] = bf_lo(xv[k]); v[a][2 * k + 1] = bf_hi(xv[k]); }
      }
#pragma unroll
      for (int a = 0; a < 2; ++a) mean[a] = xrow_sum(row_sum(((v[a][0] + v[a][1]) + (v[a][2] + v[a][3])) + ((v[a][4] + v[a][5]) + (v[a][6] + v[a][7])))) * (1.0f / 512);
#pragma unroll
      for (int a = 0; a < 2; ++a) {
        float q0 = 0.f, q1 = 0.f;
#pragma unroll
        for (int k = 0; k < 4; ++k) { float d = v[a][k] - mean[a], e = v[a][k + 4] - mean[a]; q0 += d * d; q1 += e * e; }
        var[a] = xrow_sum(row_sum(q0 + q1)) * (1.0f / 512);
      }
#pragma unroll
      for (int a = 0; a < 2; ++a) {
        stat[a] = pk_bf16(mean[a], __builtin_amdgcn_sqrtf(rnd_bf(rnd_bf(var[a]) + 1e-5f)));
        const float sc = __builtin_amdgcn_rcpf(bf_hi(stat[a])) * XS, of = -bf_lo(stat[a]) * sc;
        u32x4 xh;
#pragma unroll
        for (int k = 0; k < 4; ++k) xh[k] = pk_f16(__builtin_fmaf(v[a][2 * k], sc, of), __builtin_fmaf(v[a][2 * k + 1], sc, of));
        *(LAS u32x4*)(img + a * 4096 + (((4 * p16 + t) ^ ((p16 >> 1) & 2)) << 4)) = xh;
      }
      asm volatile("" ::: "memory");
      u32x2 tra[2][2];
#pragma unroll
      for (int a = 0; a < 2; ++a)
#pragma unroll
        for (int hh = 0; hh < 2; ++hh) tra[a][hh] = tr_read(img + a * 4096 + (4 * t + (p16 >> 2)) * 64 + ((hh ^ (t & 1)) << 5) + (p16 & 3) * 8);
      D1(2, 0); D1(2, 1);
      f32x4 d1re[2][2], d1im[2][2], d2re[2], d2im[2];
#pragma unroll
      for (int a = 0; a < 2; ++a) {
#pragma unroll
        for (int hh = 0; hh < 2; ++hh) { d1re[a][hh] = mfma16_f16(tra[a][hh], b1re, z4); d1im[a][hh] = mfma16_f16(tra[a][hh], b1im, z4); }
        D1(2, 2 + a);
      }
#pragma unroll
      for (int a = 0; a < 2; ++a)
#pragma unroll
        for (int hh = 0; hh < 2; ++hh) {
          u32x4 ta = cmulc_f16(d1re[a][hh], d1im[a][hh], tw[hh]);
          d2re[a] = mfma32_f16(ta, b2re[hh], hh ? d2re[a] : z4); d2im[a] = mfma32_f16(ta, b2im[hh], hh ? d2im[a] : z4);
          D1(2, 4 + 2 * a + hh);
        }
      float ny[2];
#pragma unroll
      for (int a = 0; a < 2; ++a) { f32x4 q = d1re[a][0] + d1re[a][1]; ny[a] = xrow_sum((q[0] - q[1]) + (q[2] - q[3])); D1(2, 8 + a); }
      int e = 4 * p16 + t; e ^= (e >> 3) & 3;
#pragma unroll
      for (int a = 0; a < 2; ++a) {
        if (lane == 0) d2im[a][0] = ny[a];
        *(LAS u32x4*)(img + a * 4096 + e * 16) = pack4<false>(d2re[a], d2im[a]);
        D1(2, 10 + a);
      }
    }
    WAITG(12);
    XBAR(0);
    D1(3, 0);
    u32x4 uo[2][2];
    {
      u32x2 e0[2], e1[2];
#pragma unroll
      for (int k = 0; k < 2; ++k) {
        int e = 16 * hw + 8 * k + (p16 >> 1); e ^= (e >> 3) & 3;
        e0[k] = *(LAS u32x2*)(sm + FS + (2 * t) * 1024 + e * 16 + (p16 & 1) * 8);
        e1[k] = *(LAS u32x2*)(sm + FS + (2 * t + 1) * 1024 + e * 16 + (p16 & 1) * 8);
      }
      D1(3, 1);
      u32 wl[2][2][2], wh[2][2][2];
      {
        LAS char* fp = sm + FS + 8192 + t * 1024 + hw * 128 + p16 * 4;
#pragma unroll
        for (int hf = 0; hf < 2; ++hf)
#pragma unroll
          for (int k = 0; k < 2; ++k) {
#pragma unroll
            for (int u = 0; u < 2; ++u) {
              LAS char* q = fp + hf * 510 + k * 64 + u * 2;
              wl[hf][k][u] = *(LAS unsigned short*)q; wh[hf][k][u] = *(LAS unsigned short*)(q + 4096);
            }
            if (k) D1(3, 2 + hf);
          }
      }
      f32x4 d8[4];
#pragma unroll
      for (int u = 0; u < 4; ++u) d8[u] = mfma16_f16(u32x2{qa8[0], qa8[1]}, u32x2{e0[u >> 1][u & 1], e1[u >> 1][u & 1]}, z4);
      f32x4 d8s = mfma16_f16(u32x2{qa8[2], qa8[3]}, u32x2{e0[0][0], e1[0][0]}, z4);
      D1(3, 4);
      u32 p0 = tpat(p16 & 3, 0), p1 = tpat(p16 & 3, 1);
      PIN(p0); PIN(p1);
      D1(3, 5);
      if ((hw | p16) == 0) d8[0] = d8s;
      D1(3, 6); D1(3, 7);
#pragma unroll
      for (int hf = 0; hf < 2; ++hf)
#pragma unroll
        for (int k = 0; k < 2; ++k) {
          uo[hf][k] = u32x4{(pk_f16(d8[2 * k][2 * hf], d8[2 * k][2 * hf + 1]) & 0xfff8fff8u) | p0, wl[hf][k][0] | (wh[hf][k][0] << 16),
                            (pk_f16(d8[2 * k + 1][2 * hf], d8[2 * k + 1][2 * hf + 1]) & 0xfff8fff8u) | p1, wl[hf][k][1] | (wh[hf][k][1] << 16)};
          PIN(uo[hf][k]);
          D1(3, 8 + 2 * hf + k);
        }
    }
    {
      const u32 ub = WS_U + b * 16384 + (2 * t) * 2048 + hw * 512 + p16 * 16;
#pragma unroll
      for (int hf = 0; hf < 2; ++hf)
#pragma unroll
        for (int k = 0; k < 2; ++k) st_sc1(rw, ub + hf * 2048 + k * 256, uo[hf][k]);
      if (lane < 2) st_sc1_2(rw, WS_ST + b * 64 + hw * 8 + lane * 32, u32x2{lane ? stat[1] : stat[0], tag});
    }
    GWAIT(12 + 5);
    GBAR();
    {
      const float jf = (float)jp;
      const float d1 = (8.0f * pf + jf) * (1.0f / 2048), a1 = 4.0f * tf * d1, d2 = pf * (1.0f / 256), a2 = (32.0f * tf + jf) * (pf * (1.0f / 2048));
#pragma unroll
      for (int r = 0; r < 4; ++r) {
        D1(4, 3 * r);
        const float n1 = a1 + r * d1, n2 = a2 + r * d2;
        u32 q0 = pk_f16(cosr(n1 * 16.0f), sinr(n1 * 16.0f)), q1 = rot16(q0) ^ 0x8000u, q2 = pk_f16(cosr(n1), sinr(n1));
        PIN(q0); PIN(q1); PIN(q2);
        bc1re[r] = q0; bc1im[r] = q1; t1[r] = q2;
        D1(4, 3 * r + 1);
        u32 q3 = pk_bf16(sinr(n2 * 16.0f), cosr(n2 * 16.0f)), q4 = rot16(q3) ^ 0x80000000u;
        PIN(q3); PIN(q4);
        ac4im[r] = q3; ac4re[r] = q4;
        D1(4, 3 * r + 2);
        float q5 = cosr(n2), q6 = sinr(n2);
        PIN(q5); PIN(q6);
        t2c[r] = q5; t2s[r] = q6;
      }
      PIN(bc1re); PIN(bc1im); PIN(ac4re); PIN(ac4im); PIN(t1); PIN(t2c); PIN(t2s);
    }
    {
      const u32 vo = b * 7168 + hw * 2048 + lane * 16;
      glds_a(lds0 + hw * 2048, G ? FS : 0, vo, (hw < 3 ? vo : 0x7fffffffu), sd_, 0);
    }
    GWAIT(5 + 12 + 2);
    XBAR(1);
#pragma unroll
    for (int hh = 0; hh < 2; ++hh) {
      const float n2 = (float)(8 * (p16 >> 2) + (p16 & 3)) + 4.0f * hh;
#pragma unroll
      for (int m = 0; m < 4; ++m) {
        D1(5, 6 * hh + (3 * m) / 2);
        float rev = n2 * ((tf + 4.0f * m) * (1.0f / 32));
        u32 q0 = pk_bf16(sinr(rev), cosr(rev)), q1 = rot16(q0) ^ 0x80000000u;
        PIN(q0); PIN(q1);
        bi1im[hh][m] = q0; bi1re[hh][m] = q1;
        if (m & 1) D1(5, 6 * hh + (3 * m) / 2 + 1);
        float rev2 = n2 * ((4.0f * tf + m) * (1.0f / 512));
        float q2 = cosr(rev2), q3 = sinr(rev2);
        PIN(q2); PIN(q3);
        t4c[hh][m] = q2; t4s[hh][m] = q3;
      }
      PIN(bi1re[hh]); PIN(bi1im[hh]); PIN(t4c[hh]); PIN(t4s[hh]);
    }
    u32x4 ug[4];
    const int uq = ht & 3;
    const int ui = (((ht >> 2) & 3) << 4) + (ht >> 4);
    const u32 uoff = WS_U + ui * 16384 + jp * 2048 + g * 64 + uq * 16;
    auto caddr = [&](int i) { return (LAS char*)(sm + FS + uq * 2048 + ((((i & 192) + 4 * (i & 15) + ((i >> 4) & 3)) * 4) ^ (uq << 5))); };
    if (!G) {
#pragma nounroll
      for (int spin = 0; spin < SPIN_CAP; ++spin) {
        asm volatile("" ::: "memory");
        u32x2 q = ld_sc1_2(rw, WS_ST + (b + 224) * 64 + hw * 8);
        if (all_ok(q[1] == tag)) break;
        __builtin_amdgcn_s_sleep(8);
      }
      __builtin_amdgcn_s_sleep(20);
    }
#pragma unroll
    for (int k = 0; k < 4; ++k) ALD4S(ug[k], uoff, sw, k * 64 * 16384, " sc1");
    GWAIT(12 + 4);
    GBAR();
#pragma unroll
    for (int k = 0; k < 8; ++k) D1(6, k);
    WAITG(8);
    PIN(ug[0]); PIN(ug[1]); PIN(ug[2]); PIN(ug[3]);
    const u32 c0 = tpat(uq, 0), c1 = tpat(uq, 1);
#define CHK1 all_ok(((((ug[0][0] ^ c0) | (ug[1][0] ^ c0) | (ug[2][0] ^ c0) | (ug[3][0] ^ c0) | (ug[0][2] ^ c1) | (ug[1][2] ^ c1) | (ug[2][2] ^ c1) | (ug[3][2] ^ c1)) & 0x00070007u) == 0))
    if (__builtin_expect(!CHK1, 0)) {
#pragma nounroll
      for (int spin = 0; spin < SPIN_CAP; ++spin) {
        asm volatile("" ::: "memory");
#pragma unroll
        for (int k = 0; k < 4; ++k) ug[k] = ld_sc1(rw, uoff + k * 64 * 16384);
        if (CHK1) break;
      }
    }
    {
      LAS char* cp = caddr(ui);
      const u32 x0 = c0 ^ 0x00040004u, x1 = c1 ^ 0x00040004u;
#pragma unroll
      for (int k = 0; k < 4; ++k) {
        *(LAS u32*)(cp + k * 256) = ug[k][0] ^ x0; *(LAS u32*)(cp + k * 256 + 1024) = ug[k][2] ^ x1;
        *(LAS u32*)(cp + k * 256 + 8192) = ug[k][1]; *(LAS u32*)(cp + k * 256 + 9216) = ug[k][3];
        D1(6, 8 + k);
      }
    }
    u32x4 fsp[2];
    const bool special = !G && b < 8 && hw == 0;
    if (special) {
#pragma nounroll
      for (int spin = 0; spin < SPIN_CAP; ++spin) {
        asm volatile("" ::: "memory");
        bool ok = true;
#pragma unroll
        for (int c = 0; c < 2; ++c) {
          const u32x4 fa = ld_sc1(rw, WS_SF + jp * 4096 + c * 2048 + lane * 32), fb = ld_sc1(rw, WS_SF + jp * 4096 + c * 2048 + lane * 32 + 16);
          ok &= tags_ok(fa, tag) & tags_ok(fb, tag);
          fsp[c] = u32x4{fa[0], fa[2], fb[0], fb[2]};
        }
        if (all_ok(ok)) break;
      }
    }
    XBAR(3);
    LAS char* const colp = sm + FS + hw * 2048 + ((lane * 16) ^ (hw << 5));
    {
      u32x4 uin[2], fin[2];
#pragma unroll
      for (int a = 0; a < 2; ++a) { uin[a] = *(LAS u32x4*)(colp + a * 1024); fin[a] = *(LAS u32x4*)(colp + 8192 + a * 1024); }
      const bool two = special && (jp & 3) == 0;
      f32x4 dre[2], dim[2], keep;
      auto col_xform = [&](auto NA_) {
        constexpr int NA = decltype(NA_)::v;
        u32x4 ta[2];
        D1(7, 0); D1(7, 1);
#pragma unroll
        for (int a = 0; a < NA; ++a) { dre[a] = mfma32_f16(uin[a], bc1re, z4); dim[a] = mfma32_f16(uin[a], bc1im, z4); }
        D1(7, 2);
#pragma unroll
        for (int a = 0; a < NA; ++a) { ta[a] = cmulc_f16(dre[a], dim[a], t1); dre[a] = mfma32_f16(ac2re, ta[a], z4); dim[a] = mfma32_f16(ac2im, ta[a], z4); D1(7, 3 + a); }
#pragma unroll
        for (int a = 0; a < NA; ++a) {
          f32x4 fr_, fi_;
#pragma unroll
          for (int r = 0; r < 4; ++r) { fr_[r] = bf_lo(fin[a][r]); fi_[r] = bf_hi(fin[a][r]); }
          PIN(fr_); PIN(fi_);
          D1(7, 5 + 2 * a);
          ta[a] = cmul_pack<false, true>(dre[a], dim[a], fr_, fi_); dre[a] = mfma32_bf16(ta[a], bc3re, z4); dim[a] = mfma32_bf16(ta[a], bc3im, z4);
          D1(7, 6 + 2 * a);
        }
#pragma unroll
        for (int a = 0; a < NA; ++a) {
          ta[a] = cmul_pack<false, true>(dre[a], dim[a], t2c, t2s); dre[a] = mfma32_bf16(ac4re, ta[a], z4); dim[a] = mfma32_bf16(ac4im, ta[a], z4);
          D1(7, 9 + a);
        }
        D1(7, 11);
      };
      if (!G) {
        if (special) {
          fin[0] = jp > 4 ? fsp[1] : fsp[0];
          if (two) {
            const u32x4 us = uin[0];
            uin[0] = us >> 16; fin[0] = fsp[1];
            col_xform(IC<1>{});
            keep = dre[0];
            uin[0] = us & 0xffffu; fin[0] = fsp[0];
          }
        }
      }
      col_xform(IC<2>{});
      if (!G) { if (two) dim[0] = keep; }
#pragma unroll
      for (int a = 0; a < 2; ++a) *(LAS u32x4*)(colp + a * 1024) = pack4<true>(dre[a], dim[a]);
    }
    u32x2 csv[4], stv;
    const u32 csoff = WS_CS + ((t & 1) * F_ + fj * 128 + hw * 32 + (lane >> 5) * 16 + ((p16 >> 2) & 1) * 8 + (p16 >> 3) * 4 + (p16 & 3)) * 8;
    const u32 stoff = WS_ST + (((mi & 1) * 128 + hw * 32 + (lane & 31)) * 8 + (mi >> 1)) * 8;
    if (G) {
      ALD2S(csv[0], csoff, sw, 0, " sc1"); ALD2S(csv[1], csoff, sw, 2 * F_ * 8, " sc1"); ALD2S(csv[2], csoff, sw, 4 * F_ * 8, " sc1"); ALD2S(csv[3], csoff, sw, 6 * F_ * 8, " sc1");
      ALD2(stv, stoff, sw, 0, " sc1");
    }
    GWAIT(12 + 5);
    XBAR(4);
    {
      const int ps = ht >> 2;
      u32 vo[4][2];
#pragma unroll
      for (int k = 0; k < 4; ++k) { LAS char* cp = caddr((ps >> 5) + 2 * k + 8 * (ps & 31)); vo[k][0] = *(LAS u32*)cp; vo[k][1] = *(LAS u32*)(cp + 1024); }
#pragma unroll
      for (int k = 0; k < 4; ++k) st_sc1(rw, WS_V + b * 16384 + k * 4096 + ht * 16, u32x4{vo[k][0], tag, vo[k][1], tag});
    }
    wd_setup();
    if (G) {
      WAITV(4);
      *(volatile LAS u32*)(sm + MB + 48 + hw * 4) = tag;
      PIN(csv[0]); PIN(csv[1]); PIN(csv[2]); PIN(csv[3]); PIN(stv);
#define CHK3 all_ok((stv[1] == tag) & (csv[0][1] == tag) & (csv[1][1] == tag) & (csv[2][1] == tag) & (csv[3][1] == tag))
      if (__builtin_expect(!CHK3, 0)) {
#pragma nounroll
        for (int spin = 0; spin < SPIN_CAP; ++spin) {
          asm volatile("" ::: "memory");
#pragma unroll
          for (int k = 0; k < 4; ++k) csv[k] = ld_sc1_2(rw, csoff + k * 2 * F_ * 8);
          stv = ld_sc1_2(rw, stoff);
          if (CHK3) break;
        }
      }
      const u32 s0 = csv[0][0], s1 = csv[1][0], s2 = csv[2][0], s3 = csv[3][0];
      const float cs = (__builtin_bit_cast(float, s0) + __builtin_bit_cast(float, s1)) + (__builtin_bit_cast(float, s2) + __builtin_bit_cast(float, s3));
      const float ch = rnd_bf(cs);
      LAS char* cp = sm + FS + 8192 + hw * 2048 + lane * 4;
      *(LAS u32*)cp = pk_bf16(ch, cs - ch);
      *(LAS u32*)(cp + 256) = __builtin_amdgcn_perm(0u, stv[0], 0x01000100u) ^ 0x80008000u;
      asm volatile("s_waitcnt lgkmcnt(0)" ::: "memory");
      *(volatile LAS u32*)(sm + MB + 80 + hw * 4) = tag;
    }
    wd_issue(w, 0, 0); wd_issue(w, 1, 1); wd_issue(w, 2, 2);
    if (G) WAITV(12);
    u32x4 rv[2][2];
    const u32 voff = WS_V + ((b & 7) * 32 + (b >> 3)) * 64 + (p16 & 3) * 16 + ((8 * hw + (p16 >> 2)) * 8 + 2 * t) * 16384;
    if (!G) {
      WAITV(12);
#pragma nounroll
      for (int spin = 0; spin < SPIN_CAP; ++spin) {
        asm volatile("" ::: "memory");
        u32x2 q = ld_sc1_2(rw, WS_V + (b + 224) * 16384 + hw * 4096);
        if (all_ok(q[1] == tag)) break;
        __builtin_amdgcn_s_sleep(8);
      }
      __builtin_amdgcn_s_sleep(20);
    }
    hsync(G ? MB + 64 : G1_STAGE + 96);
    auto mwait = [&](int off) {
      if (G) {
#pragma nounroll
        for (int spin = 0; spin < SPIN_CAP; ++spin) { if (all_ok(*(volatile LAS u32*)(sm + off + (lane & 3) * 4) == tag)) break; __builtin_amdgcn_s_sleep(1); }
        asm volatile("" ::: "memory");
      }
    };
    WAITV(0);
    wd_read(0, 0); wd_read(1, 1); wd_read(2, 2);
    asm volatile("s_waitcnt lgkmcnt(0)" ::: "memory");
    *(volatile LAS u32*)(sm + MB + 112 + hw * 4) = tag;
    mwait(MB + 32);
    {
      ALD4S(rv[0][0], voff, sw, 0, " sc1"); ALD4S(rv[0][1], voff, sw, 16384, " sc1"); ALD4S(rv[1][0], voff, sw, 32 * 16384, " sc1"); ALD4S(rv[1][1], voff, sw, 33 * 16384, " sc1");
    }
    WAITV(0);
    PIN(rv[0][0]); PIN(rv[0][1]); PIN(rv[1][0]); PIN(rv[1][1]);
#define CHK4 all_ok(tags_ok(rv[0][0], tag) & tags_ok(rv[0][1], tag) & tags_ok(rv[1][0], tag) & tags_ok(rv[1][1], tag))
    if (__builtin_expect(!CHK4, 0)) {
#pragma nounroll
      for (int spin = 0; spin < SPIN_CAP; ++spin) {
        asm volatile("" ::: "memory");
#pragma unroll
        for (int k = 0; k < 2; ++k) { rv[k][0] = ld_sc1(rw, voff + k * 32 * 16384); rv[k][1] = ld_sc1(rw, voff + k * 32 * 16384 + 16384); }
        if (CHK4) break;
        __builtin_amdgcn_s_sleep(4);
      }
    }
    LAS char* const yb = sm + FS;
#pragma unroll
    for (int k = 0; k < 2; ++k) {
      f32x4 da = mfma16_bf16(u32x2{qa8i[0], qa8i[1]}, u32x2{rv[k][0][0], rv[k][1][0]}, z4), db = mfma16_bf16(u32x2{qa8i[0], qa8i[1]}, u32x2{rv[k][0][2], rv[k][1][2]}, z4);
      if (k == 0) { f32x4 ds = mfma16_bf16(u32x2{qa8i[2], qa8i[3]}, u32x2{rv[0][0][0], rv[0][1][0]}, z4); if ((hw | p16) == 0) da = ds; }
      *(LAS u32x2*)(yb + (2 * t) * 1024 + (32 * hw + 16 * k + p16) * 8) = u32x2{pk_bf16(da[0], da[1]), pk_bf16(db[0], db[1])};
      *(LAS u32x2*)(yb + (2 * t + 1) * 1024 + (32 * hw + 16 * k + p16) * 8) = u32x2{pk_bf16(da[2], da[3]), pk_bf16(db[2], db[3])};
    }
    hsync(MB + 0);
    u32x4 ro[2];
#pragma unroll
    for (int a = 0; a < 2; ++a) {
      u32x4 av;
#pragma unroll
      for (int m = 0; m < 4; ++m) av[m] = *(LAS u32*)(yb + (hw + 4 * a) * 1024 + (lane + 64 * m) * 4);
      const float nyq = bf_hi(*(LAS u32*)(yb + (hw + 4 * a) * 1024));
#pragma unroll
      for (int hh = 0; hh < 2; ++hh) {
        f32x4 gre = mfma32_bf16(av, bi1re[hh], z4), gim = mfma32_bf16(av, bi1im[hh], z4);
        u32x4 bb = cmul_pack<false, true>(gre, gim, t4c[hh], t4s[hh]);
        f32x4 d = mfma32_bf16(bb, bc3re, f32x4{nyq, -nyq, nyq, -nyq});
        ro[a][2 * hh] = pk_bf16(__builtin_fmaf(d[0], FSC, bf_lo(xr[a][2 * hh])), __builtin_fmaf(d[1], FSC, bf_hi(xr[a][2 * hh])));
        ro[a][2 * hh + 1] = pk_bf16(__builtin_fmaf(d[2], FSC, bf_lo(xr[a][2 * hh + 1])), __builtin_fmaf(d[3], FSC, bf_hi(xr[a][2 * hh + 1])));
      }
      PIN(ro[a]);
    }
#pragma unroll
    for (int a = 0; a < 2; ++a) st_sc1(rw, WS_R + (b + 256 * hw + 1024 * a) * 1024 + (4 * p16 + t) * 16, ro[a]);
    stat01 = lane ? stat[1] : stat[0];
  };
  if (w < 4) {
    if (is_gemm) {
      tag = load_tag();
      const int h = lane >> 5;
      int xoff[4], woff[2];
#pragma unroll
      for (int ks = 0; ks < 4; ++ks) { int tok = lane & 31; xoff[ks] = tok * 128 + (((2 * ks + h) ^ ((tok >> 1) & 7)) << 4); PIN(xoff[ks]); }
      {
        int arr = t & 1, q = p16 >> 2, p = lane & 3;
#pragma unroll
        for (int m = 0; m < 2; ++m) {
          woff[m] = 16384 + arr * 16384 + (8 * h + q) * 256 + (((hw * 2 + m) ^ (q | (arr << 2))) << 5) + (p & 1) * 16 + (p >> 1) * 8;
          PIN(woff[m]);
        }
      }
      auto rdx = [&](Frag& f, int st, int ks, int n) { f.bx[n] = *(LAS u32x4*)(sm + (st % 3) * G1_STAGE + xoff[ks] + n * 4096); };
      auto rdw = [&](Frag& f, int st, int ks, int m) {
        LAS char* buf = sm + (st % 3) * G1_STAGE;
        u32x2 lo = tr_read(buf + woff[m] + ks * 4096), hi = tr_read(buf + woff[m] + ks * 4096 + 1024);
        f.aw[m] = u32x4{lo[0], lo[1], hi[0], hi[1]};
      };
      f32x16 acc[2][4];
#pragma unroll
      for (int m = 0; m < 2; ++m)
#pragma unroll
        for (int n = 0; n < 4; ++n)
#pragma unroll
          for (int k = 0; k < 16; ++k) acc[m][n][k] = 0.f;
      Frag fr[2];
      u32 qa[2], qb[4], pv;
      auto twait = [&](int off) {
#pragma nounroll
        for (int spin = 0; spin < SPIN_CAP; ++spin) { if (all_ok(*(volatile LAS u32*)(sm + off + (lane & 3) * 4) == tag)) break; }
        asm volatile("" ::: "memory");
      };
      SB();
      g1_issue(1, 0); g1_issue(1, 1);
      WAITV(4);
      {
        LAS char* mb = sm + G1_STAGE + 32768 + 3072;
        *(volatile LAS u32*)(mb + (lane & 3) * 4096 + hw * 4) = tag;
#pragma nounroll
        for (int spin = 0; spin < SPIN_CAP; ++spin) { if (all_ok(*(volatile LAS u32*)(mb + hw * 4096 + (lane & 3) * 4) == tag)) break; }
        asm volatile("" ::: "memory");
      }
      rdw(fr[0], 0, 0, 0); rdx(fr[0], 0, 0, 0); rdx(fr[0], 0, 0, 1); rdx(fr[0], 0, 0, 2); rdx(fr[0], 0, 0, 3); rdw(fr[0], 0, 0, 1);
      static_for<0, 30>([&](auto KS_) {
        constexpr int KS = decltype(KS_)::v, st = KS >> 2, ks = KS & 3;
        constexpr int nst = ks < 3 ? st : st + 1, nks = (ks + 1) & 3;
        Frag& f = fr[KS & 1]; Frag& g = fr[(KS + 1) & 1];
        SB();
        if constexpr (ks == 3) {
          if constexpr (st == 0) WAITV(0);
          if constexpr (st < 6) BAR();
          else { if (__builtin_expect(!all_ok(pv == tag), 0)) twait(MB + 48); asm volatile("" ::: "memory"); }
          SB();
        }
        acc[0][0] = mfma3232(f.aw[0], f.bx[0], acc[0][0]); SB(); rdw(g, nst, nks, 0); SB();
        acc[0][1] = mfma3232(f.aw[0], f.bx[1], acc[0][1]); SB(); rdx(g, nst, nks, 0); SB();
        if constexpr (KS == 0) { g1_issue(1, 2); SB(); }
        if constexpr (KS == 1) { g1_issue(1, 5); SB(); }
        acc[0][2] = mfma3232(f.aw[0], f.bx[2], acc[0][2]); SB(); rdx(g, nst, nks, 1); SB();
        acc[0][3] = mfma3232(f.aw[0], f.bx[3], acc[0][3]); SB(); rdx(g, nst, nks, 2); SB();
        if constexpr (KS == 0) { g1_issue(1, 3); SB(); }
        acc[1][3] = mfma3232(f.aw[1], f.bx[3], acc[1][3]); SB(); rdx(g, nst, nks, 3); SB();
        acc[1][2] = mfma3232(f.aw[1], f.bx[2], acc[1][2]); SB(); rdw(g, nst, nks, 1); SB();
        acc[1][1] = mfma3232(f.aw[1], f.bx[1], acc[1][1]); SB();
        if constexpr (KS == 0) { g1_issue(1, 4); SB(); }
        if constexpr (KS == 26) { pv = *(volatile LAS u32*)(sm + MB + 48 + (lane & 3) * 4); SB(); }
        if constexpr (KS == 28) { pv = *(volatile LAS u32*)(sm + MB + 80 + (lane & 3) * 4); SB(); }
        if constexpr (KS == 29) {
          if (__builtin_expect(!all_ok(pv == tag), 0)) twait(MB + 80);
          asm volatile("" ::: "memory");
          LAS char* cp = sm + FS + 8192 + (lane & 31) * 4;
          qa[0] = *(LAS u32*)(cp + hw * 2048); qa[1] = *(LAS u32*)(cp + hw * 2048 + 128);
#pragma unroll
          for (int n = 0; n < 4; ++n) qb[n] = *(LAS u32*)(cp + n * 2048 + 256);
          SB();
        }
        acc[1][0] = mfma3232(f.aw[1], f.bx[0], acc[1][0]); SB();
      });
      rdw(fr[1], 7, 3, 0); rdx(fr[1], 7, 3, 0); rdw(fr[1], 7, 3, 1); rdx(fr[1], 7, 3, 1); rdx(fr[1], 7, 3, 2); rdx(fr[1], 7, 3, 3);
      __builtin_amdgcn_s_setprio(3);
      u32x4 ca[2] = {u32x4{h ? 0u : qa[0], 0u, 0u, 0u}, u32x4{h ? 0u : qa[1], 0u, 0u, 0u}}, cb = {0u, 0u, 0u, 0u};
      const u32 hoff = WS_H + ((mi * 8 + (t & 1)) * 56 + fj * 4 + hw) * 1024 + h * 256 + p16 * 16;
      float ev[8];
      auto epi_a = [&](int q) {
#pragma unroll
        for (int k = 0; k < 8; ++k) ev[k] = relu(acc[q & 1][q >> 1][k]);
      };
      auto epi_b = [&](int q) {
#pragma unroll
        for (int k = 0; k < 8; ++k) ev[k] *= acc[q & 1][q >> 1][8 + k];
      };
      auto epi_c = [&](int q) {
        __builtin_amdgcn_raw_buffer_store_b128(u32x4{pk_bf16(ev[0], ev[1]), pk_bf16(ev[2], ev[3]), pk_bf16(ev[4], ev[5]), pk_bf16(ev[6], ev[7])}, rw,
                                               hoff + (q & 1) * 512, (q >> 1) * (2 * 56 * 1024), 16);
      };
#pragma unroll
      for (int q = 0; q < 8; ++q) {
        const int m = q & 1, n = q >> 1;
        SB();
        if (m == 0) cb[0] = qb[n];
        acc[m][n] = mfma3232(fr[0].aw[m], fr[0].bx[n], acc[m][n]); SB();
        if (q >= 2) { epi_c(q - 2); SB(); }
        acc[m][n] = mfma3232(ca[m], cb, acc[m][n]); SB();
        if (q >= 1) { epi_a(q - 1); SB(); }
        acc[m][n] = mfma3232(fr[1].aw[m], fr[1].bx[n], acc[m][n]); SB();
        if (q >= 1) { epi_b(q - 1); SB(); }
      }
      epi_c(6); SB(); epi_a(7); epi_b(7); epi_c(7);
      *(volatile LAS u32*)(sm + MB + 32 + hw * 4) = tag;
      wd_setup();
      WAITV(4);
      if (lane == 0) __builtin_amdgcn_raw_buffer_store_b32(tag, rw, WS_F2 + (mi * 2) * 2048 + fj * 128 + hw * 4, 0, 16);
      WAITV(1);
      if (lane == 0) __builtin_amdgcn_raw_buffer_store_b32(tag, rw, WS_F2 + (mi * 2 + 1) * 2048 + fj * 128 + hw * 4, 0, 16);
      __builtin_amdgcn_s_setprio(2);
    } else {
      wd_setup();
      const int un = b >> 1, ua = un >= 7, uc = (b & 1) * 7 + (ua ? un - 7 : un);
      const u32x4 scs = {ua ? ka[8] : ka[6], ua ? ka[9] : ka[7], b < 28 ? 1835008u : 0u, 0x00020000u};
      const u32 vcs = (hw * 4 + t) * 3584 + uc * 256 + p16 * 16;
      u32x4 vv[16];
      f32x4 a0 = z4, a1 = z4;
      u32 sfa, sfb;
      u32 sel_lo = 0x00003f80u, sel_hi = 0x3f800000u;
      PIN(sel_lo); PIN(sel_hi);
      const unsigned long long t_start = __builtin_amdgcn_s_memrealtime();
#pragma unroll
      for (int half = 0; half < 2; ++half) {
        if (half == 1) { while (__builtin_amdgcn_s_memrealtime() - t_start < CS_DELAY) __builtin_amdgcn_s_sleep(4); }
#pragma unroll
        for (int k = 0; k < 16; ++k) ALD4S(vv[k], vcs, scs, (half * 16 + k) * 16 * 3584, "");
        if (half == 0) {
          const int q = 64 * g + 16 * hw + p16, ks = 8 * q + jp, km = (2048 - ks) & 2047, col = (t >> 1) * 256;
          const GAS unsigned short* wa = (t & 1) ? wi : wr;
          asm volatile("global_load_ushort %0, %1, off" : "=v"(sfa) : "v"(wa + ks * DF_ + col) : "memory");
          asm volatile("global_load_ushort %0, %1, off" : "=v"(sfb) : "v"(wa + km * DF_ + col) : "memory");
          tag = load_tag();
        }
        static_for<0, 4>([&](auto Q_) {
          constexpr int Q = decltype(Q_)::v;
          if (half == 0) WAITV(14 - 4 * Q); else WAITV(12 - 4 * Q);
#pragma unroll
          for (int k = 4 * Q; k < 4 * Q + 4; ++k) {
            PIN(vv[k]);
#pragma unroll
            for (int e = 0; e < 2; ++e) {
              const u32 p = vv[k][e], q = vv[k][2 + e];
              a0[2 * e] = __builtin_amdgcn_fdot2_f32_bf16(__builtin_bit_cast(b16x2, p), __builtin_bit_cast(b16x2, sel_lo), a0[2 * e], false);
              a0[2 * e + 1] = __builtin_amdgcn_fdot2_f32_bf16(__builtin_bit_cast(b16x2, p), __builtin_bit_cast(b16x2, sel_hi), a0[2 * e + 1], false);
              a1[2 * e] = __builtin_amdgcn_fdot2_f32_bf16(__builtin_bit_cast(b16x2, q), __builtin_bit_cast(b16x2, sel_lo), a1[2 * e], false);
              a1[2 * e + 1] = __builtin_amdgcn_fdot2_f32_bf16(__builtin_bit_cast(b16x2, q), __builtin_bit_cast(b16x2, sel_hi), a1[2 * e + 1], false);
            }
          }
        });
        PIN(a0); PIN(a1);
        if (half == 0) {
          WAITV(0);
          PIN(sfa); PIN(sfb);
          const float wh = 0.5f * ((t & 1) ? bf_lo(sfa) - bf_lo(sfb) : bf_lo(sfa) + bf_lo(sfb));
          const u32 wb = pk_bf16(wh, wh) & 0xffffu;
          auto s2 = __builtin_amdgcn_permlane16_swap(wb, wb, false, false);
          if ((t & 1) == 0) st_sc1_2(rw, WS_SF + jp * 4096 + (t >> 1) * 2048 + (64 * g + 4 * p16 + hw) * 8, u32x2{(u32)s2[0] | ((u32)s2[1] << 16), tag});
        }
      }
      if (b < 28) {
#pragma unroll
        for (int k = 0; k < 4; ++k) { a0[k] = xrow_sum(a0[k]); a1[k] = xrow_sum(a1[k]); }
        const float s0 = t == 0 ? a0[0] : t == 1 ? a0[2] : t == 2 ? a1[0] : a1[2];
        const float s1 = t == 0 ? a0[1] : t == 1 ? a0[3] : t == 2 ? a1[1] : a1[3];
        st_sc1(rw, WS_CS + hw * (2 * F_ * 8) + (ua * 14 + uc) * 1024 + p16 * 64 + t * 16, u32x4{__builtin_bit_cast(u32, s0), tag, __builtin_bit_cast(u32, s1), tag});
      }
    }
    {
      const int off = is_gemm ? MB + 32 : MB + 112;
#pragma nounroll
      for (int spin = 0; spin < SPIN_CAP; ++spin) { if (all_ok(*(volatile LAS u32*)(sm + off + (lane & 3) * 4) == tag)) break; __builtin_amdgcn_s_sleep(1); }
      asm volatile("" ::: "memory");
    }
    __builtin_amdgcn_s_setprio(1);
    wd_issue(w, 0, 6); wd_issue(w, 1, 7); wd_issue(w, 2, 8);
    wd_issue(w + 4, 3, 3); wd_issue(w + 4, 4, 4); wd_issue(w + 4, 5, 5);
    {
#pragma nounroll
      for (int spin = 0; spin < SPIN_CAP; ++spin) { if (all_ok(*(volatile LAS u32*)(sm + MB + 112 + hw * 4) == tag)) break; __builtin_amdgcn_s_sleep(1); }
      asm volatile("" ::: "memory");
    }
    wd_issue(w, 5, 2); wd_issue(w, 6, 1); wd_issue(w + 4, 6, 0);
    WAITV(24);
    wd_read(0, 6); wd_read(1, 7); wd_read(2, 8);
    asm volatile("s_waitcnt lgkmcnt(0)" ::: "memory");
    wd_issue(w, 3, 6); wd_issue(w, 4, 7);
  } else if (is_gemm) helper(IC<1>{});
  else helper(IC<0>{});
  wait_h_and_load();
  __builtin_amdgcn_s_setprio(0);
  WAITV(7);
  if (w >= 4 && lane < 2) st_sc1_2(rw, WS_ST2 + b * 64 + hw * 8 + lane * 32, u32x2{stat01, tag});
#pragma unroll
  for (int i = 3; i < 7; ++i) {
    LAS char* p0 = sm + (w < 4 ? wslot(i == 6 ? 1 : i == 5 ? 2 : i + 3) : wslot(i == 6 ? 0 : i));
#pragma unroll
    for (int u = 0; u < 4; ++u) {
      LAS char* p = p0 + (wbase ^ (u << 5));
      u32x2 lo = tr_read(p), hi = tr_read(p + 512);
      wf[i][u] = u32x4{lo[0], lo[1], hi[0], hi[1]};
    }
  }
  const int oc = lane & 7, otok = (w & 1) * 8 + (lane >> 3);
  const int orow = rt * 64 + (((w >> 1) + ct) & 3) * 16 + otok;
  const u32 roff = WS_R + orow * 1024 + (ct * 8 + oc) * 16;
  const u32 soff_ = WS_ST2 + ((orow & 255) * 8 + (orow >> 8)) * 8;
  u32x4 r0; u32x2 sto; bool rok;
#pragma unroll
  for (int tt = 0; tt < 4; ++tt) {
    f32x4 acc[4] = {z4, z4, z4, z4};
#pragma unroll
    for (int ks = 0; ks < 7; ++ks) {
#pragma unroll
      for (int u = 0; u < 4; ++u) acc[u] = mfma32_bf16(wf[ks][u], hf[tt][ks], acc[u]);
      if (tt < 3) { SB(); hf[(tt + 1) & 3][ks] = ld_h(tt + 1, ks); SB(); }
    }
    if (tt == 2) sto = ld_sc1_2(rw, soff_);
    if (tt == 3) { SB(); asm volatile("" : "+v"(sto) :: "memory"); rok = all_ok(sto[1] == tag); r0 = ld_sc1(rw, roff); }
#pragma unroll
    for (int u = 0; u < 4; ++u) *(LAS f32x4*)(sm + (w < 4 ? wslot(tt < 3 ? 6 + tt : 1) : wslot(tt < 3 ? 3 + tt : 0)) + hw * 4096 + u * 1024 + t * 256 + ((p16 ^ (4 * u + (t & 2))) << 4)) = acc[u];
    SB();
  }
  BAR();
  f32x4 sa = z4, sb = z4;
  {
    const int tt = w >> 1;
    const int o0 = (oc >> 1) * 1024 + (oc & 1) * 512 + ((otok ^ (2 * oc)) << 4);
    const int bm = (tt < 3 ? wslot(6) + tt * 16384 : wslot(1)) + o0, bh = (tt < 3 ? wslot(3) + tt * 16384 : wslot(0)) + o0;
#pragma unroll
    for (int pw = 0; pw < 4; ++pw) {
      sa += *(LAS f32x4*)(sm + bm + pw * 4096) + *(LAS f32x4*)(sm + bh + pw * 4096);
      sb += *(LAS f32x4*)(sm + bm + 256 + pw * 4096) + *(LAS f32x4*)(sm + bh + 256 + pw * 4096);
    }
  }
  if (__builtin_expect(!rok, 0)) {
#pragma nounroll
    for (int spin = 0; spin < SPIN_CAP; ++spin) {
      asm volatile("" ::: "memory");
      sto = ld_sc1_2(rw, soff_);
      if (all_ok(sto[1] == tag)) break;
    }
    asm volatile("" ::: "memory");
    r0 = ld_sc1(rw, roff);
  }
  {
    const float rs = __builtin_amdgcn_rcpf(bf_hi(sto[0])), rs2 = rs * rs;
    *(GAS u32x4*)(out + (size_t)orow * 1024 + ct * 128 + oc * 16) =
        u32x4{pk_bf16(__builtin_fmaf(sa[0], rs2, bf_lo(r0[0])), __builtin_fmaf(sa[1], rs2, bf_hi(r0[0]))),
              pk_bf16(__builtin_fmaf(sa[2], rs2, bf_lo(r0[1])), __builtin_fmaf(sa[3], rs2, bf_hi(r0[1]))),
              pk_bf16(__builtin_fmaf(sb[0], rs2, bf_lo(r0[2])), __builtin_fmaf(sb[1], rs2, bf_hi(r0[2]))),
              pk_bf16(__builtin_fmaf(sb[2], rs2, bf_lo(r0[3])), __builtin_fmaf(sb[3], rs2, bf_hi(r0[3])))};
  }
  if (b == 0 && tid == 0) __builtin_amdgcn_raw_buffer_store_b32(tag, rw, WS_EPOCH, 0, 16);
}
#if defined(__HIP_DEVICE_COMPILE__)
#pragma clang attribute pop
#endif

extern "C" void kernel_launch(void* const* d_in, const int* in_sizes, int n_in, void* d_out, int out_size, void* d_ws, size_t ws_size,
                              hipStream_t stream) {
  (void)hipFuncSetAttribute((const void*)parallel_sum__spectral_gfnet__glu_gated_reglu__prompt_kernel, hipFuncAttributeMaxDynamicSharedMemorySize, LDS_BYTES);
  parallel_sum__spectral_gfnet__glu_gated_reglu__prompt_kernel<<<256, 512, LDS_BYTES, stream>>>((const char*)d_in[0], (const unsigned short*)d_in[1], (const unsigned short*)d_in[2], (const char*)d_in[4],
                                                (const char*)d_in[5], (const char*)d_in[6], (char*)d_ws, (char*)d_out);
}
```
